```python
import torch

def _softmax(x, axis=-1):
    return torch.softmax(x, dim=axis)
def _gelu(x):
    return torch.nn.functional.gelu(x, approximate="tanh")
def _silu(x):
    return torch.nn.functional.silu(x)
def _sigmoid(x):
    return torch.sigmoid(x)
def _layernorm(x, axis=-1, eps=1e-5):
    m = x.mean(dim=axis, keepdim=True)
    v = x.var(dim=axis, keepdim=True, unbiased=False)
    return (x - m) / torch.sqrt(v + eps)
def _scatter_add(tgt, idx, src, axis=0):
    return tgt.clone().index_add_(axis, idx, src)
def _segment_sum(src, seg, n):
    out = torch.zeros((n,) + src.shape[1:], dtype=src.dtype)
    return out.index_add_(0, seg, src)
def _pad(x, pw):
    flat = tuple(v for pair in reversed(pw) for v in pair)
    return torch.nn.functional.pad(x, flat)

kernel_name = 'parallel_sum__ssm_conv_s4__dense_activation_gelu__prompt'

def setup_inputs():

    _w = (lambda a: (lambda t: t.to(torch.bfloat16) if t.is_floating_point() else t)(torch.as_tensor(a)))
    def _A_setup():
        B, L, D, N = 1, 2048, 640, 128
        A_re = -(lambda _x: torch.exp(torch.as_tensor(_x)))((lambda *s: torch.randn(*s, dtype=torch.bfloat16) * 6.4)(D, N))
        A_im = torch.pi * (lambda *s: torch.randn(*s, dtype=torch.bfloat16) * 0.05)(D, N)
        B_re = (lambda *s: torch.randn(*s, dtype=torch.bfloat16) * 0.1)(D, N)
        B_im = (lambda *s: torch.randn(*s, dtype=torch.bfloat16) * 0.1)(D, N)
        C_re = (lambda *s: torch.randn(*s, dtype=torch.bfloat16) * 0.1)(D, N)
        C_im = (lambda *s: torch.randn(*s, dtype=torch.bfloat16) * 0.1)(D, N)
        log_dt = (lambda *s: torch.randn(*s, dtype=torch.bfloat16) * 0.05)(D) * 0.5 - 4.0
        Dskip = (lambda *s: torch.randn(*s, dtype=torch.bfloat16) * 0.05)(D)
        return {
            "u": _w((lambda *s: torch.randn(*s, dtype=torch.bfloat16) * 0.05)(B, L, D)),
            "A_re": _w(A_re),
            "A_im": _w(A_im),
            "B_re": _w(B_re),
            "B_im": _w(B_im),
            "C_re": _w(C_re),
            "C_im": _w(C_im),
            "log_dt": _w(log_dt),
            "Dskip": _w(Dskip),
        }
    _A_in = _A_setup()

    def _B_setup():
        B, S, D, F = 1, 2048, 640, 1728
        return {
            "x": _w((lambda *s: torch.randn(*s, dtype=torch.bfloat16) * 0.05)(B, S, D)),
            "w1": _w((lambda *s: torch.randn(*s, dtype=torch.bfloat16) * 0.8)(D, F) * (D ** -0.5)),
            "b1": _w((lambda *s: torch.randn(*s, dtype=torch.bfloat16) * 0.2)(F) * 0.01),
            "w2": _w((lambda *s: torch.randn(*s, dtype=torch.bfloat16) * 0.2)(F, D) * (F ** -0.5)),
            "b2": _w((lambda *s: torch.randn(*s, dtype=torch.bfloat16) * 0.2)(D) * 0.01),
        }
    _B_in = _B_setup()

    return {
        "A_u": _A_in["u"],
        "A_A_re": _A_in["A_re"],
        "A_A_im": _A_in["A_im"],
        "A_B_re": _A_in["B_re"],
        "A_B_im": _A_in["B_im"],
        "A_C_re": _A_in["C_re"],
        "A_C_im": _A_in["C_im"],
        "A_log_dt": _A_in["log_dt"],
        "A_Dskip": _A_in["Dskip"],
        "B_x": _B_in["x"],
        "B_w1": _B_in["w1"],
        "B_b1": _B_in["b1"],
        "B_w2": _B_in["w2"],
        "B_b2": _B_in["b2"],
    }

def reference(A_u, A_A_re, A_A_im, A_B_re, A_B_im, A_C_re, A_C_im, A_log_dt, A_Dskip, B_x, B_w1, B_b1, B_w2, B_b2):
    def _A_ref(u, A_re, A_im, B_re, B_im, C_re, C_im, log_dt, Dskip):
        Bsz, L, D = u.shape
        dt = torch.exp(log_dt)[:, None]
        A = A_re + 1j * A_im
        Bc = B_re + 1j * B_im
        Cc = C_re + 1j * C_im
        logdA = dt * A
        dB = dt * Bc
        l = (lambda x, dt: x.to(dt))(torch.arange(L), A_re.dtype)
        P = torch.exp(logdA[:, :, None] * l[None, None, :])
        K = torch.einsum("dn,dnl,dn->dl", Cc, P, dB).real
        L2 = 2 * L
        Kp = _pad(K, ((0, 0), (0, L)))
        up = (lambda x, *d: x.permute(*d))(u, 0, 2, 1)
        up = _pad(up, ((0, 0), (0, 0), (0, L)))
        Kf = (lambda x, axes=(-2,-1): torch.fft.rfft2(x.float(), dim=axes))(Kp[:, None, :], axes=(-2, -1))
        Uf = (lambda x, axes=(-2,-1): torch.fft.rfft2(x.float(), dim=axes))(up[:, :, None, :], axes=(-2, -1))
        Yf = Uf * Kf[None, :, :, :]
        y = (lambda x, s, axes=(-2,-1): torch.fft.irfft2(x, s=s, dim=axes).to(torch.bfloat16))(Yf, (1, L2), axes=(-2, -1))[:, :, 0, :L]
        y = (lambda x, *d: x.permute(*d))(y, 0, 2, 1) + u * Dskip[None, None, :]
        return y

    def _B_ref(x, w1, b1, w2, b2):
        h = torch.einsum("bsd,df->bsf", x, w1) + b1
        h = _gelu(h)
        return torch.einsum("bsf,fd->bsd", h, w2) + b2

    x = A_u
    xn = _layernorm(x, axis=-1)
    return x + _A_ref(xn, A_A_re, A_A_im, A_B_re, A_B_im, A_C_re, A_C_im, A_log_dt, A_Dskip) + _B_ref(xn, B_w1, B_b1, B_w2, B_b2)
```

```cpp
#include <hip/hip_runtime.h>
#include <hip/hip_bf16.h>
#include <stdint.h>

#define L_ 2048
#define D_ 640
#define N_ 128
#define F_ 1728

typedef unsigned short u16;
__device__ __forceinline__ float bf2f(u16 v){ return __uint_as_float(((unsigned)v)<<16); }
__device__ __forceinline__ u16 f2bf(float f){ unsigned u=__float_as_uint(f); u += 0x7fffu + ((u>>16)&1u); return (u16)(u>>16); }
__device__ __forceinline__ float rbf(float f){ return bf2f(f2bf(f)); }

__global__ void k_ln(const u16* x, u16* xn){
  int row = blockIdx.x; int t = threadIdx.x;
  float v[10]; float s=0;
  for(int i=0;i<10;i++){ v[i]=bf2f(x[row*D_+t+64*i]); s+=v[i]; }
  for(int o=32;o;o>>=1) s+=__shfl_xor(s,o);
  float mean=s/D_;
  float q=0; for(int i=0;i<10;i++){ float d=v[i]-mean; q+=d*d; }
  for(int o=32;o;o>>=1) q+=__shfl_xor(q,o);
  float var=rbf(q/D_); float m=rbf(mean);
  float sd=rbf(sqrtf(rbf(var+1e-5f)));
  for(int i=0;i<10;i++){ xn[row*D_+t+64*i]=f2bf(rbf(v[i]-m)/sd); }
}
__global__ void k_kgen(const u16* Are,const u16* Aim,const u16* Bre,const u16* Bim,const u16* Cre,const u16* Cim,const u16* logdt,float* K){
  int d=blockIdx.y; int l=blockIdx.x*256+threadIdx.x;
  float dt=rbf(expf(bf2f(logdt[d])));
  float lf=rbf((float)l);
  float acc=0;
  for(int n=0;n<N_;n++){
    float ar=dt*bf2f(Are[d*N_+n]), ai=dt*bf2f(Aim[d*N_+n]);
    float br=dt*bf2f(Bre[d*N_+n]), bi=dt*bf2f(Bim[d*N_+n]);
    float cr=bf2f(Cre[d*N_+n]), ci=bf2f(Cim[d*N_+n]);
    float wr=cr*br-ci*bi, wi=cr*bi+ci*br;
    float e=expf(ar*lf); float sn,cs; sincosf(ai*lf,&sn,&cs);
    acc += e*(wr*cs-wi*sn);
  }
  K[d*L_+l]=acc;
}
__global__ void k_conv(const u16* xn,const float* K,float* y){
  int d=blockIdx.x*64+threadIdx.x; int l=blockIdx.y;
  float acc=0;
  for(int j=0;j<=l;j++) acc+=K[d*L_+j]*bf2f(xn[(l-j)*D_+d]);
  y[l*D_+d]=acc;
}
__global__ void k_g1(const u16* xn,const u16* w1,const u16* b1,u16* h){
  int f=blockIdx.x*64+threadIdx.x; int r=blockIdx.y;
  float acc=0;
  for(int k=0;k<D_;k++) acc+=bf2f(xn[r*D_+k])*bf2f(w1[k*F_+f]);
  float v=rbf(rbf(acc)+bf2f(b1[f]));
  float g=0.5f*v*(1.f+tanhf(0.7978845608028654f*(v+0.044715f*v*v*v)));
  h[r*F_+f]=f2bf(g);
}
__global__ void k_g2(const u16* x,const u16* xn,const u16* h,const u16* w2,const u16* b2,const float* y,const u16* Dsk,u16* out){
  int c=blockIdx.x*64+threadIdx.x; int r=blockIdx.y;
  float acc=0;
  for(int k=0;k<F_;k++) acc+=bf2f(h[r*F_+k])*bf2f(w2[k*D_+c]);
  float ffn=rbf(rbf(acc)+bf2f(b2[c]));
  float a=rbf(rbf(y[r*D_+c])+rbf(bf2f(xn[r*D_+c])*bf2f(Dsk[c])));
  out[r*D_+c]=f2bf(rbf(bf2f(x[r*D_+c])+a)+ffn);
}

extern "C" void kernel_launch(void* const* d_in, const int* in_sizes, int n_in, void* d_out, int out_size, void* d_ws, size_t ws_size, hipStream_t stream) {
  const u16* x=(const u16*)d_in[0];
  char* ws=(char*)d_ws;
  u16* xn=(u16*)ws; float* K=(float*)(ws+(4<<20)); float* y=(float*)(ws+(12<<20)); u16* h=(u16*)(ws+(20<<20));
  k_ln<<<L_,64,0,stream>>>(x,xn);
  k_kgen<<<dim3(L_/256,D_),256,0,stream>>>((const u16*)d_in[1],(const u16*)d_in[2],(const u16*)d_in[3],(const u16*)d_in[4],(const u16*)d_in[5],(const u16*)d_in[6],(const u16*)d_in[7],K);
  k_conv<<<dim3(D_/64,L_),64,0,stream>>>(xn,K,y);
  k_g1<<<dim3(F_/64,L_),64,0,stream>>>(xn,(const u16*)d_in[10],(const u16*)d_in[11],h);
  k_g2<<<dim3(D_/64,L_),64,0,stream>>>(x,xn,h,(const u16*)d_in[12],(const u16*)d_in[13],y,(const u16*)d_in[8],(u16*)d_out);
}
```

```cpp
#include <hip/hip_runtime.h>
#include <hip/hip_bf16.h>
#include <stdint.h>

#define L_ 2048
#define D_ 640
#define N_ 128
#define F_ 1728

typedef unsigned short u16;
__device__ __forceinline__ float bf2f(u16 v){ return __uint_as_float(((unsigned)v)<<16); }
__device__ __forceinline__ u16 f2bf(float f){ unsigned u=__float_as_uint(f); u += 0x7fffu + ((u>>16)&1u); return (u16)(u>>16); }
__device__ __forceinline__ float rbf(float f){ return bf2f(f2bf(f)); }

__global__ void k_ln(const u16* x, u16* xn){
  int row = blockIdx.x; int t = threadIdx.x;
  float v[10]; float s=0;
  for(int i=0;i<10;i++){ v[i]=bf2f(x[row*D_+t+64*i]); s+=v[i]; }
  for(int o=32;o;o>>=1) s+=__shfl_xor(s,o);
  float mean=s/D_;
  float q=0; for(int i=0;i<10;i++){ float d=v[i]-mean; q+=d*d; }
  for(int o=32;o;o>>=1) q+=__shfl_xor(q,o);
  float var=rbf(q/D_); float m=rbf(mean);
  float sd=rbf(sqrtf(rbf(var+1e-5f)));
  for(int i=0;i<10;i++){ xn[row*D_+t+64*i]=f2bf(rbf(v[i]-m)/sd); }
}
__global__ void k_kgen(const u16* Are,const u16* Aim,const u16* Bre,const u16* Bim,const u16* Cre,const u16* Cim,const u16* logdt,float* K){
  int d=blockIdx.y; int l=blockIdx.x*256+threadIdx.x;
  float dt=rbf(expf(bf2f(logdt[d])));
  float lf=rbf((float)l);
  float acc=0;
  for(int n=0;n<N_;n++){
    float ar=dt*bf2f(Are[d*N_+n]), ai=dt*bf2f(Aim[d*N_+n]);
    float br=dt*bf2f(Bre[d*N_+n]), bi=dt*bf2f(Bim[d*N_+n]);
    float cr=bf2f(Cre[d*N_+n]), ci=bf2f(Cim[d*N_+n]);
    float wr=cr*br-ci*bi, wi=cr*bi+ci*br;
    float e=expf(ar*lf); float sn,cs; sincosf(ai*lf,&sn,&cs);
    acc += e*(wr*cs-wi*sn);
  }
  K[d*L_+l]=acc;
}
__global__ void k_conv(const u16* xn,const float* K,float* y){
  int d=blockIdx.x*64+threadIdx.x; int l=blockIdx.y;
  float acc=0;
  for(int j=0;j<=l;j++) acc+=K[d*L_+j]*bf2f(xn[(l-j)*D_+d]);
  y[l*D_+d]=acc;
}
__global__ void k_g1(const u16* xn,const u16* w1,const u16* b1,u16* h){
  int f=blockIdx.x*64+threadIdx.x; int r=blockIdx.y;
  float acc=0;
  for(int k=0;k<D_;k++) acc+=bf2f(xn[r*D_+k])*bf2f(w1[k*F_+f]);
  float v=rbf(rbf(acc)+bf2f(b1[f]));
  float g=0.5f*v*(1.f+tanhf(0.7978845608028654f*(v+0.044715f*v*v*v)));
  h[r*F_+f]=f2bf(g);
}
__global__ void k_g2(const u16* x,const u16* xn,const u16* h,const u16* w2,const u16* b2,const float* y,const u16* Dsk,u16* out){
  int c=blockIdx.x*64+threadIdx.x; int r=blockIdx.y;
  float acc=0;
  for(int k=0;k<F_;k++) acc+=bf2f(h[r*F_+k])*bf2f(w2[k*D_+c]);
  float ffn=rbf(rbf(acc)+bf2f(b2[c]));
  float a=rbf(rbf(y[r*D_+c])+rbf(bf2f(xn[r*D_+c])*bf2f(Dsk[c])));
  out[r*D_+c]=f2bf(rbf(bf2f(x[r*D_+c])+a)+ffn);
}


typedef unsigned long long u64;
template<int ROT,int WHICH>
__global__ __launch_bounds__(512) void k_probe(const char* X,const char* W,u64* stamps){
  extern __shared__ __attribute__((aligned(16))) char smem[];
  int bid=blockIdx.x; int xcd=bid&7; int r=bid>>3;
  int tid=threadIdx.x; int wave=__builtin_amdgcn_readfirstlane(tid>>6), lane=tid&63;
  u64 t0=__builtin_amdgcn_s_memrealtime();
  if(WHICH==0){
    int rb=(xcd>>1)*4+(r&3); int cb=(xcd&1)*7+(r>>2);
    if(cb==13) cb=12;
    for(int s0=0;s0<10;s0++){
      int s=(s0+(ROT?xcd:0))%10;
      for(int u=0;u<2;u++){
        int row=(wave*2+u)*8+(lane>>3);
        __builtin_amdgcn_global_load_lds((const __attribute__((address_space(1))) void*)(X+(size_t)(rb*128+row)*1280+s*128+(lane&7)*16),
          (__attribute__((address_space(3))) void*)(smem+((s0&3)*32768+(wave*2+u)*1024)),16,0,0);
        int k=s*64+(wave*2+u)*4+(lane>>4);
        __builtin_amdgcn_global_load_lds((const __attribute__((address_space(1))) void*)(W+(size_t)k*3456+cb*256+(lane&15)*16),
          (__attribute__((address_space(3))) void*)(smem+((s0&3)*32768+16384+(wave*2+u)*1024)),16,0,0);
      }
      asm volatile("s_waitcnt vmcnt(12)":::"memory");
    }
    asm volatile("s_waitcnt vmcnt(0)":::"memory");
  } else {
    __builtin_amdgcn_global_load_lds((const __attribute__((address_space(1))) void*)(W+(size_t)(bid*8+wave)*1024+lane*16),
          (__attribute__((address_space(3))) void*)(smem+(wave*1024)),16,0,0);
    asm volatile("s_waitcnt vmcnt(0)":::"memory");
  }
  __syncthreads();
  u64 t1=__builtin_amdgcn_s_memrealtime();
  if(tid==0){ stamps[bid*2]=t0; stamps[bid*2+1]=t1; }
}
__global__ void k_spin(const u64* stamps,int n,int mode){
  u64 mn=~0ull,mx=0,sum=0; for(int i=0;i<n;i++){ u64 a=stamps[2*i],b=stamps[2*i+1]; mn=a<mn?a:mn; mx=b>mx?b:mx; sum+=b-a; }
  u64 d = mode==0 ? (mx-mn)*1000ull : sum*1000ull/n;
  if(d>3000000ull) d=3000000ull;
  u64 t0=__builtin_amdgcn_s_memrealtime();
  while(__builtin_amdgcn_s_memrealtime()-t0<d){ __builtin_amdgcn_s_sleep(8); }
}

extern "C" void kernel_launch(void* const* d_in, const int* in_sizes, int n_in, void* d_out, int out_size, void* d_ws, size_t ws_size, hipStream_t stream) {
  const u16* x=(const u16*)d_in[0];
  char* ws=(char*)d_ws;
  u16* xn=(u16*)ws; float* K=(float*)(ws+(4<<20)); float* y=(float*)(ws+(12<<20)); u16* h=(u16*)(ws+(20<<20));
  hipFuncSetAttribute((const void*)k_probe<0,1>,hipFuncAttributeMaxDynamicSharedMemorySize,131072);
  k_probe<0,1><<<224,512,131072,stream>>>((const char*)d_in[0],(const char*)d_in[10],(u64*)(ws+(40<<20)));
  k_spin<<<1,1,0,stream>>>((const u64*)(ws+(40<<20)),224,1);
  k_ln<<<L_,64,0,stream>>>(x,xn);
  k_kgen<<<dim3(L_/256,D_),256,0,stream>>>((const u16*)d_in[1],(const u16*)d_in[2],(const u16*)d_in[3],(const u16*)d_in[4],(const u16*)d_in[5],(const u16*)d_in[6],(const u16*)d_in[7],K);
  k_conv<<<dim3(D_/64,L_),64,0,stream>>>(xn,K,y);
  k_g1<<<dim3(F_/64,L_),64,0,stream>>>(xn,(const u16*)d_in[10],(const u16*)d_in[11],h);
  k_g2<<<dim3(D_/64,L_),64,0,stream>>>(x,xn,h,(const u16*)d_in[12],(const u16*)d_in[13],y,(const u16*)d_in[8],(u16*)d_out);
}
```

```cpp
#include <hip/hip_runtime.h>
#include <stdint.h>
#define PUB_RHO 3
#define CSUM_ON 1
#define ARR 0

typedef unsigned short u16;
typedef unsigned long long u64;
typedef short s4v __attribute__((ext_vector_type(4)));
typedef __bf16 bf8v __attribute__((ext_vector_type(8)));
typedef __bf16 bf2v __attribute__((ext_vector_type(2)));
typedef float f2v __attribute__((ext_vector_type(2)));
typedef float f4v __attribute__((ext_vector_type(4)));
typedef float f16v __attribute__((ext_vector_type(16)));
typedef unsigned u2v __attribute__((ext_vector_type(2)));
typedef unsigned short h2v __attribute__((ext_vector_type(2)));
typedef unsigned u4v __attribute__((ext_vector_type(4)));
typedef __attribute__((address_space(1))) u64 gu64;
#define LDSP(T) __attribute__((address_space(3))) T*
#define DI __device__ __forceinline__
#define RLX_AGENT __ATOMIC_RELAXED, __HIP_MEMORY_SCOPE_AGENT

constexpr unsigned WS_E = 0;
constexpr unsigned WS_FH = 4096;
constexpr unsigned WS_FA = 32768;
constexpr unsigned WS_FW = 36864;
constexpr unsigned WS_FS = 40960;
constexpr unsigned WS_FC = 45056;
constexpr unsigned WS_FY = 49152;
constexpr unsigned WS_STATS = 131072;
constexpr unsigned WS_CS = 196608, WS_B1 = 204800;
constexpr unsigned WS_STAMP = 262144;
constexpr unsigned WS_XT = 1u << 20;
constexpr unsigned WS_W2T = 4u << 20;
constexpr unsigned WS_YT = 8u << 20;
constexpr unsigned WS_HA = 12u << 20;
constexpr unsigned WS_BYTES = 20u << 20;
constexpr unsigned SLOT = 32768;
constexpr unsigned L_KC = 98304, KC_CH = 16640, KC_CP = 4160;
constexpr unsigned L_UA = 148224, UA_CH = 4576, UA_PAD = 480;
constexpr unsigned L_MISC = 161952, L_CB = L_MISC, L_CNT = L_MISC + 1024, L_DUM = L_MISC + 1088;
constexpr unsigned L_TBL = 65536, TBL_ROW = 544, TBL_CH = 53 * TBL_ROW;
constexpr unsigned L_PRM = 152064, L_TT = 158064, TT_CH = 1296;
constexpr unsigned L_SCR = L_CB;
constexpr unsigned L_RED = 0, RED_ROW = 256, RED_T = 576, RED_W = 20 * RED_T, L_YS = 92160, YS_ROW = 144, L_XS = 119296;
constexpr unsigned L_AX = 0, L_AW = 81920, L_AST = 81920 + 71680, L_ACNT = L_AST + 512, L_ACS = L_AST + 1024;
constexpr unsigned LDS_BYTES = 163840;
constexpr int SPIN_LIMIT = 400000;

#define STAMP(i)

DI float bf2f(unsigned v) { return __uint_as_float(v << 16); }
DI float bflo(unsigned v) { return __uint_as_float(v << 16); }
DI float bfhi(unsigned v) { return __uint_as_float(v & 0xffff0000u); }
DI unsigned pk2(float a, float b) { bf2v v; v[0] = (__bf16)a; v[1] = (__bf16)b; return __builtin_bit_cast(unsigned, v); }
DI float rbf(float a) { return bflo(pk2(a, a)); }
template <typename P> DI u4v mkrsrc(P p, unsigned bytes) { u64 a = (u64)p; u4v r; r[0] = (unsigned)a; r[1] = (unsigned)(a >> 32); r[2] = bytes; r[3] = 0x00020000u; return r; }
DI void dma16(u4v rs, unsigned voff, unsigned soff, unsigned lds) {
    asm volatile("s_mov_b32 m0, %3\n\ts_nop 0\n\tbuffer_load_dwordx4 %0, %1, %2 offen lds" ::"v"(voff), "s"(rs), "s"(soff), "s"(lds) : "memory");
}
DI void dma16g(const void* p, unsigned lds) {
    asm volatile("s_mov_b32 m0, %1\n\ts_nop 0\n\tglobal_load_lds_dwordx4 %0, off" ::"v"(p), "s"(lds) : "memory");
}
DI void dma16g_lanes3(const void* p, unsigned lds) {
    u64 ex;
    asm volatile("s_mov_b64 %0, exec\n\ts_mov_b64 exec, 7\n\ts_mov_b32 m0, %2\n\ts_nop 0\n\tglobal_load_lds_dwordx4 %1, off\n\ts_mov_b64 exec, %0" : "=&s"(ex) : "v"(p), "s"(lds) : "memory");
}
DI void dma16_lanes16(u4v rs, unsigned voff, unsigned soff, unsigned lds) {
    u64 ex;
    asm volatile("s_mov_b64 %0, exec\n\ts_mov_b64 exec, 0xffff\n\ts_mov_b32 m0, %4\n\ts_nop 0\n\tbuffer_load_dwordx4 %1, %2, %3 offen lds\n\ts_mov_b64 exec, %0"
                 : "=&s"(ex) : "v"(voff), "s"(rs), "s"(soff), "s"(lds) : "memory");
}
#define SB() __builtin_amdgcn_sched_barrier(0)
#define WAIT_BAR(n) asm volatile("s_waitcnt vmcnt(" #n ") lgkmcnt(0)\n\ts_barrier" ::: "memory")
#define BAR_L() asm volatile("s_waitcnt lgkmcnt(0)\n\ts_barrier" ::: "memory")
#define WAIT_V(n) asm volatile("s_waitcnt vmcnt(" #n ")" ::: "memory")
template <typename T> DI T ldsr(unsigned a) { return *(LDSP(T))a; }
template <typename T> DI void ldsw(unsigned a, T v) { *(LDSP(T))a = v; }
DI void stw(unsigned a, unsigned v, int off) { asm volatile("ds_write_b32 %0, %1 offset:%2" ::"v"(a), "v"(v), "i"(off) : "memory"); }
DI s4v trr(unsigned a) { return __builtin_amdgcn_ds_read_tr16_b64_v4i16((LDSP(s4v))a); }
DI bf8v cat(s4v a, s4v b) { return __builtin_bit_cast(bf8v, __builtin_shufflevector(a, b, 0, 1, 2, 3, 4, 5, 6, 7)); }
DI bf8v asbf(u4v a) { return __builtin_bit_cast(bf8v, a); }
template <int CTRL> DI float dpp_add(float v) { return v + __builtin_bit_cast(float, __builtin_amdgcn_update_dpp(0, __builtin_bit_cast(int, v), CTRL, 0xf, 0xf, true)); }
DI float sum8(float v) { return dpp_add<0x141>(dpp_add<0x4e>(dpp_add<0xb1>(v))); }
DI float dot2(unsigned a, unsigned b, float c) { return __builtin_amdgcn_fdot2_f32_bf16(__builtin_bit_cast(bf2v, a), __builtin_bit_cast(bf2v, b), c, false); }
DI u64 ld_flag(const char* ws, unsigned off) { return __hip_atomic_load((gu64*)(ws + off), RLX_AGENT); }
DI void wait_flags(const char* ws, unsigned off, u64 tag) {
#pragma nounroll
    for (int it = 0; it < SPIN_LIMIT; ++it) {
        if (__all(ld_flag(ws, off) == tag)) break;
        __builtin_amdgcn_s_sleep(1);
    }
    asm volatile("" ::: "memory");
}
DI unsigned lds_count(unsigned cnt_lds) {
    unsigned old, one = 1u;
    u64 ex;
    asm volatile("s_mov_b64 %1, exec\n\ts_mov_b64 exec, 1\n\ts_waitcnt lgkmcnt(0)\n\tds_add_rtn_u32 %0, %2, %3\n\ts_waitcnt lgkmcnt(0)\n\ts_mov_b64 exec, %1"
                 : "=&v"(old), "=&s"(ex) : "v"(cnt_lds), "v"(one) : "memory");
    return __builtin_amdgcn_readfirstlane(old);
}
DI void raise_flag(char* ws, unsigned flag_off, u64 tag) { __hip_atomic_store((gu64*)(ws + flag_off), tag, RLX_AGENT); }
DI void arrive_publish(unsigned cnt_lds, char* ws, unsigned flag_off, u64 tag, unsigned last = 7u) {
    if (lds_count(cnt_lds) == last) raise_flag(ws, flag_off, tag);
}
DI void lds_wait(unsigned cnt_lds, unsigned n) {
#pragma nounroll
    for (int it = 0; it < SPIN_LIMIT; ++it) {
        if (__builtin_amdgcn_readfirstlane(*(volatile LDSP(unsigned))cnt_lds) >= n) break;
        __builtin_amdgcn_s_sleep(1);
    }
    asm volatile("" ::: "memory");
}
DI unsigned oob_if(bool c) { return c ? 0x80000000u : 0u; }
DI f2v ln_stats(float s1, float s2) {
    const float mean = s1 * (1.f / 640.f), var = fmaxf(__builtin_fmaf(-mean, mean, s2 * (1.f / 640.f)), 0.f);
    const float rs = __builtin_amdgcn_rcpf(rbf(__builtin_amdgcn_sqrtf(rbf(rbf(var) + 1e-5f))));
    return f2v{rs, rbf(mean) * rs};
}
DI f2v cmul_a(f2v a, f2v b) { f2v t; asm("v_pk_mul_f32 %0, %1, %2 op_sel_hi:[0,1]" : "=v"(t) : "v"(a), "v"(b)); return t; }
DI f2v cmul_b(f2v a, f2v b, f2v t) {
    f2v o;
    asm("v_pk_fma_f32 %0, %1, %2, %3 op_sel:[1,1,0] op_sel_hi:[1,0,1] neg_lo:[0,1,0]" : "=v"(o) : "v"(a), "v"(b), "v"(t));
    return o;
}
DI f2v cmul(f2v a, f2v b) { return cmul_b(a, b, cmul_a(a, b)); }
DI f2v cexp_p(float are, float aim, float p) {
    const float m = __builtin_amdgcn_exp2f(are * p), an = __builtin_amdgcn_fractf(aim * p);
    return f2v{m * __builtin_amdgcn_cosf(an), m * __builtin_amdgcn_sinf(an)};
}

DI void addtid10(unsigned m0v, const unsigned (&p)[10]) {
    asm volatile("s_mov_b32 m0, %10\n\ts_nop 0\n\t"
                 "ds_write_addtid_b32 %0 offset:%11\n\tds_write_addtid_b32 %1 offset:%12\n\tds_write_addtid_b32 %2 offset:%13\n\tds_write_addtid_b32 %3 offset:%14\n\t"
                 "ds_write_addtid_b32 %4 offset:%15\n\tds_write_addtid_b32 %5 offset:%16\n\tds_write_addtid_b32 %6 offset:%17\n\tds_write_addtid_b32 %7 offset:%18\n\t"
                 "ds_write_addtid_b32 %8 offset:%19\n\tds_write_addtid_b32 %9 offset:%20"
                 ::"v"(p[0]), "v"(p[1]), "v"(p[2]), "v"(p[3]), "v"(p[4]), "v"(p[5]), "v"(p[6]), "v"(p[7]), "v"(p[8]), "v"(p[9]), "s"(m0v),
                   "i"(0), "i"(RED_ROW), "i"(RED_T), "i"(RED_T + RED_ROW), "i"(2 * RED_T), "i"(2 * RED_T + RED_ROW), "i"(3 * RED_T), "i"(3 * RED_T + RED_ROW),
                   "i"(4 * RED_T), "i"(4 * RED_T + RED_ROW) : "memory");
}

#define GP(T) __attribute__((address_space(1))) T*
typedef unsigned u8v __attribute__((ext_vector_type(8)));
DI u64 mk64(unsigned lo, unsigned hi) { return ((u64)hi << 32) | lo; }
__global__ __launch_bounds__(512, 2) void s4ffn_fused(const u16*, const u16*, const u16*, const u16*, const u16*, const u16*, const u16*, const u16*, const u16*,
                                                      const u16*, const u16*, const u16*, const u16*, u16*, char*) {
    const int tid = threadIdx.x, lane = tid & 63, w = __builtin_amdgcn_readfirstlane(tid >> 6);
    const int xcd = blockIdx.x & 7, r = ((blockIdx.x >> 3) + 28) & 31, bid = r * 8 + xcd;
    const bool is_gemm = r < 28;
    STAMP(0);
    u64 a_x, a_prm;
    u8v a_m;
    u4v a_t;
    asm volatile("s_load_dwordx2 %0, %4, 0x0\n\ts_load_dwordx2 %1, %4, %5 offset:0x8\n\ts_load_dwordx8 %2, %4, 0x48\n\ts_load_dwordx4 %3, %4, 0x68\n\ts_waitcnt lgkmcnt(0)"
                 : "=&s"(a_x), "=&s"(a_prm), "=&s"(a_m), "=&s"(a_t) : "s"(__builtin_amdgcn_kernarg_segment_ptr()), "s"(8 * w));
    const GP(u16) x = (const GP(u16))a_x;
    const char* prm = (const char*)a_prm;
    const GP(u16) w1 = (const GP(u16))mk64(a_m[0], a_m[1]);
    const GP(u16) b1 = (const GP(u16))mk64(a_m[2], a_m[3]);
    const GP(u16) w2 = (const GP(u16))mk64(a_m[4], a_m[5]);
    const GP(u16) b2 = (const GP(u16))mk64(a_m[6], a_m[7]);
    GP(u16) out = (GP(u16))mk64(a_t[0], a_t[1]);
    char* ws = (char*)mk64(a_t[2], a_t[3]);
    u64 tag;
    const auto wsr = __builtin_amdgcn_make_buffer_rsrc((void*)ws, 0, WS_BYTES, 0x00020000);
    const int nch = bid < 192 ? 3 : 2, ch0 = 3 * bid - max(bid - 192, 0);
    const int g = lane >> 4, l16 = lane & 15;
    const int rb2 = xcd * 4 + (r & 3), cb2 = r >> 2;
    const bool has7 = w < 6;
    u4v bw[35];
    int rot2 = 7 * cb2 + 14 * (r & 3) + w;
    rot2 = rot2 >= 54 ? rot2 - 54 : rot2;
    auto sidx = [&](int i) { const int s = rot2 + 8 * i; return s >= 54 ? s - 54 : s; };
    auto loadb = [&](int n) {
        bw[n] = __builtin_amdgcn_raw_buffer_load_b128(wsr, lane * 16 | oob_if(n >= 30 && !has7), WS_W2T + ((cb2 * 54 + sidx(n / 5)) * 5 + n % 5) * 1024, 16);
    };

    if (__builtin_expect(is_gemm, 1)) {
        if (w < 6) dma16g(prm + (ch0 + min(g, nch - 1)) * 256 + l16 * 16, L_PRM + w * 1024);
        else dma16g_lanes3(lane == 2 ? (const char*)ws + WS_E : prm + min(((ch0 * 2) & ~15) + lane * 16, 1264), L_SCR + (w - 6) * 64);
        SB();
        const int gi = r & 3, gj = r >> 2, rb = (xcd >> 1) * 4 + gi, cb = (xcd & 1) * 7 + gj;
        const int fw = w & 3, tw = w >> 2;
        const u4v rx = mkrsrc(x, 2048 * 1280), rw = mkrsrc(w1, 640 * 3456);
        unsigned vx[2];
#pragma unroll
        for (int u = 0; u < 2; ++u) vx[u] = (rb * 128 + 16 * w + 8 * u + (lane >> 3)) * 1280 + (((lane & 7) ^ (4 * u + g)) << 4);
        const int wc = l16 ^ (2 * g) ^ ((w & 1) << 3);
        const unsigned vw = (unsigned)((8 * w + g) * 3456 + cb * 256 + wc * 16) | (cb == 13 ? (unsigned)(wc & 8) << 28 : 0u);
        int rot = gj + 3 * gi + xcd;
        rot = rot >= 20 ? rot - 20 : rot >= 10 ? rot - 10 : rot;
        auto stage1w = [&](int t, int slot, int p, int dw) {
            int kt = t + rot; kt = kt >= 10 ? kt - 10 : kt;
            const unsigned lb = slot * SLOT + (w + dw) * 2048 + (p >> 1) * 1024;
            if (p & 1) dma16(rw, vw + dw * 8 * 3456, kt * (64 * 3456) + (p >> 1) * 4 * 3456, lb + 16384);
            else dma16(rx, vx[p >> 1] + dw * 16 * 1280, kt * 128, lb);
        };
        auto stage1 = [&](int t, int slot, int p) { stage1w(t, slot, p, 0); };
#pragma unroll
        for (int i = 0; i < 4; ++i) stage1(0, 0, i);
        SB();
        unsigned ix[12];
        {
            const int l0 = 8 * (tid & 255) - 3;
            const float lf = (float)l0;
            const unsigned kb = L_TT + tw * TT_CH, kc = kb - 2u * (0x4300 - 128);
#pragma unroll
            for (int i = 0; i < 6; ++i) {
                const unsigned c2 = pk2(lf + (float)(2 * i), lf + (float)(2 * i + 1));
                asm("v_mad_u32_u16 %0, %1, 2, %2" : "=v"(ix[2 * i]) : "v"(c2), "s"(kc));
                asm("v_mad_u32_u16 %0, %1, 2, %2 op_sel:[1,0,0,0]" : "=v"(ix[2 * i + 1]) : "v"(c2), "s"(kc));
            }
            if (fw == 0) {
#pragma unroll
                for (int i = 0; i < 11; ++i) {
                    const int l = l0 + i;
                    const unsigned lo = kb + 2u * min((unsigned)l, 641u), m = (unsigned)((l - 128) >> 31);
                    ix[i] = (lo & m) | (ix[i] & ~m);
                }
            }
#pragma unroll
            for (int i = 0; i < 11; ++i) asm volatile("" : "+v"(ix[i]));
        }
        if (w == 0) {
            ldsw<unsigned>(L_CNT + (lane & 7) * 4, 0u);
            ldsw<u16>(L_TT + min(lane, 2) * TT_CH + 641 * 2, (u16)0);
        }
        STAMP(10);
        WAIT_BAR(4);
        STAMP(11);
        if (w >= 4) {
#pragma unroll
            for (int i = 0; i < 8; ++i) stage1w(1, 1, i & 3, i < 4 ? 0 : -4);
        }
        const unsigned so = (ch0 * 2) & 15;
        { const u2v tv = ldsr<u2v>(L_SCR + 32); tag = mk64(__builtin_amdgcn_readfirstlane(tv[0]), __builtin_amdgcn_readfirstlane(tv[1])) + 1ull; }
        const unsigned dsk01 = __builtin_amdgcn_readfirstlane(ldsr<u16>(L_SCR + 64 + so) | ((unsigned)ldsr<u16>(L_SCR + 64 + so + 2) << 16));
        const unsigned dsk2 = __builtin_amdgcn_readfirstlane(ldsr<u16>(L_SCR + 64 + so + 4));
        {
            const int hc = w < 6 ? w : w - 2, c = hc >> 1, mode = (hc & 1) * 64 + lane;
            const float ldt = bf2f(ldsr<u16>(L_SCR + so + 2 * c));
            const unsigned pb = L_PRM + c * 256 + mode * 2;
            const float pa = bf2f(ldsr<u16>(pb)), pi = bf2f(ldsr<u16>(pb + 1024));
            const f2v Bc = {bf2f(ldsr<u16>(pb + 2048)), bf2f(ldsr<u16>(pb + 3072))}, Cc = {bf2f(ldsr<u16>(pb + 4096)), bf2f(ldsr<u16>(pb + 5120))};
            if (c < nch) {
                const float dt = rbf(__builtin_amdgcn_exp2f(ldt * 1.4426950408889634f));
                const float are = dt * pa * 1.4426950408889634f, aim = dt * pi * 0.15915494309189535f;
                const unsigned tb = L_TBL + c * TBL_CH + mode * 4;
                if (w < 6) {
                    const f2v z8 = cexp_p(are, aim, 8.f), z16 = cexp_p(are, aim, 16.f), z64 = cexp_p(are, aim, 64.f);
                    f2v e[8];
                    e[0] = f2v{1.f, 0.f}; e[1] = z8; e[2] = z16; e[3] = cmul(z16, z8); e[4] = cmul(z16, z16);
                    e[5] = cmul(e[3], z16); e[6] = cmul(e[4], z16); e[7] = cmul(e[4], e[3]);
#pragma unroll
                    for (int i = 0; i < 4; ++i) {
                        f2v t[8];
#pragma unroll
                        for (int j = 0; j < 8; ++j) {
                            if (i < 3) t[j] = cmul_a(e[j], z64);
                            stw(tb, pk2(e[j][0], e[j][1]), (21 + 8 * i + j) * TBL_ROW);
                        }
                        if (i < 3) {
#pragma unroll
                            for (int j = 0; j < 8; ++j) e[j] = cmul_b(e[j], z64, t[j]);
                        }
                    }
                }
                if (w < 4 || w >= 6) {
                    const f2v z1 = cexp_p(are, aim, 1.f), z2 = cexp_p(are, aim, 2.f), z4 = cexp_p(are, aim, 4.f);
                    const f2v z256 = cexp_p(are, aim, 256.f), z1024 = cexp_p(are, aim, 1024.f);
                    const f2v W = cmul(Cc, Bc * dt);
                    auto putx = [&](int row, f2v v) { stw(tb, pk2(v[0], -v[1]), row * TBL_ROW); };
                    const f2v x1 = cmul(W, z1), x2 = cmul(W, z2), x8 = cmul(W, z256), x16 = cmul(W, z1024);
                    const f2v x3 = cmul(x1, z2), x9 = cmul(x8, z2), x12 = cmul(x8, z256), x17 = cmul(x16, z256), x20 = cmul(x16, z1024);
                    const f2v x14 = cmul(x12, z256), x18 = cmul(x17, z256);
                    putx(0, W); putx(1, x1); putx(2, x2); putx(3, x3); putx(8, x8); putx(9, x9); putx(12, x12); putx(14, x14);
                    putx(16, x16); putx(17, x17); putx(18, x18); putx(20, x20);
                    putx(4, cmul(W, z4)); putx(5, cmul(x1, z4)); putx(6, cmul(x2, z4)); putx(7, cmul(x3, z4));
                    putx(10, cmul(x8, z4)); putx(11, cmul(x9, z4)); putx(13, cmul(x12, z4)); putx(15, cmul(x14, z4)); putx(19, cmul(x18, z256));
                }
            }
        }
        BAR_L();
        if (w < 2 * nch) {
            const int c = w >> 1, col = 16 * (w & 1) + l16;
            const unsigned ta = L_TBL + c * TBL_CH + l16 * TBL_ROW + g * 16;
            const unsigned te = ta + (21 + 16 * (w & 1)) * TBL_ROW;
            u4v fa[8], fb[8], fe[8];
#pragma unroll
            for (int s = 0; s < 8; ++s) { fe[s] = ldsr<u4v>(te + s * 64); fa[s] = ldsr<u4v>(ta + s * 64); fb[s] = ldsr<u4v>(ta + 16 * TBL_ROW + s * 64); }
            const unsigned tt = L_TT + c * TT_CH;
            const unsigned ta0 = tt + ((col << (3 - max(g - 1, 0))) + ((0x30200100u >> (8 * g)) & 0xffu) * (g == 1 ? 4u : 8u)) * 2;
            const unsigned ta1 = ta0 + (g < 3 ? 4 : 128);
            const unsigned tb0 = tt + (g == 0 ? 512 + col : 644) * 2, st1 = g == 0 ? 64 : 0;
            const unsigned tb1 = g == 1 && col == 0 ? tt + 640 * 2 : tb0;
            SB();
            f4v k0 = {}, k1 = {};
#pragma unroll
            for (int s = 0; s < 8; ++s) {
                k0 = __builtin_amdgcn_mfma_f32_16x16x32_bf16(asbf(fa[s]), asbf(fe[s]), k0, 0, 0, 0);
                k1 = __builtin_amdgcn_mfma_f32_16x16x32_bf16(asbf(fb[s]), asbf(fe[s]), k1, 0, 0, 0);
            }
            ldsw<unsigned>(ta0, pk2(k0[0], k0[1]));
            ldsw<unsigned>(ta1, pk2(k0[2], k0[3]));
            const unsigned q01 = pk2(k1[0], k1[1]), q23 = pk2(k1[2], k1[3]);
            ldsw<u16>(tb1, (u16)q01);
            ldsw<u16>(tb0 + st1, (u16)(q01 >> 16));
            ldsw<u16>(tb0 + 2 * st1, (u16)q23);
            ldsw<u16>(tb0 + 3 * st1, (u16)(q23 >> 16));
        }
        BAR_L();
        STAMP(14);
        {
            const bool two = w < 4 && nch == 3;
            const int t = tid & 255;
#pragma unroll
            for (int k = 0; k < 2; ++k) {
                if (k == 0 || two) {
                    unsigned pe[6], po[5];
#pragma unroll
                    for (int i = 0; i < 5; ++i) pe[i] = __builtin_bit_cast(unsigned, h2v{ldsr<u16>(ix[2 * i] + k * 2 * TT_CH), ldsr<u16>(ix[2 * i + 1] + k * 2 * TT_CH)});
                    pe[5] = ldsr<u16>(ix[10] + k * 2 * TT_CH);
                    if (k == 0) { stage1(2, 2, 0); stage1(2, 2, 1); stage1(2, 2, 2); stage1(2, 2, 3); }
#pragma unroll
                    for (int i = 0; i < 5; ++i) po[i] = __builtin_amdgcn_alignbit(pe[i + 1], pe[i], 16);
                    const unsigned cbase = L_KC + (tw + 2 * k) * KC_CH, d = cbase + 32 + t * 16;
                    ldsw<u4v>(d, u4v{po[1], po[2], po[3], po[4]});
                    ldsw<u4v>(d + KC_CP, u4v{pe[1], pe[2], pe[3], pe[4]});
                    ldsw<u4v>(d + 2 * KC_CP, u4v{po[0], po[1], po[2], po[3]});
                    ldsw<u4v>(d + 3 * KC_CP, u4v{pe[0], pe[1], pe[2], pe[3]});
                }
            }
            if (w == 7) ldsw<u4v>(L_KC + min(lane >> 1, 11) * KC_CP + (lane & 1) * 16, u4v{0u, 0u, 0u, 0u});
        }
        STAMP(1);
        f4v acc1[2][4];
#pragma unroll
        for (int i = 0; i < 8; ++i) acc1[i >> 2][i & 3] = f4v{};
        unsigned bx[2], aw[2];
        {
            const int q4 = l16 >> 2, p4 = lane & 3;
#pragma unroll
            for (int i = 0; i < 2; ++i) {
                bx[i] = (64 * tw + l16) * 128 + (((4 * i + g) ^ (l16 >> 1)) << 4);
                aw[i] = 16384 + (8 * g + q4) * 256 + (((2 * fw + i) ^ q4 ^ ((g & 1) << 2)) << 5) + p4 * 8;
            }
        }
        bf8v fa[2][2][2], fb[2][2][4];
        auto rd = [&](int buf, int i) {
            const int ks = i >> 1, h = i & 1;
            fa[buf][ks][h] = cat(trr(aw[h] + ks * 8192), trr(aw[h] + ks * 8192 + 1024));
            fb[buf][ks][2 * h] = asbf(ldsr<u4v>(bx[ks] + h * 4096));
            fb[buf][ks][2 * h + 1] = asbf(ldsr<u4v>(bx[ks] + h * 4096 + 2048));
        };
        unsigned sb = 0;
        int kt = rot + 2; kt = kt >= 10 ? kt - 10 : kt;
        auto tile = [&](int cur, bool more, bool stage, auto&& side) {
            const unsigned d = sb == 2 * SLOT ? (unsigned)(-2 * (int)SLOT) : SLOT, lb = sb + w * 2048;
            kt = kt == 9 ? 0 : kt + 1;
            sb += d; aw[0] += d; aw[1] += d; bx[0] += d; bx[1] += d;
#pragma unroll
            for (int i = 0; i < 4; ++i) {
                SB();
                if (more) rd(cur ^ 1, i);
                SB();
#pragma unroll
                for (int j = 0; j < 4; ++j) acc1[i & 1][j] = __builtin_amdgcn_mfma_f32_16x16x32_bf16(fa[cur][i >> 1][i & 1], fb[cur][i >> 1][j], acc1[i & 1][j], 0, 0, 0);
                SB();
                if (stage) {
                    if (i & 1) dma16(rw, vw, kt * (64 * 3456) + (i >> 1) * 4 * 3456, lb + (i >> 1) * 1024 + 16384);
                    else dma16(rx, vx[i >> 1], kt * 128, lb + (i >> 1) * 1024);
                }
                side(i);
            }
            SB();
        };
        auto none = [&](int) {};
        if (w >= 4) WAIT_V(12); else WAIT_V(4);
        BAR_L();
        STAMP(15);
#pragma unroll
        for (int i = 0; i < 4; ++i) rd(0, i);
        u4v uld[2], cbv;
        f2v sv[4];
        u64 fpoll = 0;
        const unsigned f1a = WS_FS + 2 * rb * 128, f1b = WS_FC + 2 * cb * 128;
        const unsigned f1_off = min(((lane & 2) ? f1b : f1a) + (lane & 1) * 128, WS_FC + 26 * 128), f2_off = WS_FA + lane * 128;
#pragma nounroll
        for (int t = 0; t < 3; ++t) {
            WAIT_BAR(4); tile(0, true, true, none);
            WAIT_BAR(4); tile(1, true, true, none);
        }
        WAIT_BAR(4); tile(0, true, true, none);
        WAIT_BAR(4); tile(1, true, false, [&](int i) { if (i == 0 && w == 0) fpoll = ld_flag(ws, f1_off); });
        if (w == 0 && !__all(fpoll == tag)) wait_flags(ws, f1_off, tag);
        WAIT_BAR(0);
        STAMP(3);
        tile(0, true, false, [&](int i) {
            sv[i] = __builtin_bit_cast(f2v, __builtin_amdgcn_raw_buffer_load_b64(wsr, l16 * 8, WS_STATS + (rb * 128 + 64 * tw + 16 * i) * 8, 16));
            if (i == 0 && w == 1) cbv = __builtin_amdgcn_raw_buffer_load_b128(wsr, (lane >> 5) * (WS_B1 - WS_CS) + (lane & 31) * 16, WS_CS + cb * 512, 16);
            if (i == 3 && w == 0) fpoll = ld_flag(ws, f2_off);
        });
        tile(1, false, false, none);
        if (w == 1) ldsw<u4v>(L_CB + lane * 16, cbv);
        if (w >= 2 && w < 5) ldsw<u4v>(L_UA + (w - 2) * UA_CH + min(lane, 29) * 16, u4v{0u, 0u, 0u, 0u});
        if (w == 0 && !__all(fpoll == tag)) wait_flags(ws, f2_off, tag);
        BAR_L();
        STAMP(2);
        {
            f4v cs[2], bb[2];
#pragma unroll
            for (int u = 0; u < 2; ++u) { cs[u] = ldsr<f4v>(L_CB + (32 * fw + 16 * u + 4 * g) * 4); bb[u] = ldsr<f4v>(L_CB + 512 + (32 * fw + 16 * u + 4 * g) * 4); }
            const int s = 4 * cb + fw;
            const unsigned ho = (unsigned)(WS_HA + ((2 * rb + tw) * 54 + s) * 4096 + lane * 16) | oob_if(s >= 54);
#pragma unroll
            for (int ti = 0; ti < 4; ++ti) {
                const f2v rs2 = {sv[ti][0], sv[ti][0]}, nm2 = {-sv[ti][1], -sv[ti][1]};
                unsigned hp[4];
#pragma unroll
                for (int i = 0; i < 4; ++i) {
                    const int slot = 4 * ti + i, u = i >> 1, e = 2 * (i & 1);
                    if (slot < 2) {
                        const int q = w + 8 * slot, c = q >> 2;
                        uld[slot] = __builtin_amdgcn_raw_buffer_load_b128(wsr, lane * 16 | oob_if(c >= nch), WS_XT + (ch0 + c) * 4096 + (q & 3) * 1024, 16);
                    } else if ((slot & 1) == 0 && slot < 12) loadb(slot / 2 - 1);
                    SB();
                    const f2v v = __builtin_elementwise_fma(rs2, f2v{acc1[u][ti][e], acc1[u][ti][e + 1]}, __builtin_elementwise_fma(nm2, f2v{cs[u][e], cs[u][e + 1]}, f2v{bb[u][e], bb[u][e + 1]}));
                    const f2v z = v * __builtin_elementwise_fma(v * v, f2v{-0.10294324f, -0.10294324f}, f2v{-2.30220820f, -2.30220820f});
                    const f2v d = f2v{__builtin_amdgcn_exp2f(z[0]), __builtin_amdgcn_exp2f(z[1])} + f2v{1.f, 1.f};
                    const f2v gl = v * f2v{__builtin_amdgcn_rcpf(d[0]), __builtin_amdgcn_rcpf(d[1])};
                    hp[i] = pk2(gl[0], gl[1]);
                }
                __builtin_amdgcn_raw_buffer_store_b128(u4v{hp[0], hp[1], hp[2], hp[3]}, wsr, ho + 1024 * ti, 0, 16);
                SB();
            }
        }
#pragma unroll
        for (int i = 0; i < 2; ++i) {
            const int q = w + 8 * i, c = q >> 2;
            if (q < 12) ldsw<u4v>(L_UA + c * UA_CH + UA_PAD + (q & 3) * 1024 + lane * 16, uld[i]);
        }
        STAMP(4);
        BAR_L();
        {
            const int typ = w < 3 ? 0 : w < 6 ? 1 : 2;
            const int c1 = w < 3 ? w : w < 6 ? w - 3 : 2 * (w - 6), c2 = w == 6 ? 1 : c1;
            const unsigned kl = L_KC + (l16 >> 2) * KC_CP + 56 - 16 * g + 8 * (lane & 3);
            const unsigned ul = L_UA + UA_PAD + 32 * l16 - 32 + 16 * g - 448;
            unsigned ka[3], ua[3];
            ka[0] = kl + c1 * KC_CH;
            ka[1] = typ == 2 ? kl + c2 * KC_CH - 4 * 512 : ka[0];
            ka[2] = typ == 1 ? ka[0] - 6 * 512 : ka[1];
            ua[0] = ul + c1 * UA_CH - typ * 1024;
            ua[2] = ul + c2 * UA_CH;
            ua[1] = typ == 2 ? ua[2] : ua[0];
            bf8v uf[8], kf[8];
            auto rdk = [&](int sl, int rho) { const unsigned ad = ka[sl < 4 ? 0 : sl < 6 ? 1 : 2] + 512u * sl + 64u * rho; kf[sl] = cat(trr(ad + 8u), trr(ad)); };
            auto rdu = [&](int j, int rho) { uf[j] = asbf(ldsr<u4v>(ua[j >= 4 ? 0 : j >= 2 ? 1 : 2] + 448u - 64u * rho + 512u * j)); };
#pragma unroll
            for (int sl = 0; sl < 8; ++sl) { rdk(sl, 0); rdu(7 - sl, 0); }
            f4v A[3] = {}, B[5] = {};
            const unsigned fo = WS_FH + (rb * 14 + cb) * 128;
#pragma unroll
            for (int rho = 0; rho < 8; ++rho) {
#pragma unroll
                for (int sl = 0; sl < 8; ++sl) {
                    SB();
                    const int ia = sl < 4 ? 0 : sl < 6 ? 1 : 2, ib = sl < 3 ? 0 : sl - 2;
                    A[ia] = __builtin_amdgcn_mfma_f32_16x16x32_bf16(kf[sl], uf[7 - sl], A[ia], 0, 0, 0);
                    if (sl < 7) B[ib] = __builtin_amdgcn_mfma_f32_16x16x32_bf16(kf[sl], uf[6 - sl], B[ib], 0, 0, 0);
                    SB();
                    if (rho < 7) { rdk(sl, rho + 1); rdu(7 - sl, rho + 1); }
                    SB();
                    if (rho == PUB_RHO && sl == 0) {
                        asm volatile("s_waitcnt vmcnt(%0)" ::"i"(3 * PUB_RHO) : "memory");
                        arrive_publish(L_CNT, ws, fo, tag);
                    }
                    if (sl == 1 || sl == 4 || sl == 6) loadb(5 + 3 * rho + (sl == 1 ? 0 : sl == 4 ? 1 : 2));
                }
            }
            SB();
            f4v y[4];
            if (typ == 0) { y[0] = A[0] + A[1] + A[2]; y[1] = (B[0] + B[1]) + (B[2] + B[3]) + B[4]; y[2] = y[0]; y[3] = y[1]; }
            else if (typ == 1) { y[0] = A[0] + A[1]; y[1] = B[0] + B[1] + B[2]; y[2] = A[2]; y[3] = B[4]; }
            else { y[0] = A[0]; y[1] = B[0]; y[2] = A[1] + A[2]; y[3] = B[2] + B[3] + B[4]; }
#pragma unroll
            for (int i = 0; i < 4; ++i) {
                const int c = i < 2 ? c1 : c2, tau = (i < 2 ? 7 - 2 * typ : 2 * typ - 1) - (i & 1);
                const bool on = c < nch && (i < 2 || (typ != 0 && w != 7));
                const u2v uv = ldsr<u2v>(L_UA + c * UA_CH + UA_PAD + 512 * max(tau, 0) + 32 * l16 + 8 * g);
                const float dk = c == 0 ? bflo(dsk01) : c == 1 ? bfhi(dsk01) : bflo(dsk2);
                const u2v o = {pk2(y[i][0] + dk * bflo(uv[0]), y[i][1] + dk * bfhi(uv[0])), pk2(y[i][2] + dk * bflo(uv[1]), y[i][3] + dk * bfhi(uv[1]))};
                __builtin_amdgcn_raw_buffer_store_b64(o, wsr, (32 * l16 + 8 * g) | oob_if(!on), WS_YT + (ch0 + c) * 4096 + 512 * max(tau, 0), 16);
            }
        }
        STAMP(5);
    } else {
        const int a = (r - 28) * 8 + xcd;
        const u4v rx = mkrsrc(x, 2048 * 1280), rw = mkrsrc(w2, 1728 * 1280);
        {
            unsigned vo[5];
#pragma unroll
            for (int i = 0; i < 5; ++i) {
                const int n = i * 64 + lane, rl = n / 80, cp = n - rl * 80;
                vo[i] = (a * 64 + 8 * w + rl) * 1280 + ((cp ^ (2 * w) ^ ((rl >> 1) << 3)) << 4);
                dma16(rx, vo[i], 0, L_AX + (w * 10 + i) * 1024);
            }
#pragma unroll
            for (int i = 5; i < 10; ++i) dma16(rx, vo[i - 5], 4 * 1280, L_AX + (w * 10 + i) * 1024);
        }
        const u64 e0 = ld_flag(ws, WS_E);
#pragma unroll
        for (int m = 0; m < 9; ++m) {
            const int q = w + 8 * m, j = q / 10, i = q - 10 * j, id = a + 32 * j, i5 = i >= 5 ? i - 5 : i;
            const int n = i5 * 64 + lane, rl = n / 40;
            dma16(rw, (unsigned)(rl * 1280 + (n - rl * 40) * 16) | oob_if(id >= 216), ((id >> 2) * 32 + ((id >> 1) & 1) * 8 + (i >= 5 ? 16 : 0)) * 1280 + (id & 1) * 640,
                  id < 216 ? L_AW + q * 1024 : L_ACS);
        }
        if (w == 0) ldsw<unsigned>(L_ACNT + (lane & 7) * 4, 0u);
        STAMP(10);
        WAIT_V(10);
        STAMP(11);
        {
            const int row = 8 * w + (lane >> 3), sw = (2 * w) ^ (((row >> 1) & 1) << 3);
            u4v v[10];
#pragma unroll
            for (int i = 0; i < 10; ++i) v[i] = ldsr<u4v>(L_AX + row * 1280 + (((lane & 7) + 8 * i) ^ sw) * 16);
            SB();
            float s1[2] = {0.f, 0.f}, s2[2] = {0.f, 0.f};
#pragma unroll
            for (int i = 0; i < 10; ++i)
#pragma unroll
                for (int j = 0; j < 4; ++j) { s1[j & 1] = dot2(v[i][j], 0x3f803f80u, s1[j & 1]); s2[j & 1] = dot2(v[i][j], v[i][j], s2[j & 1]); }
            const f2v sv = ln_stats(sum8(s1[0] + s1[1]), sum8(s2[0] + s2[1]));
            ldsw<f2v>(L_AST + row * 8, sv);
            __builtin_amdgcn_raw_buffer_store_b64(__builtin_bit_cast(u2v, sv), wsr, (WS_STATS + (a * 64 + row) * 8) | oob_if(lane & 7), 0, 16);
        }
        BAR_L();
        STAMP(14);
        u4v cv[10];
        {
            const auto rw1 = __builtin_amdgcn_make_buffer_rsrc((void*)(u64)w1, 0, 640 * 3456, 0x00020000);
            const int o = lane & 7, cq = lane >> 3;
            f2v rs2[4], nm2[4];
#pragma unroll
            for (int i = 0; i < 4; ++i) { const f4v q = ldsr<f4v>(L_AST + o * 64 + i * 16); rs2[i] = f2v{q[0], q[2]}; nm2[i] = f2v{-q[1], -q[3]}; }
            u2v v[3][8];
#pragma unroll
            for (int k = 0; k < 3; ++k)
#pragma unroll
                for (int t = 0; t < 8; ++t) v[k][t] = ldsr<u2v>(L_AX + (8 * o + t) * 1280 + ((4 * min(w + 8 * k, 19) + (cq >> 1)) ^ ((2 * o) ^ (((t >> 1) & 1) << 3))) * 16 + (cq & 1) * 8);
            SB();
#pragma unroll
            for (int k = 0; k < 3; ++k) {
                if (k < 2 || w < 4) {
#pragma unroll
                    for (int e = 0; e < 4; ++e) {
                        u4v ov;
#pragma unroll
                        for (int p = 0; p < 4; ++p) {
                            const unsigned va = v[k][2 * p][e >> 1], vb = v[k][2 * p + 1][e >> 1];
                            const f2v n2 = __builtin_elementwise_fma((e & 1) ? f2v{bfhi(va), bfhi(vb)} : f2v{bflo(va), bflo(vb)}, rs2[p], nm2[p]);
                            ov[p] = pk2(n2[0], n2[1]);
                        }
                        __builtin_amdgcn_raw_buffer_store_b128(ov, wsr, WS_XT + (32 * (w + 8 * k) + 4 * cq + e) * 4096 + a * 128 + o * 16, 0, 16);
                        SB();
                    }
                }
                if (k == 0) {
                    WAIT_V(4);
                    tag = mk64(__builtin_amdgcn_readfirstlane((unsigned)e0), __builtin_amdgcn_readfirstlane((unsigned)(e0 >> 32))) + 1ull;
                    arrive_publish(L_ACNT + 8, ws, WS_FS + a * 128, tag);
                }
            }
#pragma unroll
            for (int i = 0; i < 10; ++i) cv[i] = __builtin_amdgcn_raw_buffer_load_b128(rw1, ((lane >> 3) * 3456 + (lane & 7) * 16) | oob_if(a >= 27), (80 * w + 8 * i) * 3456 + a * 128, 0);
        }
        STAMP(15);
        BAR_L();
        {
            const unsigned tl = L_AW + (4 * (g & 1) + (l16 >> 2)) * 640 + (lane >> 5) * 32 + (lane & 3) * 8;
            bf8v f[9];
#pragma unroll
            for (int m = 0; m < 9; ++m) { const int q = min(w + 8 * m, 69), j = q / 10, i = q - 10 * j; f[m] = cat(trr(tl + (j * 10240 + i * 64)), trr(tl + (j * 10240 + i * 64) + 8 * 640)); }
            SB();
#pragma unroll
            for (int m = 0; m < 9; ++m) {
                const int q = w + 8 * m, j = q / 10, i = q - 10 * j, id = a + 32 * j;
                const int P = 20 * (id & 1) + 2 * i + (lane >> 5), cbb = P / 5, cfr = P - cbb * 5;
                __builtin_amdgcn_raw_buffer_store_b128(__builtin_bit_cast(u4v, f[m]), wsr,
                    (unsigned)(WS_W2T + (id >> 2) * 5120 + ((id >> 1) & 1) * 512 + (cbb * 270 + cfr) * 1024 + (lane & 31) * 16) | oob_if(id >= 216), 0, 16);
            }
        }
        WAIT_V(19);
        arrive_publish(L_ACNT + 12, ws, WS_FA + a * 128, tag);
        STAMP(1);
        {
            unsigned selo = 0x00003f80u, sehi = 0x3f800000u;
            asm volatile("" : "+v"(selo), "+v"(sehi));
            float sm[8] = {0.f, 0.f, 0.f, 0.f, 0.f, 0.f, 0.f, 0.f};
#pragma unroll
            for (int i = 0; i < 10; ++i)
#pragma unroll
                for (int j = 0; j < 4; ++j) { sm[2 * j] = dot2(cv[i][j], selo, sm[2 * j]); sm[2 * j + 1] = dot2(cv[i][j], sehi, sm[2 * j + 1]); }
#pragma unroll
            for (int j = 0; j < 8; ++j) sm[j] = dpp_add<0x128>(sm[j]);
            ldsw<f4v>(L_ACS + (4 * w + g) * 256 + (lane & 7) * 32 + (lane & 8) * 2, (lane & 8) ? f4v{sm[4], sm[5], sm[6], sm[7]} : f4v{sm[0], sm[1], sm[2], sm[3]});
        }
        WAIT_V(0);
        arrive_publish(L_ACNT + 16, ws, WS_FW + a * 128, tag);
        BAR_L();
        if (w == 7 && a < 27) {
            const float bf = bf2f(b1[64 * a + lane]);
            float p[32], sum = 0.f;
#pragma unroll
            for (int i = 0; i < 32; ++i) p[i] = ldsr<float>(L_ACS + i * 256 + lane * 4);
#pragma unroll
            for (int i = 0; i < 32; i += 4) sum += (p[i] + p[i + 1]) + (p[i + 2] + p[i + 3]);
            __builtin_amdgcn_raw_buffer_store_b32(__builtin_bit_cast(unsigned, sum), wsr, WS_CS + (64 * a + lane) * 4, 0, 16);
            __builtin_amdgcn_raw_buffer_store_b32(__builtin_bit_cast(unsigned, bf), wsr, WS_B1 + (64 * a + lane) * 4, 0, 16);
            WAIT_V(0);
            raise_flag(ws, WS_FC + a * 128, tag);
        }
        STAMP(2);
        if (w == 0) wait_flags(ws, WS_FW + (lane & 31) * 128, tag);
        BAR_L();
        STAMP(3);
#pragma unroll
        for (int n = 0; n < 29; ++n) loadb(n);
        __builtin_amdgcn_s_waitcnt(0x0F70);
    }

#pragma unroll
    for (int n = 29; n < 35; ++n) loadb(n);
    if (is_gemm) {
        WAIT_V(6);
        arrive_publish(L_CNT + 4, ws, WS_FY + bid * 128, tag);
    }
    if (w == 0) wait_flags(ws, WS_FH + ((rb2 >> 1) * 14 + min(lane, 13)) * 128, tag);
    BAR_L();
    STAMP(6);
    {
        const u4v rx = mkrsrc(x, 2048 * 1280);
        const int n1 = 64 + (lane & 15);
        dma16(rx, (lane / 10) * 1280 + (lane % 10) * 16, (rb2 * 64 + 8 * w) * 1280 + cb2 * 160, L_XS + w * 1280);
        dma16_lanes16(rx, (n1 / 10) * 1280 + (n1 % 10) * 16, (rb2 * 64 + 8 * w) * 1280 + cb2 * 160, L_XS + w * 1280 + 1024);
        if (w == 0) dma16_lanes16(mkrsrc(b2, 1280), min(lane, 9) * 16, cb2 * 160, L_XS + 10240);
    }
    u4v ha[7][4];
    auto loada = [&](int i, int t) { ha[i][t] = __builtin_amdgcn_raw_buffer_load_b128(wsr, lane * 16, WS_HA + ((rb2 * 54 + sidx(i)) * 4 + t) * 1024, 16); };
#pragma unroll
    for (int i = 0; i < 8; ++i) loada(i >> 2, i & 3);
    const int c0 = 80 * cb2;
    int lz = lane;
    asm volatile("" : "+v"(lz));
    const int cA = c0 + 10 * w, cB = cA + 9;
    const int own0 = cA < 576 ? cA / 3 : 192 + (cA - 576) / 2, own1 = cB < 576 ? cB / 3 : 192 + (cB - 576) / 2;
    u64 ypoll = tag;
    u4v y0, y1;
    f4v acc2[4][5];
#pragma unroll
    for (int i = 0; i < 20; ++i) acc2[i / 5][i % 5] = f4v{};
#pragma unroll
    for (int i = 0; i < 5; ++i) {
#pragma unroll
        for (int t = 0; t < 4; ++t) {
            SB();
#pragma unroll
            for (int c = 0; c < 5; ++c) acc2[t][c] = __builtin_amdgcn_mfma_f32_16x16x32_bf16(asbf(ha[i][t]), asbf(bw[5 * i + c]), acc2[t][c], 0, 0, 0);
            SB();
            loada(i + 2, t);
            if (i == 1 && t == 3) ypoll = ld_flag(ws, WS_FY + min(own0 + lz, own1) * 128);
            if (i == 3 && t == 3) {
                if (!__all(ypoll == tag)) wait_flags(ws, WS_FY + min(own0 + lz, own1) * 128, tag);
                y0 = __builtin_amdgcn_raw_buffer_load_b128(wsr, (lz >> 3) * 4096 + (lz & 7) * 16, WS_YT + cA * 4096 + rb2 * 128, 16);
                y1 = __builtin_amdgcn_raw_buffer_load_b128(wsr, (unsigned)((8 + (lz >> 3)) * 4096 + (lz & 7) * 16) | oob_if(lz >= 16), WS_YT + cA * 4096 + rb2 * 128, 16);
            }
        }
    }
    STAMP(7);
    auto dump = [&](int t) {
        unsigned p[10];
#pragma unroll
        for (int c = 0; c < 5; ++c) { p[2 * c] = pk2(acc2[t][c][0], acc2[t][c][1]); p[2 * c + 1] = pk2(acc2[t][c][2], acc2[t][c][3]); }
        addtid10(L_RED + w * RED_W + t * 5 * RED_T, p);
    };
#pragma unroll
    for (int t = 0; t < 4; ++t) {
        SB();
#pragma unroll
        for (int c = 0; c < 5; ++c) acc2[t][c] = __builtin_amdgcn_mfma_f32_16x16x32_bf16(asbf(ha[5][t]), asbf(bw[25 + c]), acc2[t][c], 0, 0, 0);
        SB();
        if (t > 0) dump(t - 1);
        SB();
#pragma unroll
        for (int c = 0; c < 5; ++c) acc2[t][c] = __builtin_amdgcn_mfma_f32_16x16x32_bf16(asbf(ha[6][t]), asbf(bw[30 + c]), acc2[t][c], 0, 0, 0);
    }
    SB();
    ldsw<u4v>(L_YS + (10 * w + (lz >> 3)) * YS_ROW + (lz & 7) * 16, y0);
    ldsw<u4v>(lz < 16 ? L_YS + (10 * w + 8 + (lz >> 3)) * YS_ROW + (lz & 7) * 16 : L_YS + 80 * YS_ROW, y1);
    dump(3);
    BAR_L();
    STAMP(8);
    {
        unsigned selo = 0x00003f80u, sehi = 0x3f800000u;
        asm volatile("" : "+v"(selo), "+v"(sehi));
#pragma unroll
        for (int k = 0; k < 3; ++k) {
            if (k < 2 || w < 4) {
                const int id = w * 64 + lz + 512 * k, tp = id / 40, c2 = id - tp * 40;
                const unsigned ra = L_RED + ((tp >> 3) * 5 + (c2 >> 3)) * RED_T + (tp & 1) * RED_ROW + ((tp >> 1) & 3) * 64 + (c2 & 7) * 8;
                u2v p[8];
#pragma unroll
                for (int j = 0; j < 8; ++j) p[j] = ldsr<u2v>(ra + j * RED_W);
                const unsigned ya = ldsr<unsigned>(L_YS + 2 * c2 * YS_ROW + tp * 4), yb = ldsr<unsigned>(L_YS + (2 * c2 + 1) * YS_ROW + tp * 4);
                const unsigned xa = ldsr<unsigned>(L_XS + tp * 320 + c2 * 4), xb = ldsr<unsigned>(L_XS + tp * 320 + 160 + c2 * 4), bv = ldsr<unsigned>(L_XS + 10240 + c2 * 4);
                float o00 = bflo(ya) + bflo(xa) + bflo(bv), o01 = bflo(yb) + bfhi(xa) + bfhi(bv);
                float o10 = bfhi(ya) + bflo(xb) + bflo(bv), o11 = bfhi(yb) + bfhi(xb) + bfhi(bv);
#pragma unroll
                for (int j = 0; j < 8; ++j) {
                    o00 = dot2(p[j][0], selo, o00); o10 = dot2(p[j][0], sehi, o10);
                    o01 = dot2(p[j][1], selo, o01); o11 = dot2(p[j][1], sehi, o11);
                }
                GP(unsigned) op = (GP(unsigned))(out + (rb2 * 64 + 2 * tp) * 640 + c0 + 2 * c2);
                op[0] = pk2(o00, o01);
                op[320] = pk2(o10, o11);
            }
        }
    }
    if (bid == 0 && w == 5) {
#pragma unroll
        for (int i = 0; i < 4; ++i) wait_flags(ws, WS_FH + (i * 64 + lz) * 128, tag);
        raise_flag(ws, WS_E, tag);
    }
}

extern "C" void kernel_launch(void* const* d_in, const int* in_sizes, int n_in, void* d_out, int out_size, void* d_ws, size_t ws_size, hipStream_t stream) {
    (void)in_sizes; (void)n_in; (void)out_size; (void)ws_size;
    (void)hipFuncSetAttribute((const void*)s4ffn_fused, hipFuncAttributeMaxDynamicSharedMemorySize, LDS_BYTES);
    s4ffn_fused<<<256, 512, LDS_BYTES, stream>>>((const u16*)d_in[0], (const u16*)d_in[1], (const u16*)d_in[2], (const u16*)d_in[3], (const u16*)d_in[4],
                                                 (const u16*)d_in[5], (const u16*)d_in[6], (const u16*)d_in[7], (const u16*)d_in[8], (const u16*)d_in[10],
                                                 (const u16*)d_in[11], (const u16*)d_in[12], (const u16*)d_in[13], (u16*)d_out, (char*)d_ws);
}
```

```cpp
#include <hip/hip_runtime.h>
#include <stdint.h>
#define PUB_RHO 4
#define CSUM_ON 1
#define CODE_CHUNKS 4
#define CODE_BYTES 28672u
#define ARR 1

typedef unsigned short u16;
typedef unsigned long long u64;
typedef short s4v __attribute__((ext_vector_type(4)));
typedef __bf16 bf8v __attribute__((ext_vector_type(8)));
typedef __bf16 bf2v __attribute__((ext_vector_type(2)));
typedef float f2v __attribute__((ext_vector_type(2)));
typedef float f4v __attribute__((ext_vector_type(4)));
typedef float f16v __attribute__((ext_vector_type(16)));
typedef unsigned u2v __attribute__((ext_vector_type(2)));
typedef unsigned short h2v __attribute__((ext_vector_type(2)));
typedef unsigned u4v __attribute__((ext_vector_type(4)));
typedef __attribute__((address_space(1))) u64 gu64;
#define LDSP(T) __attribute__((address_space(3))) T*
#define DI __device__ __forceinline__
#define RLX_AGENT __ATOMIC_RELAXED, __HIP_MEMORY_SCOPE_AGENT

constexpr unsigned WS_E = 0;
constexpr unsigned WS_FH = 4096;
constexpr unsigned WS_FA = 32768;
constexpr unsigned WS_FW = 81920;
constexpr unsigned WS_FS = 40960;
constexpr unsigned WS_FC = 45056;
constexpr unsigned WS_FY = 49152;
constexpr unsigned WS_STATS = 131072;
constexpr unsigned WS_CS = 196608, WS_B1 = 204800;
constexpr unsigned WS_STAMP = 262144;
constexpr unsigned WS_XT = 1u << 20;
constexpr unsigned WS_W2T = 4u << 20;
constexpr unsigned WS_YT = 8u << 20;
constexpr unsigned WS_HA = 12u << 20;
constexpr unsigned WS_BYTES = 20u << 20;
constexpr unsigned SLOT = 32768;
constexpr unsigned L_KC = 98304, KC_CH = 16640, KC_CP = 4160;
constexpr unsigned L_UA = 148224, UA_CH = 4576, UA_PAD = 480;
constexpr unsigned L_MISC = 162368, L_CB = L_MISC, L_CNT = L_MISC + 1024;
constexpr unsigned L_TBL = 65536, TBL_ROW = 544, TBL_CH = 53 * TBL_ROW;
constexpr unsigned L_PRM = 152064, L_TT = 158464, TT_CH = 1296, L_W2U = L_PRM;
constexpr unsigned L_SCR = L_CB;
constexpr unsigned L_XS = 0, L_RED = 16384, RED_ROW = 256, RED_T = 576, RED_W = 20 * RED_T, L_YS = L_RED + 8 * RED_W, YS_ROW = 144;
constexpr unsigned L_AX = 0, L_AST = 81920, L_ACNT = L_AST + 512, L_ACS = L_AST + 1024;
static_assert(L_TT + 3 * TT_CH <= L_MISC && L_CNT + 64 <= 163840 && L_W2U + 10240 <= L_MISC, "LDS map");
constexpr unsigned LDS_BYTES = 163840;
constexpr int SPIN_LIMIT = 400000;

#define STAMP(i)

DI float bf2f(unsigned v) { return __uint_as_float(v << 16); }
DI float bflo(unsigned v) { return __uint_as_float(v << 16); }
DI float bfhi(unsigned v) { return __uint_as_float(v & 0xffff0000u); }
DI unsigned pk2(float a, float b) { bf2v v; v[0] = (__bf16)a; v[1] = (__bf16)b; return __builtin_bit_cast(unsigned, v); }
DI float rbf(float a) { return bflo(pk2(a, a)); }
template <typename P> DI u4v mkrsrc(P p, unsigned bytes) { u64 a = (u64)p; u4v r; r[0] = (unsigned)a; r[1] = (unsigned)(a >> 32); r[2] = bytes; r[3] = 0x00020000u; return r; }
DI void dma16(u4v rs, unsigned voff, unsigned soff, unsigned lds) {
    asm volatile("s_mov_b32 m0, %3\n\ts_nop 0\n\tbuffer_load_dwordx4 %0, %1, %2 offen lds" ::"v"(voff), "s"(rs), "s"(soff), "s"(lds) : "memory");
}
DI void dma16g(const void* p, unsigned lds) {
    asm volatile("s_mov_b32 m0, %1\n\ts_nop 0\n\tglobal_load_lds_dwordx4 %0, off" ::"v"(p), "s"(lds) : "memory");
}
DI void dma16g_lanes3(const void* p, unsigned lds) {
    u64 ex;
    asm volatile("s_mov_b64 %0, exec\n\ts_mov_b64 exec, 7\n\ts_mov_b32 m0, %2\n\ts_nop 0\n\tglobal_load_lds_dwordx4 %1, off\n\ts_mov_b64 exec, %0" : "=&s"(ex) : "v"(p), "s"(lds) : "memory");
}
DI void dma16_lanes16(u4v rs, unsigned voff, unsigned soff, unsigned lds) {
    u64 ex;
    asm volatile("s_mov_b64 %0, exec\n\ts_mov_b64 exec, 0xffff\n\ts_mov_b32 m0, %4\n\ts_nop 0\n\tbuffer_load_dwordx4 %1, %2, %3 offen lds\n\ts_mov_b64 exec, %0"
                 : "=&s"(ex) : "v"(voff), "s"(rs), "s"(soff), "s"(lds) : "memory");
}
DI void touch_code(unsigned chunk, int lane, unsigned lds) {
    u64 pc;
    asm volatile("s_getpc_b64 %0" : "=s"(pc));
    const u64 p = (pc & ~127ull) + min(chunk * 8192u + lane * 128u, CODE_BYTES - 128u);
    asm volatile("s_mov_b32 m0, %1\n\ts_nop 0\n\tglobal_load_lds_dword %0, off" ::"v"(p), "s"(lds) : "memory");
}
#define SB() __builtin_amdgcn_sched_barrier(0)
#define WAIT_BAR(n) asm volatile("s_waitcnt vmcnt(" #n ") lgkmcnt(0)\n\ts_barrier" ::: "memory")
#define BAR_L() asm volatile("s_waitcnt lgkmcnt(0)\n\ts_barrier" ::: "memory")
#define WAIT_V(n) asm volatile("s_waitcnt vmcnt(" #n ")" ::: "memory")
template <typename T> DI T ldsr(unsigned a) { return *(LDSP(T))a; }
template <typename T> DI void ldsw(unsigned a, T v) { *(LDSP(T))a = v; }
DI void stw(unsigned a, unsigned v, int off) { asm volatile("ds_write_b32 %0, %1 offset:%2" ::"v"(a), "v"(v), "i"(off) : "memory"); }
DI s4v trr(unsigned a) { return __builtin_amdgcn_ds_read_tr16_b64_v4i16((LDSP(s4v))a); }
DI bf8v cat(s4v a, s4v b) { return __builtin_bit_cast(bf8v, __builtin_shufflevector(a, b, 0, 1, 2, 3, 4, 5, 6, 7)); }
DI bf8v asbf(u4v a) { return __builtin_bit_cast(bf8v, a); }
template <int CTRL> DI float dpp_add(float v) { return v + __builtin_bit_cast(float, __builtin_amdgcn_update_dpp(0, __builtin_bit_cast(int, v), CTRL, 0xf, 0xf, true)); }
DI float sum8(float v) { return dpp_add<0x141>(dpp_add<0x4e>(dpp_add<0xb1>(v))); }
DI float dot2(unsigned a, unsigned b, float c) { return __builtin_amdgcn_fdot2_f32_bf16(__builtin_bit_cast(bf2v, a), __builtin_bit_cast(bf2v, b), c, false); }
DI u64 ld_flag(const char* ws, unsigned off) { return __hip_atomic_load((gu64*)(ws + off), RLX_AGENT); }
DI void wait_flags(const char* ws, unsigned off, u64 tag) {
#pragma nounroll
    for (int it = 0; it < SPIN_LIMIT; ++it) {
        if (__all(ld_flag(ws, off) == tag)) break;
        __builtin_amdgcn_s_sleep(1);
    }
    asm volatile("" ::: "memory");
}
DI unsigned lds_count(unsigned cnt_lds) {
    unsigned old, one = 1u;
    u64 ex;
    asm volatile("s_mov_b64 %1, exec\n\ts_mov_b64 exec, 1\n\ts_waitcnt lgkmcnt(0)\n\tds_add_rtn_u32 %0, %2, %3\n\ts_waitcnt lgkmcnt(0)\n\ts_mov_b64 exec, %1"
                 : "=&v"(old), "=&s"(ex) : "v"(cnt_lds), "v"(one) : "memory");
    return __builtin_amdgcn_readfirstlane(old);
}
DI void raise_flag(char* ws, unsigned flag_off, u64 tag) { __hip_atomic_store((gu64*)(ws + flag_off), tag, RLX_AGENT); }
DI void arrive_publish(unsigned cnt_lds, char* ws, unsigned flag_off, u64 tag, unsigned last = 7u) {
    if (lds_count(cnt_lds) == last) raise_flag(ws, flag_off, tag);
}
DI void lds_wait(unsigned cnt_lds, unsigned n) {
#pragma nounroll
    for (int it = 0; it < SPIN_LIMIT; ++it) {
        if (__builtin_amdgcn_readfirstlane(*(volatile LDSP(unsigned))cnt_lds) >= n) break;
        __builtin_amdgcn_s_sleep(1);
    }
    asm volatile("" ::: "memory");
}
DI unsigned oob_if(bool c) { return c ? 0x80000000u : 0u; }
DI f2v ln_stats(float s1, float s2) {
    const float mean = s1 * (1.f / 640.f), var = fmaxf(__builtin_fmaf(-mean, mean, s2 * (1.f / 640.f)), 0.f);
    const float rs = __builtin_amdgcn_rcpf(rbf(__builtin_amdgcn_sqrtf(rbf(rbf(var) + 1e-5f))));
    return f2v{rs, rbf(mean) * rs};
}
DI f2v cmul_a(f2v a, f2v b) { f2v t; asm("v_pk_mul_f32 %0, %1, %2 op_sel_hi:[0,1]" : "=v"(t) : "v"(a), "v"(b)); return t; }
DI f2v cmul_b(f2v a, f2v b, f2v t) {
    f2v o;
    asm("v_pk_fma_f32 %0, %1, %2, %3 op_sel:[1,1,0] op_sel_hi:[1,0,1] neg_lo:[0,1,0]" : "=v"(o) : "v"(a), "v"(b), "v"(t));
    return o;
}
DI f2v cmul(f2v a, f2v b) { return cmul_b(a, b, cmul_a(a, b)); }
DI f2v cexp_p(float are, float aim, float p) {
    const float m = __builtin_amdgcn_exp2f(are * p), an = __builtin_amdgcn_fractf(aim * p);
    return f2v{m * __builtin_amdgcn_cosf(an), m * __builtin_amdgcn_sinf(an)};
}

DI void addtid10(unsigned m0v, const unsigned (&p)[10]) {
    asm volatile("s_mov_b32 m0, %10\n\ts_nop 0\n\t"
                 "ds_write_addtid_b32 %0 offset:%11\n\tds_write_addtid_b32 %1 offset:%12\n\tds_write_addtid_b32 %2 offset:%13\n\tds_write_addtid_b32 %3 offset:%14\n\t"
                 "ds_write_addtid_b32 %4 offset:%15\n\tds_write_addtid_b32 %5 offset:%16\n\tds_write_addtid_b32 %6 offset:%17\n\tds_write_addtid_b32 %7 offset:%18\n\t"
                 "ds_write_addtid_b32 %8 offset:%19\n\tds_write_addtid_b32 %9 offset:%20"
                 ::"v"(p[0]), "v"(p[1]), "v"(p[2]), "v"(p[3]), "v"(p[4]), "v"(p[5]), "v"(p[6]), "v"(p[7]), "v"(p[8]), "v"(p[9]), "s"(m0v),
                   "i"(0), "i"(RED_ROW), "i"(RED_T), "i"(RED_T + RED_ROW), "i"(2 * RED_T), "i"(2 * RED_T + RED_ROW), "i"(3 * RED_T), "i"(3 * RED_T + RED_ROW),
                   "i"(4 * RED_T), "i"(4 * RED_T + RED_ROW) : "memory");
}

#define GP(T) __attribute__((address_space(1))) T*
typedef unsigned u8v __attribute__((ext_vector_type(8)));
DI u64 mk64(unsigned lo, unsigned hi) { return ((u64)hi << 32) | lo; }
__global__ __launch_bounds__(512, 2) void s4ffn_fused(const u16*, const u16*, const u16*, const u16*, const u16*, const u16*, const u16*, const u16*, const u16*,
                                                      const u16*, const u16*, const u16*, const u16*, u16*, char*) {
    const int tid = threadIdx.x, lane = tid & 63, w = __builtin_amdgcn_readfirstlane(tid >> 6);
    const int xcd = blockIdx.x & 7, r = ((blockIdx.x >> 3) + 28) & 31, bid = r * 8 + xcd;
    const bool is_gemm = r < 28;
    STAMP(0);
    if (w < CODE_CHUNKS && ((r + w) & 3) == 0) touch_code(w, lane, 163840 - 256);
    u64 a_x, a_prm;
    u8v a_m;
    u4v a_t;
    asm volatile("s_load_dwordx2 %0, %4, 0x0\n\ts_load_dwordx2 %1, %4, %5 offset:0x8\n\ts_load_dwordx8 %2, %4, 0x48\n\ts_load_dwordx4 %3, %4, 0x68\n\ts_waitcnt lgkmcnt(0)"
                 : "=&s"(a_x), "=&s"(a_prm), "=&s"(a_m), "=&s"(a_t) : "s"(__builtin_amdgcn_kernarg_segment_ptr()), "s"(8 * w));
    const GP(u16) x = (const GP(u16))a_x;
    const char* prm = (const char*)a_prm;
    const GP(u16) w1 = (const GP(u16))mk64(a_m[0], a_m[1]);
    const GP(u16) b1 = (const GP(u16))mk64(a_m[2], a_m[3]);
    const GP(u16) w2 = (const GP(u16))mk64(a_m[4], a_m[5]);
    const GP(u16) b2 = (const GP(u16))mk64(a_m[6], a_m[7]);
    GP(u16) out = (GP(u16))mk64(a_t[0], a_t[1]);
    char* ws = (char*)mk64(a_t[2], a_t[3]);
    u64 tag;
    const auto wsr = __builtin_amdgcn_make_buffer_rsrc((void*)ws, 0, WS_BYTES, 0x00020000);
    const int nch = bid < 192 ? 3 : 2, ch0 = 3 * bid - max(bid - 192, 0);
    const int g = lane >> 4, l16 = lane & 15;
    const int rb2 = xcd * 4 + (r & 3), cb2 = r >> 2;
    const bool has7 = w < 6;
    u4v bw[35];
    int rot2 = 7 * cb2 + 14 * (r & 3) + w;
    rot2 = rot2 >= 54 ? rot2 - 54 : rot2;
    auto sidx = [&](int i) { const int s = rot2 + 8 * i; return s >= 54 ? s - 54 : s; };
    auto loadb = [&](int n) {
        bw[n] = __builtin_amdgcn_raw_buffer_load_b128(wsr, lane * 16 | oob_if(n >= 30 && !has7), WS_W2T + ((cb2 * 54 + sidx(n / 5)) * 5 + n % 5) * 1024, 16);
    };

    auto stage_x = [&]() {
        const u4v rx = mkrsrc(x, 2048 * 1280);
        const int n1 = 64 + (lane & 15);
        dma16(rx, (lane / 10) * 1280 + (lane % 10) * 16, (rb2 * 64 + 8 * w) * 1280 + cb2 * 160, L_XS + w * 1280);
        dma16_lanes16(rx, (n1 / 10) * 1280 + (n1 % 10) * 16, (rb2 * 64 + 8 * w) * 1280 + cb2 * 160, L_XS + w * 1280 + 1024);
        if (w == 0) dma16_lanes16(mkrsrc(b2, 1280), min(lane, 9) * 16, cb2 * 160, L_XS + 10240);
    };
    if (__builtin_expect(is_gemm, 1)) {
        if (w < 6) dma16g(prm + (ch0 + min(g, nch - 1)) * 256 + l16 * 16, L_PRM + w * 1024);
        else dma16g_lanes3(lane == 2 ? (const char*)ws + WS_E : prm + min(((ch0 * 2) & ~15) + lane * 16, 1264), L_SCR + (w - 6) * 64);
        SB();
        const int gi = r & 1, gj = r >> 1, rb = xcd * 2 + gi, cb = gj;
        const int fw = w & 3, tw = w >> 2;
        const u4v rx = mkrsrc(x, 2048 * 1280), rw = mkrsrc(w1, 640 * 3456);
        unsigned vx[2];
#pragma unroll
        for (int u = 0; u < 2; ++u) vx[u] = (rb * 128 + 16 * w + 8 * u + (lane >> 3)) * 1280 + (((lane & 7) ^ (4 * u + g)) << 4);
        const int wc = l16 ^ (2 * g) ^ ((w & 1) << 3);
        const unsigned vw = (unsigned)((8 * w + g) * 3456 + cb * 256 + wc * 16) | (cb == 13 ? (unsigned)(wc & 8) << 28 : 0u);
        int rot = gj + 3 * gi + xcd;
        rot = rot >= 20 ? rot - 20 : rot >= 10 ? rot - 10 : rot;
        auto stage1w = [&](int t, int slot, int p, int dw) {
            int kt = t + rot; kt = kt >= 10 ? kt - 10 : kt;
            const unsigned lb = slot * SLOT + (w + dw) * 2048 + (p >> 1) * 1024;
            if (p & 1) dma16(rw, vw + dw * 8 * 3456, kt * (64 * 3456) + (p >> 1) * 4 * 3456, lb + 16384);
            else dma16(rx, vx[p >> 1] + dw * 16 * 1280, kt * 128, lb);
        };
        auto stage1 = [&](int t, int slot, int p) { stage1w(t, slot, p, 0); };
#pragma unroll
        for (int i = 0; i < 4; ++i) stage1(0, 0, i);
        u4v w2a, w2b;
        {
            const auto rw2 = __builtin_amdgcn_make_buffer_rsrc((void*)(u64)w2, 0, 1728 * 1280, 0x00020000);
            const unsigned so2 = ((bid >> 2) * 32 + ((bid >> 1) & 1) * 8 + (w >> 2) * 16 + (w & 3) * 2) * 1280 + (bid & 1) * 640;
            w2a = __builtin_amdgcn_raw_buffer_load_b128(rw2, lane * 16 + (lane >= 40 ? 640 : 0), so2, 0);
            w2b = __builtin_amdgcn_raw_buffer_load_b128(rw2, (1280 + 384 + lane * 16) | oob_if(lane >= 16), so2, 0);
        }
        SB();
        unsigned ix[12];
        {
            const int l0 = 8 * (tid & 255) - 3;
            const float lf = (float)l0;
            const unsigned kb = L_TT + tw * TT_CH, kc = kb - 2u * (0x4300 - 128);
#pragma unroll
            for (int i = 0; i < 6; ++i) {
                const unsigned c2 = pk2(lf + (float)(2 * i), lf + (float)(2 * i + 1));
                asm("v_mad_u32_u16 %0, %1, 2, %2" : "=v"(ix[2 * i]) : "v"(c2), "s"(kc));
                asm("v_mad_u32_u16 %0, %1, 2, %2 op_sel:[1,0,0,0]" : "=v"(ix[2 * i + 1]) : "v"(c2), "s"(kc));
            }
            if (fw == 0) {
#pragma unroll
                for (int i = 0; i < 11; ++i) {
                    const int l = l0 + i;
                    const unsigned lo = kb + 2u * min((unsigned)l, 641u), m = (unsigned)((l - 128) >> 31);
                    ix[i] = (lo & m) | (ix[i] & ~m);
                }
            }
#pragma unroll
            for (int i = 0; i < 11; ++i) asm volatile("" : "+v"(ix[i]));
        }
        if (w == 0) {
            ldsw<unsigned>(L_CNT + (lane & 7) * 4, 0u);
        }
        STAMP(10);
        WAIT_BAR(6);
        STAMP(11);
        const unsigned so = (ch0 * 2) & 15;
        { const u2v tv = ldsr<u2v>(L_SCR + 32); tag = mk64(__builtin_amdgcn_readfirstlane(tv[0]), __builtin_amdgcn_readfirstlane(tv[1])) + 1ull; }
        const unsigned dsk01 = __builtin_amdgcn_readfirstlane(ldsr<u16>(L_SCR + 64 + so) | ((unsigned)ldsr<u16>(L_SCR + 64 + so + 2) << 16));
        const unsigned dsk2 = __builtin_amdgcn_readfirstlane(ldsr<u16>(L_SCR + 64 + so + 4));
        {
            const int hc = w < 6 ? w : w - 2, c = hc >> 1, mode = (hc & 1) * 64 + lane;
            const float ldt = bf2f(ldsr<u16>(L_SCR + so + 2 * c));
            const unsigned pb = L_PRM + c * 256 + mode * 2;
            const float pa = bf2f(ldsr<u16>(pb)), pi = bf2f(ldsr<u16>(pb + 1024));
            const f2v Bc = {bf2f(ldsr<u16>(pb + 2048)), bf2f(ldsr<u16>(pb + 3072))}, Cc = {bf2f(ldsr<u16>(pb + 4096)), bf2f(ldsr<u16>(pb + 5120))};
            lds_count(L_CNT + 12);
            if (w >= 4) {
#pragma unroll
                for (int i = 0; i < 8; ++i) stage1w(1, 1, i & 3, i < 4 ? 0 : -4);
            }
            if (c < nch) {
                const float dt = rbf(__builtin_amdgcn_exp2f(ldt * 1.4426950408889634f));
                const float are = dt * pa * 1.4426950408889634f, aim = dt * pi * 0.15915494309189535f;
                const unsigned tb = L_TBL + c * TBL_CH + mode * 4;
                if (w < 6) {
                    const f2v z8 = cexp_p(are, aim, 8.f), z16 = cexp_p(are, aim, 16.f), z64 = cexp_p(are, aim, 64.f);
                    f2v e[8];
                    e[0] = f2v{1.f, 0.f}; e[1] = z8; e[2] = z16; e[3] = cmul(z16, z8); e[4] = cmul(z16, z16);
                    e[5] = cmul(e[3], z16); e[6] = cmul(e[4], z16); e[7] = cmul(e[4], e[3]);
#pragma unroll
                    for (int i = 0; i < 4; ++i) {
                        f2v t[8];
#pragma unroll
                        for (int j = 0; j < 8; ++j) {
                            if (i < 3) t[j] = cmul_a(e[j], z64);
                            stw(tb, pk2(e[j][0], e[j][1]), (21 + 8 * i + j) * TBL_ROW);
                        }
                        if (i < 3) {
#pragma unroll
                            for (int j = 0; j < 8; ++j) e[j] = cmul_b(e[j], z64, t[j]);
                        }
                    }
                }
                if (w < 4 || w >= 6) {
                    const f2v z1 = cexp_p(are, aim, 1.f), z2 = cexp_p(are, aim, 2.f), z4 = cexp_p(are, aim, 4.f);
                    const f2v z256 = cexp_p(are, aim, 256.f), z1024 = cexp_p(are, aim, 1024.f);
                    const f2v W = cmul(Cc, Bc * dt);
                    auto putx = [&](int row, f2v v) { stw(tb, pk2(v[0], -v[1]), row * TBL_ROW); };
                    const f2v x1 = cmul(W, z1), x2 = cmul(W, z2), x8 = cmul(W, z256), x16 = cmul(W, z1024);
                    const f2v x3 = cmul(x1, z2), x9 = cmul(x8, z2), x12 = cmul(x8, z256), x17 = cmul(x16, z256), x20 = cmul(x16, z1024);
                    const f2v x14 = cmul(x12, z256), x18 = cmul(x17, z256);
                    putx(0, W); putx(1, x1); putx(2, x2); putx(3, x3); putx(8, x8); putx(9, x9); putx(12, x12); putx(14, x14);
                    putx(16, x16); putx(17, x17); putx(18, x18); putx(20, x20);
                    putx(4, cmul(W, z4)); putx(5, cmul(x1, z4)); putx(6, cmul(x2, z4)); putx(7, cmul(x3, z4));
                    putx(10, cmul(x8, z4)); putx(11, cmul(x9, z4)); putx(13, cmul(x12, z4)); putx(15, cmul(x14, z4)); putx(19, cmul(x18, z256));
                }
            }
        }
        lds_wait(L_CNT + 12, 8u);
        ldsw<u4v>(L_W2U + w * 1280 + lane * 16, w2a);
        ldsw<u4v>(lane < 16 ? L_W2U + w * 1280 + 1024 + lane * 16 : L_CNT + 48, w2b);
        STAMP(13);
        BAR_L();
        if (w >= 6) {
            const unsigned tl = L_W2U + (4 * (g & 1) + (l16 >> 2)) * 640 + (lane >> 5) * 32 + (lane & 3) * 8 + (w - 6) * 320;
            bf8v f[5];
#pragma unroll
            for (int k = 0; k < 5; ++k) f[k] = cat(trr(tl + k * 64), trr(tl + k * 64 + 8 * 640));
            lds_count(L_CNT + 16);
#pragma unroll
            for (int k = 0; k < 5; ++k) {
                const int P = 20 * (bid & 1) + 10 * (w - 6) + 2 * k + (lane >> 5), cbb = (P * 52) >> 8, cfr = P - cbb * 5;
                __builtin_amdgcn_raw_buffer_store_b128(__builtin_bit_cast(u4v, f[k]), wsr,
                    (unsigned)(WS_W2T + (bid >> 2) * 5120 + ((bid >> 1) & 1) * 512 + (cbb * 270 + cfr) * 1024 + (lane & 31) * 16) | oob_if(bid >= 216), 0, 16);
            }
        }
        if (w < 2 * nch) {
            const int c = w >> 1, col = 16 * (w & 1) + l16;
            const unsigned ta = L_TBL + c * TBL_CH + l16 * TBL_ROW + g * 16;
            const unsigned te = ta + (21 + 16 * (w & 1)) * TBL_ROW;
            u4v fa[8], fb[8], fe[8];
#pragma unroll
            for (int s = 0; s < 8; ++s) { fe[s] = ldsr<u4v>(te + s * 64); fa[s] = ldsr<u4v>(ta + s * 64); fb[s] = ldsr<u4v>(ta + 16 * TBL_ROW + s * 64); }
            const unsigned tt = L_TT + c * TT_CH;
            const unsigned ta0 = tt + ((col << (3 - max(g - 1, 0))) + ((0x30200100u >> (8 * g)) & 0xffu) * (g == 1 ? 4u : 8u)) * 2;
            const unsigned ta1 = ta0 + (g < 3 ? 4 : 128);
            const unsigned tb0 = tt + (g == 0 ? 512 + col : 644) * 2, st1 = g == 0 ? 64 : 0;
            const unsigned tb1 = g == 1 && col < 2 ? tt + (640 + col) * 2 : tb0;
            SB();
            f4v k0 = {}, k1 = {};
#pragma unroll
            for (int s = 0; s < 8; ++s) {
                k0 = __builtin_amdgcn_mfma_f32_16x16x32_bf16(asbf(fa[s]), asbf(fe[s]), k0, 0, 0, 0);
                k1 = __builtin_amdgcn_mfma_f32_16x16x32_bf16(asbf(fb[s]), asbf(fe[s]), k1, 0, 0, 0);
            }
            lds_wait(L_CNT + 16, 2u);
            ldsw<unsigned>(ta0, pk2(k0[0], k0[1]));
            ldsw<unsigned>(ta1, pk2(k0[2], k0[3]));
            const unsigned q01 = pk2(k1[0], k1[1]), q23 = pk2(k1[2], k1[3]);
            ldsw<u16>(tb1, g == 1 && col == 1 ? (u16)0 : (u16)q01);
            ldsw<u16>(tb0 + st1, (u16)(q01 >> 16));
            ldsw<u16>(tb0 + 2 * st1, (u16)q23);
            ldsw<u16>(tb0 + 3 * st1, (u16)(q23 >> 16));
        }
        BAR_L();
        STAMP(14);
        {
            const bool two = w < 4 && nch == 3;
            const int t = tid & 255;
#pragma unroll
            for (int k = 0; k < 2; ++k) {
                if (k == 0 || two) {
                    unsigned pe[6], po[5];
#pragma unroll
                    for (int i = 0; i < 5; ++i) pe[i] = __builtin_bit_cast(unsigned, h2v{ldsr<u16>(ix[2 * i] + k * 2 * TT_CH), ldsr<u16>(ix[2 * i + 1] + k * 2 * TT_CH)});
                    pe[5] = ldsr<u16>(ix[10] + k * 2 * TT_CH);
                    if (k == 0) { stage1(2, 2, 0); stage1(2, 2, 1); stage1(2, 2, 2); stage1(2, 2, 3); }
#pragma unroll
                    for (int i = 0; i < 5; ++i) po[i] = __builtin_amdgcn_alignbit(pe[i + 1], pe[i], 16);
                    const unsigned cbase = L_KC + (tw + 2 * k) * KC_CH, d = cbase + 32 + t * 16;
                    ldsw<u4v>(d, u4v{po[1], po[2], po[3], po[4]});
                    ldsw<u4v>(d + KC_CP, u4v{pe[1], pe[2], pe[3], pe[4]});
                    ldsw<u4v>(d + 2 * KC_CP, u4v{po[0], po[1], po[2], po[3]});
                    ldsw<u4v>(d + 3 * KC_CP, u4v{pe[0], pe[1], pe[2], pe[3]});
                }
            }
            if (w == 7) ldsw<u4v>(L_KC + min(lane >> 1, 11) * KC_CP + (lane & 1) * 16, u4v{0u, 0u, 0u, 0u});
        }
        STAMP(1);
        f4v acc1[2][4];
#pragma unroll
        for (int i = 0; i < 8; ++i) acc1[i >> 2][i & 3] = f4v{};
        unsigned bx[2], aw[2];
        {
            const int q4 = l16 >> 2, p4 = lane & 3;
#pragma unroll
            for (int i = 0; i < 2; ++i) {
                bx[i] = (64 * tw + l16) * 128 + (((4 * i + g) ^ (l16 >> 1)) << 4);
                aw[i] = 16384 + (8 * g + q4) * 256 + (((2 * fw + i) ^ q4 ^ ((g & 1) << 2)) << 5) + p4 * 8;
            }
        }
        bf8v fa[2][2][2], fb[2][2][4];
        auto rd = [&](int buf, int i) {
            const int ks = i >> 1, h = i & 1;
            fa[buf][ks][h] = cat(trr(aw[h] + ks * 8192), trr(aw[h] + ks * 8192 + 1024));
            fb[buf][ks][2 * h] = asbf(ldsr<u4v>(bx[ks] + h * 4096));
            fb[buf][ks][2 * h + 1] = asbf(ldsr<u4v>(bx[ks] + h * 4096 + 2048));
        };
        unsigned sb = 0;
        int kt = rot + 2; kt = kt >= 10 ? kt - 10 : kt;
        auto tile = [&](int cur, bool more, bool stage, auto&& side) {
            const unsigned d = sb == 2 * SLOT ? (unsigned)(-2 * (int)SLOT) : SLOT, lb = sb + w * 2048;
            kt = kt == 9 ? 0 : kt + 1;
            sb += d; aw[0] += d; aw[1] += d; bx[0] += d; bx[1] += d;
#pragma unroll
            for (int i = 0; i < 4; ++i) {
                SB();
                if (more) rd(cur ^ 1, i);
                SB();
#pragma unroll
                for (int j = 0; j < 4; ++j) acc1[i & 1][j] = __builtin_amdgcn_mfma_f32_16x16x32_bf16(fa[cur][i >> 1][i & 1], fb[cur][i >> 1][j], acc1[i & 1][j], 0, 0, 0);
                SB();
                if (stage) {
                    if (i & 1) dma16(rw, vw, kt * (64 * 3456) + (i >> 1) * 4 * 3456, lb + (i >> 1) * 1024 + 16384);
                    else dma16(rx, vx[i >> 1], kt * 128, lb + (i >> 1) * 1024);
                }
                side(i);
            }
            SB();
        };
        auto none = [&](int) {};
        if (w >= 6) {
            WAIT_V(4);
            arrive_publish(L_CNT + 8, ws, WS_FW + min(bid, 216) * 128, tag, 1u);
        }
        BAR_L();
        STAMP(15);
#pragma unroll
        for (int i = 0; i < 4; ++i) rd(0, i);
        u4v uld[2], cbv;
        f2v sv[4];
        u64 fpoll = 0;
        const unsigned f1a = WS_FS + 2 * rb * 128, f1b = WS_FC + 2 * cb * 128;
        const unsigned f1_off = min(((lane & 2) ? f1b : f1a) + (lane & 1) * 128, WS_FC + 26 * 128);
        const unsigned f2_off = w == 2 ? WS_FA + (lane & 31) * 128 : WS_FW + (2 * min(54 * w + lane, 107) + (cb2 >> 2)) * 128;
        WAIT_BAR(4); tile(0, true, true, none);
        WAIT_BAR(4); tile(1, true, true, none);
        WAIT_BAR(4); tile(0, true, true, [&](int i) { if (i == 0 && w < 2) fpoll = ld_flag(ws, f2_off); });
        if (w < 2 && !__all(fpoll == tag)) wait_flags(ws, f2_off, tag);
        WAIT_BAR(4);
        STAMP(12);
        tile(1, true, true, [&](int i) { if (i == 0) loadb(0); if (i == 2) loadb(1); });
        WAIT_BAR(6); tile(0, true, true, [&](int i) { if (i == 0) loadb(2); if (i == 2) loadb(3); });
        WAIT_BAR(6); tile(1, true, true, [&](int i) { if (i == 0) loadb(4); if (i == 2) loadb(5); });
        WAIT_BAR(6); tile(0, true, true, [&](int i) { if (i == 0) loadb(6); if (i == 2) loadb(7); });
        WAIT_BAR(6); tile(1, true, false, [&](int i) { if (i == 0 && w == 0) fpoll = ld_flag(ws, f1_off); loadb(8 + i); });
        if (w == 0 && !__all(fpoll == tag)) wait_flags(ws, f1_off, tag);
        WAIT_BAR(4);
        STAMP(3);
        tile(0, true, false, [&](int i) {
            sv[i] = __builtin_bit_cast(f2v, __builtin_amdgcn_raw_buffer_load_b64(wsr, l16 * 8, WS_STATS + (rb * 128 + 64 * tw + 16 * i) * 8, 16));
            if (i == 0 && w == 1) cbv = __builtin_amdgcn_raw_buffer_load_b128(wsr, (lane >> 5) * (WS_B1 - WS_CS) + (lane & 31) * 16, WS_CS + cb * 512, 16);
            if (i == 0 && w == 2) fpoll = ld_flag(ws, f2_off);
            loadb(12 + i);
        });
        tile(1, false, false, [&](int i) { loadb(16 + i); });
        if (w == 1) ldsw<u4v>(L_CB + lane * 16, cbv);
        if (w >= 2 && w < 5) ldsw<u4v>(L_UA + (w - 2) * UA_CH + min(lane, 29) * 16, u4v{0u, 0u, 0u, 0u});
        if (w == 2 && !__all(fpoll == tag)) wait_flags(ws, f2_off, tag);
        BAR_L();
        STAMP(2);
        stage_x();
        {
            f4v cs[2], bb[2];
#pragma unroll
            for (int u = 0; u < 2; ++u) { cs[u] = ldsr<f4v>(L_CB + (32 * fw + 16 * u + 4 * g) * 4); bb[u] = ldsr<f4v>(L_CB + 512 + (32 * fw + 16 * u + 4 * g) * 4); }
            const int s = 4 * cb + fw;
            const unsigned ho = (unsigned)(WS_HA + ((2 * rb + tw) * 54 + s) * 4096 + lane * 16) | oob_if(s >= 54);
            u4v hp[4];
#pragma unroll
            for (int ti = 0; ti < 4; ++ti) {
                const f2v rs2 = {sv[ti][0], sv[ti][0]}, nm2 = {-sv[ti][1], -sv[ti][1]};
#pragma unroll
                for (int i = 0; i < 4; ++i) {
                    const int slot = 4 * ti + i, u = i >> 1, e = 2 * (i & 1);
                    if (slot < 2) {
                        const int q = w + 8 * slot, c = q >> 2;
                        uld[slot] = __builtin_amdgcn_raw_buffer_load_b128(wsr, lane * 16 | oob_if(c >= nch), WS_XT + (ch0 + c) * 4096 + (q & 3) * 1024, 16);
                    }
                    if (slot >= 2 && slot < 12) loadb(18 + slot);
                    SB();
                    const f2v v = __builtin_elementwise_fma(rs2, f2v{acc1[u][ti][e], acc1[u][ti][e + 1]}, __builtin_elementwise_fma(nm2, f2v{cs[u][e], cs[u][e + 1]}, f2v{bb[u][e], bb[u][e + 1]}));
                    const f2v z = v * __builtin_elementwise_fma(v * v, f2v{-0.10294324f, -0.10294324f}, f2v{-2.30220820f, -2.30220820f});
                    const f2v d = f2v{__builtin_amdgcn_exp2f(z[0]), __builtin_amdgcn_exp2f(z[1])} + f2v{1.f, 1.f};
                    const f2v gl = v * f2v{__builtin_amdgcn_rcpf(d[0]), __builtin_amdgcn_rcpf(d[1])};
                    hp[ti][i] = pk2(gl[0], gl[1]);
                    asm volatile("" : "+v"(hp[ti][i]));
                }
            }
            SB();
#pragma unroll
            for (int ti = 0; ti < 4; ++ti)
                __builtin_amdgcn_raw_buffer_store_b128(hp[ti], wsr, ho + 1024 * ti, 0, 16);
        }
#pragma unroll
        for (int i = 0; i < 2; ++i) {
            const int q = w + 8 * i, c = q >> 2;
            if (q < 12) ldsw<u4v>(L_UA + c * UA_CH + UA_PAD + (q & 3) * 1024 + lane * 16, uld[i]);
        }
        STAMP(4);
        BAR_L();
        {
            const int typ = w < 3 ? 0 : w < 6 ? 1 : 2;
            const int c1 = w < 3 ? w : w < 6 ? w - 3 : 2 * (w - 6), c2 = w == 6 ? 1 : c1;
            const unsigned kl = L_KC + (l16 >> 2) * KC_CP + 56 - 16 * g + 8 * (lane & 3);
            const unsigned ul = L_UA + UA_PAD + 32 * l16 - 32 + 16 * g;
            unsigned ka[3], ua[3];
            ka[0] = kl + c1 * KC_CH;
            ka[1] = typ == 2 ? kl + c2 * KC_CH - 4 * 512 : ka[0];
            ka[2] = typ == 1 ? ka[0] - 6 * 512 : ka[1];
            ua[0] = ul + c1 * UA_CH - typ * 1024;
            ua[2] = ul + c2 * UA_CH;
            ua[1] = typ == 2 ? ua[2] : ua[0];
            bf8v uf[8], kf[8];
            auto rdk = [&](int sl) { const unsigned ad = ka[sl < 4 ? 0 : sl < 6 ? 1 : 2] + 512u * sl; kf[sl] = cat(trr(ad + 8u), trr(ad)); };
            auto rdu = [&](int j) { uf[j] = asbf(ldsr<u4v>(ua[j >= 4 ? 0 : j >= 2 ? 1 : 2] + 512u * j)); };
#pragma unroll
            for (int sl = 0; sl < 8; ++sl) { rdk(sl); rdu(7 - sl); }
            f4v A[3] = {}, B[5] = {};
            const unsigned fo = WS_FH + (rb * 14 + cb) * 128;
#pragma nounroll
            for (int rho = 0; rho < 7; ++rho) {
#pragma unroll
                for (int i = 0; i < 3; ++i) { ka[i] += 64u; ua[i] -= 64u; asm volatile("" : "+v"(ka[i]), "+v"(ua[i])); }
#pragma unroll
                for (int sl = 0; sl < 8; ++sl) {
                    SB();
                    const int ia = sl < 4 ? 0 : sl < 6 ? 1 : 2, ib = sl < 3 ? 0 : sl - 2;
                    A[ia] = __builtin_amdgcn_mfma_f32_16x16x32_bf16(kf[sl], uf[7 - sl], A[ia], 0, 0, 0);
                    if (sl < 7) B[ib] = __builtin_amdgcn_mfma_f32_16x16x32_bf16(kf[sl], uf[6 - sl], B[ib], 0, 0, 0);
                    SB();
                    rdk(sl); rdu(7 - sl);
                }
                SB();
                if (rho == PUB_RHO) {
                    WAIT_V(0);
                    arrive_publish(L_CNT, ws, fo, tag);
                }
            }
#pragma unroll
            for (int sl = 0; sl < 8; ++sl) {
                const int ia = sl < 4 ? 0 : sl < 6 ? 1 : 2, ib = sl < 3 ? 0 : sl - 2;
                A[ia] = __builtin_amdgcn_mfma_f32_16x16x32_bf16(kf[sl], uf[7 - sl], A[ia], 0, 0, 0);
                if (sl < 7) B[ib] = __builtin_amdgcn_mfma_f32_16x16x32_bf16(kf[sl], uf[6 - sl], B[ib], 0, 0, 0);
            }
            SB();
            f4v y[4];
            if (typ == 0) { y[0] = A[0] + A[1] + A[2]; y[1] = (B[0] + B[1]) + (B[2] + B[3]) + B[4]; y[2] = y[0]; y[3] = y[1]; }
            else if (typ == 1) { y[0] = A[0] + A[1]; y[1] = B[0] + B[1] + B[2]; y[2] = A[2]; y[3] = B[4]; }
            else { y[0] = A[0]; y[1] = B[0]; y[2] = A[1] + A[2]; y[3] = B[2] + B[3] + B[4]; }
#pragma unroll
            for (int i = 0; i < 4; ++i) {
                const int c = i < 2 ? c1 : c2, tau = (i < 2 ? 7 - 2 * typ : 2 * typ - 1) - (i & 1);
                const bool on = c < nch && (i < 2 || (typ != 0 && w != 7));
                const u2v uv = ldsr<u2v>(L_UA + c * UA_CH + UA_PAD + 512 * max(tau, 0) + 32 * l16 + 8 * g);
                const float dk = c == 0 ? bflo(dsk01) : c == 1 ? bfhi(dsk01) : bflo(dsk2);
                const u2v o = {pk2(y[i][0] + dk * bflo(uv[0]), y[i][1] + dk * bfhi(uv[0])), pk2(y[i][2] + dk * bflo(uv[1]), y[i][3] + dk * bfhi(uv[1]))};
                __builtin_amdgcn_raw_buffer_store_b64(o, wsr, 32 * l16 + 8 * g, (WS_YT + (ch0 + c) * 4096 + 512 * max(tau, 0)) | oob_if(!on), 16);
            }
        }
        STAMP(5);
    } else {
        const int a = (r - 28) * 8 + xcd;
        const u4v rx = mkrsrc(x, 2048 * 1280);
        {
            unsigned vo[5];
#pragma unroll
            for (int i = 0; i < 5; ++i) {
                const int n = i * 64 + lane, rl = n / 80, cp = n - rl * 80;
                vo[i] = (a * 64 + 8 * w + rl) * 1280 + ((cp ^ (2 * w) ^ ((rl >> 1) << 3)) << 4);
                dma16(rx, vo[i], 0, L_AX + (w * 10 + i) * 1024);
            }
#pragma unroll
            for (int i = 5; i < 10; ++i) dma16(rx, vo[i - 5], 4 * 1280, L_AX + (w * 10 + i) * 1024);
        }
        const u64 e0 = ld_flag(ws, WS_E);
        u4v cv[10];
        {
            const auto rw1 = __builtin_amdgcn_make_buffer_rsrc((void*)(u64)w1, 0, 640 * 3456, 0x00020000);
#pragma unroll
            for (int i = 0; i < 10; ++i) cv[i] = __builtin_amdgcn_raw_buffer_load_b128(rw1, ((lane >> 3) * 3456 + (lane & 7) * 16) | oob_if(a >= 27), (80 * w + 8 * i) * 3456 + a * 128, 0);
        }
        if (w == 0) ldsw<unsigned>(L_ACNT + (lane & 7) * 4, 0u);
        STAMP(10);
        WAIT_V(11);
        STAMP(11);
        {
            const int row = 8 * w + (lane >> 3), sw = (2 * w) ^ (((row >> 1) & 1) << 3);
            u4v v[10];
#pragma unroll
            for (int i = 0; i < 10; ++i) v[i] = ldsr<u4v>(L_AX + row * 1280 + (((lane & 7) + 8 * i) ^ sw) * 16);
            SB();
            float s1[2] = {0.f, 0.f}, s2[2] = {0.f, 0.f};
#pragma unroll
            for (int i = 0; i < 10; ++i)
#pragma unroll
                for (int j = 0; j < 4; ++j) { s1[j & 1] = dot2(v[i][j], 0x3f803f80u, s1[j & 1]); s2[j & 1] = dot2(v[i][j], v[i][j], s2[j & 1]); }
            const f2v sv = ln_stats(sum8(s1[0] + s1[1]), sum8(s2[0] + s2[1]));
            ldsw<f2v>(L_AST + row * 8, sv);
            __builtin_amdgcn_raw_buffer_store_b64(__builtin_bit_cast(u2v, sv), wsr, (WS_STATS + (a * 64 + row) * 8) | oob_if(lane & 7), 0, 16);
        }
        BAR_L();
        STAMP(14);
        {
            const int o = lane & 7, cq = lane >> 3;
            f2v rs2[4], nm2[4];
#pragma unroll
            for (int i = 0; i < 4; ++i) { const f4v q = ldsr<f4v>(L_AST + o * 64 + i * 16); rs2[i] = f2v{q[0], q[2]}; nm2[i] = f2v{-q[1], -q[3]}; }
            u2v v[3][8];
#pragma unroll
            for (int k = 0; k < 3; ++k)
#pragma unroll
                for (int t = 0; t < 8; ++t) v[k][t] = ldsr<u2v>(L_AX + (8 * o + t) * 1280 + ((4 * min(w + 8 * k, 19) + (cq >> 1)) ^ ((2 * o) ^ (((t >> 1) & 1) << 3))) * 16 + (cq & 1) * 8);
            SB();
#pragma unroll
            for (int k = 0; k < 3; ++k) {
                if (k < 2 || w < 4) {
#pragma unroll
                    for (int e = 0; e < 4; ++e) {
                        u4v ov;
#pragma unroll
                        for (int p = 0; p < 4; ++p) {
                            const unsigned va = v[k][2 * p][e >> 1], vb = v[k][2 * p + 1][e >> 1];
                            const f2v n2 = __builtin_elementwise_fma((e & 1) ? f2v{bfhi(va), bfhi(vb)} : f2v{bflo(va), bflo(vb)}, rs2[p], nm2[p]);
                            ov[p] = pk2(n2[0], n2[1]);
                        }
                        __builtin_amdgcn_raw_buffer_store_b128(ov, wsr, WS_XT + (32 * (w + 8 * k) + 4 * cq + e) * 4096 + a * 128 + o * 16, 0, 16);
                        SB();
                    }
                }
                if (k == 0) {
                    WAIT_V(4);
                    tag = mk64(__builtin_amdgcn_readfirstlane((unsigned)e0), __builtin_amdgcn_readfirstlane((unsigned)(e0 >> 32))) + 1ull;
                    arrive_publish(L_ACNT + 8, ws, WS_FS + a * 128, tag);
                }
            }
        }
        STAMP(15);
        {
            unsigned selo = 0x00003f80u, sehi = 0x3f800000u;
            asm volatile("" : "+v"(selo), "+v"(sehi));
            float sm[8] = {0.f, 0.f, 0.f, 0.f, 0.f, 0.f, 0.f, 0.f};
#pragma unroll
            for (int i = 0; i < 10; ++i)
#pragma unroll
                for (int j = 0; j < 4; ++j) { sm[2 * j] = dot2(cv[i][j], selo, sm[2 * j]); sm[2 * j + 1] = dot2(cv[i][j], sehi, sm[2 * j + 1]); }
#pragma unroll
            for (int j = 0; j < 8; ++j) sm[j] = dpp_add<0x128>(sm[j]);
            ldsw<f4v>(L_ACS + (4 * w + g) * 256 + (lane & 7) * 32 + (lane & 8) * 2, (lane & 8) ? f4v{sm[4], sm[5], sm[6], sm[7]} : f4v{sm[0], sm[1], sm[2], sm[3]});
        }
        BAR_L();
        if (w != 7 || a >= 27) {
            WAIT_V(0);
            arrive_publish(L_ACNT + 12, ws, WS_FA + a * 128, tag);
        } else {
            const float bf = bf2f(b1[64 * a + lane]);
            float p[32], sum = 0.f;
#pragma unroll
            for (int i = 0; i < 32; ++i) p[i] = ldsr<float>(L_ACS + i * 256 + lane * 4);
#pragma unroll
            for (int i = 0; i < 32; i += 4) sum += (p[i] + p[i + 1]) + (p[i + 2] + p[i + 3]);
            __builtin_amdgcn_raw_buffer_store_b32(__builtin_bit_cast(unsigned, sum), wsr, WS_CS + (64 * a + lane) * 4, 0, 16);
            __builtin_amdgcn_raw_buffer_store_b32(__builtin_bit_cast(unsigned, bf), wsr, WS_B1 + (64 * a + lane) * 4, 0, 16);
            WAIT_V(0);
            raise_flag(ws, WS_FC + a * 128, tag);
            arrive_publish(L_ACNT + 12, ws, WS_FA + a * 128, tag);
        }
        STAMP(2);
        if (w < 2) wait_flags(ws, WS_FW + (2 * min(54 * w + lane, 107) + (cb2 >> 2)) * 128, tag);
        BAR_L();
        STAMP(3);
        stage_x();
#pragma unroll
        for (int n = 0; n < 30; ++n) loadb(n);
        __builtin_amdgcn_s_waitcnt(0x0F70);
    }

    asm volatile("" : "+s"(rot2));
    if (w == 0) wait_flags(ws, WS_FH + ((rb2 >> 1) * 14 + min(lane, 13)) * 128, tag);
    BAR_L();
    STAMP(6);
    u4v ha[7][4];
    auto loada = [&](int i, int t) { ha[i][t] = __builtin_amdgcn_raw_buffer_load_b128(wsr, lane * 16, WS_HA + ((rb2 * 54 + sidx(i)) * 4 + t) * 1024, 16); };
#pragma unroll
    for (int i = 0; i < 8; ++i) loada(i >> 2, i & 3);
    if (is_gemm) {
        WAIT_V(8);
        arrive_publish(L_CNT + 4, ws, WS_FY + bid * 128, tag);
    }
    const int c0 = 80 * cb2;
    int lz = lane;
    asm volatile("" : "+v"(lz));
    const int cA = c0 + 10 * w, cB = cA + 9;
    const int own0 = max((cA * 0xaaab) >> 17, (cA - 192) >> 1), own1 = max((cB * 0xaaab) >> 17, (cB - 192) >> 1);
    u64 ypoll = tag;
    u4v y0, y1;
    f4v acc2[4][5];
#pragma unroll
    for (int i = 0; i < 20; ++i) acc2[i / 5][i % 5] = f4v{};
#pragma unroll
    for (int i = 0; i < 5; ++i) {
#pragma unroll
        for (int t = 0; t < 4; ++t) {
            SB();
#pragma unroll
            for (int c = 0; c < 5; ++c) acc2[t][c] = __builtin_amdgcn_mfma_f32_16x16x32_bf16(asbf(ha[i][t]), asbf(bw[5 * i + c]), acc2[t][c], 0, 0, 0);
            SB();
            if (i > 0) loada(i + 2, t);
            else if (t == 3) { loada(2, 0); loada(2, 1); loada(2, 2); loada(2, 3); }
            if (i == 1 || (i == 2 && t == 0)) loadb(30 + 4 * (i - 1) + t);
            if (i == 3 && t == 0) ypoll = ld_flag(ws, WS_FY + min(own0 + lz, own1) * 128);
            if (i == 4 && t == 3) {
                if (!__all(ypoll == tag)) wait_flags(ws, WS_FY + min(own0 + lz, own1) * 128, tag);
                y0 = __builtin_amdgcn_raw_buffer_load_b128(wsr, (lz >> 3) * 4096 + (lz & 7) * 16, WS_YT + cA * 4096 + rb2 * 128, 16);
                y1 = __builtin_amdgcn_raw_buffer_load_b128(wsr, (unsigned)((8 + (lz >> 3)) * 4096 + (lz & 7) * 16) | oob_if(lz >= 16), WS_YT + cA * 4096 + rb2 * 128, 16);
            }
        }
    }
    STAMP(7);
    auto dump = [&](int t) {
        unsigned p[10];
#pragma unroll
        for (int c = 0; c < 5; ++c) { p[2 * c] = pk2(acc2[t][c][0], acc2[t][c][1]); p[2 * c + 1] = pk2(acc2[t][c][2], acc2[t][c][3]); }
        addtid10(L_RED + w * RED_W + t * 5 * RED_T, p);
    };
#pragma unroll
    for (int t = 0; t < 4; ++t) {
        SB();
#pragma unroll
        for (int c = 0; c < 5; ++c) acc2[t][c] = __builtin_amdgcn_mfma_f32_16x16x32_bf16(asbf(ha[5][t]), asbf(bw[25 + c]), acc2[t][c], 0, 0, 0);
        SB();
        if (t > 0) dump(t - 1);
        SB();
#pragma unroll
        for (int c = 0; c < 5; ++c) acc2[t][c] = __builtin_amdgcn_mfma_f32_16x16x32_bf16(asbf(ha[6][t]), asbf(bw[30 + c]), acc2[t][c], 0, 0, 0);
    }
    SB();
    ldsw<u4v>(L_YS + (10 * w + (lz >> 3)) * YS_ROW + (lz & 7) * 16, y0);
    ldsw<u4v>(L_YS + min(10 * w + 8 + (lz >> 3) + (lz >= 16 ? 80 : 0), 80) * YS_ROW + (lz & 7) * 16, y1);
    dump(3);
    BAR_L();
    STAMP(8);
    {
        unsigned selo = 0x00003f80u, sehi = 0x3f800000u;
        asm volatile("" : "+v"(selo), "+v"(sehi));
#pragma unroll
        for (int k = 0; k < 3; ++k) {
            if (k < 2 || w < 4) {
                const int id = w * 64 + lz + 512 * k, tp = id / 40, c2 = id - tp * 40;
                const unsigned ra = L_RED + ((tp >> 3) * 5 + (c2 >> 3)) * RED_T + (tp & 1) * RED_ROW + ((tp >> 1) & 3) * 64 + (c2 & 7) * 8;
                u2v p[8];
#pragma unroll
                for (int j = 0; j < 8; ++j) p[j] = ldsr<u2v>(ra + j * RED_W);
                const unsigned ya = ldsr<unsigned>(L_YS + 2 * c2 * YS_ROW + tp * 4), yb = ldsr<unsigned>(L_YS + (2 * c2 + 1) * YS_ROW + tp * 4);
                const unsigned xa = ldsr<unsigned>(L_XS + tp * 320 + c2 * 4), xb = ldsr<unsigned>(L_XS + tp * 320 + 160 + c2 * 4), bv = ldsr<unsigned>(L_XS + 10240 + c2 * 4);
                float o00 = bflo(ya) + bflo(xa) + bflo(bv), o01 = bflo(yb) + bfhi(xa) + bfhi(bv);
                float o10 = bfhi(ya) + bflo(xb) + bflo(bv), o11 = bfhi(yb) + bfhi(xb) + bfhi(bv);
#pragma unroll
                for (int j = 0; j < 8; ++j) {
                    o00 = dot2(p[j][0], selo, o00); o10 = dot2(p[j][0], sehi, o10);
                    o01 = dot2(p[j][1], selo, o01); o11 = dot2(p[j][1], sehi, o11);
                }
                GP(unsigned) op = (GP(unsigned))(out + (rb2 * 64 + 2 * tp) * 640 + c0 + 2 * c2);
                op[0] = pk2(o00, o01);
                op[320] = pk2(o10, o11);
            }
        }
    }
    if (bid == 0 && w == 5) {
#pragma unroll
        for (int i = 0; i < 4; ++i) wait_flags(ws, WS_FH + (i * 64 + lz) * 128, tag);
        raise_flag(ws, WS_E, tag);
    }
}

extern "C" void kernel_launch(void* const* d_in, const int* in_sizes, int n_in, void* d_out, int out_size, void* d_ws, size_t ws_size, hipStream_t stream) {
    (void)in_sizes; (void)n_in; (void)out_size; (void)ws_size;
    (void)hipFuncSetAttribute((const void*)s4ffn_fused, hipFuncAttributeMaxDynamicSharedMemorySize, LDS_BYTES);
    s4ffn_fused<<<256, 512, LDS_BYTES, stream>>>((const u16*)d_in[0], (const u16*)d_in[1], (const u16*)d_in[2], (const u16*)d_in[3], (const u16*)d_in[4],
                                                 (const u16*)d_in[5], (const u16*)d_in[6], (const u16*)d_in[7], (const u16*)d_in[8], (const u16*)d_in[10],
                                                 (const u16*)d_in[11], (const u16*)d_in[12], (const u16*)d_in[13], (u16*)d_out, (char*)d_ws);
}
```

```cpp
#define STAMPS
#include <hip/hip_runtime.h>
#include <stdint.h>
#define PUB_RHO 4
#define CSUM_ON 1
#define CODE_CHUNKS 4
#define CODE_BYTES 28672u
#define ARR 0

typedef unsigned short u16;
typedef unsigned long long u64;
typedef short s4v __attribute__((ext_vector_type(4)));
typedef __bf16 bf8v __attribute__((ext_vector_type(8)));
typedef __bf16 bf2v __attribute__((ext_vector_type(2)));
typedef float f2v __attribute__((ext_vector_type(2)));
typedef float f4v __attribute__((ext_vector_type(4)));
typedef float f16v __attribute__((ext_vector_type(16)));
typedef unsigned u2v __attribute__((ext_vector_type(2)));
typedef unsigned short h2v __attribute__((ext_vector_type(2)));
typedef unsigned u4v __attribute__((ext_vector_type(4)));
typedef __attribute__((address_space(1))) u64 gu64;
#define LDSP(T) __attribute__((address_space(3))) T*
#define DI __device__ __forceinline__
#define RLX_AGENT __ATOMIC_RELAXED, __HIP_MEMORY_SCOPE_AGENT

constexpr unsigned WS_E = 0;
constexpr unsigned WS_FH = 4096;
constexpr unsigned WS_FA = 32768;
constexpr unsigned WS_FW = 81920;
constexpr unsigned WS_FS = 40960;
constexpr unsigned WS_FC = 45056;
constexpr unsigned WS_FY = 49152;
constexpr unsigned WS_STATS = 131072;
constexpr unsigned WS_CS = 196608, WS_B1 = 204800;
constexpr unsigned WS_STAMP = 262144;
constexpr unsigned WS_XT = 1u << 20;
constexpr unsigned WS_W2T = 4u << 20;
constexpr unsigned WS_YT = 8u << 20;
constexpr unsigned WS_HA = 12u << 20;
constexpr unsigned WS_BYTES = 20u << 20;
constexpr unsigned SLOT = 32768;
constexpr unsigned L_KC = 98304, KC_CH = 16640, KC_CP = 4160;
constexpr unsigned L_UA = 148224, UA_CH = 4576, UA_PAD = 480;
constexpr unsigned L_MISC = 162368, L_CB = L_MISC, L_CNT = L_MISC + 1024;
constexpr unsigned L_TBL = 65536, TBL_ROW = 544, TBL_CH = 53 * TBL_ROW;
constexpr unsigned L_PRM = 152064, L_TT = 158464, TT_CH = 1296, L_W2U = L_PRM;
constexpr unsigned L_SCR = L_CB;
constexpr unsigned L_XS = 0, L_RED = 16384, RED_ROW = 256, RED_T = 576, RED_W = 20 * RED_T, L_YS = L_RED + 8 * RED_W, YS_ROW = 144;
constexpr unsigned L_AX = 0, L_AST = 81920, L_ACNT = L_AST + 512, L_ACS = L_AST + 1024;
static_assert(L_TT + 3 * TT_CH <= L_MISC && L_CNT + 64 <= 163840 && L_W2U + 10240 <= L_MISC, "LDS map");
constexpr unsigned LDS_BYTES = 163840;
constexpr int SPIN_LIMIT = 400000;

constexpr int NSTAMP = 16;
#define STAMP(i) do { st[i] = __builtin_amdgcn_s_memrealtime(); } while (0)

DI float bf2f(unsigned v) { return __uint_as_float(v << 16); }
DI float bflo(unsigned v) { return __uint_as_float(v << 16); }
DI float bfhi(unsigned v) { return __uint_as_float(v & 0xffff0000u); }
DI unsigned pk2(float a, float b) { bf2v v; v[0] = (__bf16)a; v[1] = (__bf16)b; return __builtin_bit_cast(unsigned, v); }
DI float rbf(float a) { return bflo(pk2(a, a)); }
template <typename P> DI u4v mkrsrc(P p, unsigned bytes) { u64 a = (u64)p; u4v r; r[0] = (unsigned)a; r[1] = (unsigned)(a >> 32); r[2] = bytes; r[3] = 0x00020000u; return r; }
DI void dma16(u4v rs, unsigned voff, unsigned soff, unsigned lds) {
    asm volatile("s_mov_b32 m0, %3\n\ts_nop 0\n\tbuffer_load_dwordx4 %0, %1, %2 offen lds" ::"v"(voff), "s"(rs), "s"(soff), "s"(lds) : "memory");
}
DI void dma16g(const void* p, unsigned lds) {
    asm volatile("s_mov_b32 m0, %1\n\ts_nop 0\n\tglobal_load_lds_dwordx4 %0, off" ::"v"(p), "s"(lds) : "memory");
}
DI void dma16g_lanes3(const void* p, unsigned lds) {
    u64 ex;
    asm volatile("s_mov_b64 %0, exec\n\ts_mov_b64 exec, 7\n\ts_mov_b32 m0, %2\n\ts_nop 0\n\tglobal_load_lds_dwordx4 %1, off\n\ts_mov_b64 exec, %0" : "=&s"(ex) : "v"(p), "s"(lds) : "memory");
}
DI void dma16_lanes16(u4v rs, unsigned voff, unsigned soff, unsigned lds) {
    u64 ex;
    asm volatile("s_mov_b64 %0, exec\n\ts_mov_b64 exec, 0xffff\n\ts_mov_b32 m0, %4\n\ts_nop 0\n\tbuffer_load_dwordx4 %1, %2, %3 offen lds\n\ts_mov_b64 exec, %0"
                 : "=&s"(ex) : "v"(voff), "s"(rs), "s"(soff), "s"(lds) : "memory");
}
DI void touch_code(unsigned chunk, int lane, unsigned lds) {
    u64 pc;
    asm volatile("s_getpc_b64 %0" : "=s"(pc));
    const u64 p = (pc & ~127ull) + min(chunk * 8192u + lane * 128u, CODE_BYTES - 128u);
    asm volatile("s_mov_b32 m0, %1\n\ts_nop 0\n\tglobal_load_lds_dword %0, off" ::"v"(p), "s"(lds) : "memory");
}
#define SB() __builtin_amdgcn_sched_barrier(0)
#define WAIT_BAR(n) asm volatile("s_waitcnt vmcnt(" #n ") lgkmcnt(0)\n\ts_barrier" ::: "memory")
#define BAR_L() asm volatile("s_waitcnt lgkmcnt(0)\n\ts_barrier" ::: "memory")
#define WAIT_V(n) asm volatile("s_waitcnt vmcnt(" #n ")" ::: "memory")
template <typename T> DI T ldsr(unsigned a) { return *(LDSP(T))a; }
template <typename T> DI void ldsw(unsigned a, T v) { *(LDSP(T))a = v; }
DI void stw(unsigned a, unsigned v, int off) { asm volatile("ds_write_b32 %0, %1 offset:%2" ::"v"(a), "v"(v), "i"(off) : "memory"); }
DI s4v trr(unsigned a) { return __builtin_amdgcn_ds_read_tr16_b64_v4i16((LDSP(s4v))a); }
DI bf8v cat(s4v a, s4v b) { return __builtin_bit_cast(bf8v, __builtin_shufflevector(a, b, 0, 1, 2, 3, 4, 5, 6, 7)); }
DI bf8v asbf(u4v a) { return __builtin_bit_cast(bf8v, a); }
template <int CTRL> DI float dpp_add(float v) { return v + __builtin_bit_cast(float, __builtin_amdgcn_update_dpp(0, __builtin_bit_cast(int, v), CTRL, 0xf, 0xf, true)); }
DI float sum8(float v) { return dpp_add<0x141>(dpp_add<0x4e>(dpp_add<0xb1>(v))); }
DI float dot2(unsigned a, unsigned b, float c) { return __builtin_amdgcn_fdot2_f32_bf16(__builtin_bit_cast(bf2v, a), __builtin_bit_cast(bf2v, b), c, false); }
DI u64 ld_flag(const char* ws, unsigned off) { return __hip_atomic_load((gu64*)(ws + off), RLX_AGENT); }
DI void wait_flags(const char* ws, unsigned off, u64 tag) {
#pragma nounroll
    for (int it = 0; it < SPIN_LIMIT; ++it) {
        if (__all(ld_flag(ws, off) == tag)) break;
        __builtin_amdgcn_s_sleep(1);
    }
    asm volatile("" ::: "memory");
}
DI unsigned lds_count(unsigned cnt_lds) {
    unsigned old, one = 1u;
    u64 ex;
    asm volatile("s_mov_b64 %1, exec\n\ts_mov_b64 exec, 1\n\ts_waitcnt lgkmcnt(0)\n\tds_add_rtn_u32 %0, %2, %3\n\ts_waitcnt lgkmcnt(0)\n\ts_mov_b64 exec, %1"
                 : "=&v"(old), "=&s"(ex) : "v"(cnt_lds), "v"(one) : "memory");
    return __builtin_amdgcn_readfirstlane(old);
}
DI void raise_flag(char* ws, unsigned flag_off, u64 tag) { __hip_atomic_store((gu64*)(ws + flag_off), tag, RLX_AGENT); }
DI void arrive_publish(unsigned cnt_lds, char* ws, unsigned flag_off, u64 tag, unsigned last = 7u) {
    if (lds_count(cnt_lds) == last) raise_flag(ws, flag_off, tag);
}
DI void lds_wait(unsigned cnt_lds, unsigned n) {
#pragma nounroll
    for (int it = 0; it < SPIN_LIMIT; ++it) {
        if (__builtin_amdgcn_readfirstlane(*(volatile LDSP(unsigned))cnt_lds) >= n) break;
        __builtin_amdgcn_s_sleep(1);
    }
    asm volatile("" ::: "memory");
}
DI unsigned oob_if(bool c) { return c ? 0x80000000u : 0u; }
DI f2v ln_stats(float s1, float s2) {
    const float mean = s1 * (1.f / 640.f), var = fmaxf(__builtin_fmaf(-mean, mean, s2 * (1.f / 640.f)), 0.f);
    const float rs = __builtin_amdgcn_rcpf(rbf(__builtin_amdgcn_sqrtf(rbf(rbf(var) + 1e-5f))));
    return f2v{rs, rbf(mean) * rs};
}
DI f2v cmul_a(f2v a, f2v b) { f2v t; asm("v_pk_mul_f32 %0, %1, %2 op_sel_hi:[0,1]" : "=v"(t) : "v"(a), "v"(b)); return t; }
DI f2v cmul_b(f2v a, f2v b, f2v t) {
    f2v o;
    asm("v_pk_fma_f32 %0, %1, %2, %3 op_sel:[1,1,0] op_sel_hi:[1,0,1] neg_lo:[0,1,0]" : "=v"(o) : "v"(a), "v"(b), "v"(t));
    return o;
}
DI f2v cmul(f2v a, f2v b) { return cmul_b(a, b, cmul_a(a, b)); }
DI f2v cexp_p(float are, float aim, float p) {
    const float m = __builtin_amdgcn_exp2f(are * p), an = __builtin_amdgcn_fractf(aim * p);
    return f2v{m * __builtin_amdgcn_cosf(an), m * __builtin_amdgcn_sinf(an)};
}

DI void addtid10(unsigned m0v, const unsigned (&p)[10]) {
    asm volatile("s_mov_b32 m0, %10\n\ts_nop 0\n\t"
                 "ds_write_addtid_b32 %0 offset:%11\n\tds_write_addtid_b32 %1 offset:%12\n\tds_write_addtid_b32 %2 offset:%13\n\tds_write_addtid_b32 %3 offset:%14\n\t"
                 "ds_write_addtid_b32 %4 offset:%15\n\tds_write_addtid_b32 %5 offset:%16\n\tds_write_addtid_b32 %6 offset:%17\n\tds_write_addtid_b32 %7 offset:%18\n\t"
                 "ds_write_addtid_b32 %8 offset:%19\n\tds_write_addtid_b32 %9 offset:%20"
                 ::"v"(p[0]), "v"(p[1]), "v"(p[2]), "v"(p[3]), "v"(p[4]), "v"(p[5]), "v"(p[6]), "v"(p[7]), "v"(p[8]), "v"(p[9]), "s"(m0v),
                   "i"(0), "i"(RED_ROW), "i"(RED_T), "i"(RED_T + RED_ROW), "i"(2 * RED_T), "i"(2 * RED_T + RED_ROW), "i"(3 * RED_T), "i"(3 * RED_T + RED_ROW),
                   "i"(4 * RED_T), "i"(4 * RED_T + RED_ROW) : "memory");
}

#define GP(T) __attribute__((address_space(1))) T*
typedef unsigned u8v __attribute__((ext_vector_type(8)));
DI u64 mk64(unsigned lo, unsigned hi) { return ((u64)hi << 32) | lo; }
__global__ __launch_bounds__(512, 2) void s4ffn_fused(const u16*, const u16*, const u16*, const u16*, const u16*, const u16*, const u16*, const u16*, const u16*,
                                                      const u16*, const u16*, const u16*, const u16*, u16*, char*) {
    const int tid = threadIdx.x, lane = tid & 63, w = __builtin_amdgcn_readfirstlane(tid >> 6);
    const int xcd = blockIdx.x & 7, r = ((blockIdx.x >> 3) + 28) & 31, bid = r * 8 + xcd;
    const bool is_gemm = r < 28;
    u64 st[NSTAMP];
    for (int i = 0; i < NSTAMP; ++i) st[i] = 0;
    u64 cyc0 = __builtin_amdgcn_s_memtime();
    STAMP(0);
    if (w < CODE_CHUNKS && ((r + w) & 3) == 0) touch_code(w, lane, 163840 - 256);
    u64 a_x, a_prm;
    u8v a_m;
    u4v a_t;
    asm volatile("s_load_dwordx2 %0, %4, 0x0\n\ts_load_dwordx2 %1, %4, %5 offset:0x8\n\ts_load_dwordx8 %2, %4, 0x48\n\ts_load_dwordx4 %3, %4, 0x68\n\ts_waitcnt lgkmcnt(0)"
                 : "=&s"(a_x), "=&s"(a_prm), "=&s"(a_m), "=&s"(a_t) : "s"(__builtin_amdgcn_kernarg_segment_ptr()), "s"(8 * w));
    const GP(u16) x = (const GP(u16))a_x;
    const char* prm = (const char*)a_prm;
    const GP(u16) w1 = (const GP(u16))mk64(a_m[0], a_m[1]);
    const GP(u16) b1 = (const GP(u16))mk64(a_m[2], a_m[3]);
    const GP(u16) w2 = (const GP(u16))mk64(a_m[4], a_m[5]);
    const GP(u16) b2 = (const GP(u16))mk64(a_m[6], a_m[7]);
    GP(u16) out = (GP(u16))mk64(a_t[0], a_t[1]);
    char* ws = (char*)mk64(a_t[2], a_t[3]);
    u64 tag;
    const auto wsr = __builtin_amdgcn_make_buffer_rsrc((void*)ws, 0, WS_BYTES, 0x00020000);
    const int nch = bid < 192 ? 3 : 2, ch0 = 3 * bid - max(bid - 192, 0);
    const int g = lane >> 4, l16 = lane & 15;
    const int rb2 = xcd * 4 + (r & 3), cb2 = r >> 2;
    const bool has7 = w < 6;
    u4v bw[35];
    int rot2 = 7 * cb2 + 14 * (r & 3) + w;
    rot2 = rot2 >= 54 ? rot2 - 54 : rot2;
    auto sidx = [&](int i) { const int s = rot2 + 8 * i; return s >= 54 ? s - 54 : s; };
    auto loadb = [&](int n) {
        bw[n] = __builtin_amdgcn_raw_buffer_load_b128(wsr, lane * 16 | oob_if(n >= 30 && !has7), WS_W2T + ((cb2 * 54 + sidx(n / 5)) * 5 + n % 5) * 1024, 16);
    };

    auto stage_x = [&]() {
        const u4v rx = mkrsrc(x, 2048 * 1280);
        const int n1 = 64 + (lane & 15);
        dma16(rx, (lane / 10) * 1280 + (lane % 10) * 16, (rb2 * 64 + 8 * w) * 1280 + cb2 * 160, L_XS + w * 1280);
        dma16_lanes16(rx, (n1 / 10) * 1280 + (n1 % 10) * 16, (rb2 * 64 + 8 * w) * 1280 + cb2 * 160, L_XS + w * 1280 + 1024);
        if (w == 0) dma16_lanes16(mkrsrc(b2, 1280), min(lane, 9) * 16, cb2 * 160, L_XS + 10240);
    };
    if (__builtin_expect(is_gemm, 1)) {
        if (w < 6) dma16g(prm + (ch0 + min(g, nch - 1)) * 256 + l16 * 16, L_PRM + w * 1024);
        else dma16g_lanes3(lane == 2 ? (const char*)ws + WS_E : prm + min(((ch0 * 2) & ~15) + lane * 16, 1264), L_SCR + (w - 6) * 64);
        SB();
        const int gi = r & 3, gj = r >> 2, rb = (xcd >> 1) * 4 + gi, cb = (xcd & 1) * 7 + gj;
        const int fw = w & 3, tw = w >> 2;
        const u4v rx = mkrsrc(x, 2048 * 1280), rw = mkrsrc(w1, 640 * 3456);
        unsigned vx[2];
#pragma unroll
        for (int u = 0; u < 2; ++u) vx[u] = (rb * 128 + 16 * w + 8 * u + (lane >> 3)) * 1280 + (((lane & 7) ^ (4 * u + g)) << 4);
        const int wc = l16 ^ (2 * g) ^ ((w & 1) << 3);
        const unsigned vw = (unsigned)((8 * w + g) * 3456 + cb * 256 + wc * 16) | (cb == 13 ? (unsigned)(wc & 8) << 28 : 0u);
        int rot = gj + 3 * gi + xcd;
        rot = rot >= 20 ? rot - 20 : rot >= 10 ? rot - 10 : rot;
        auto stage1w = [&](int t, int slot, int p, int dw) {
            int kt = t + rot; kt = kt >= 10 ? kt - 10 : kt;
            const unsigned lb = slot * SLOT + (w + dw) * 2048 + (p >> 1) * 1024;
            if (p & 1) dma16(rw, vw + dw * 8 * 3456, kt * (64 * 3456) + (p >> 1) * 4 * 3456, lb + 16384);
            else dma16(rx, vx[p >> 1] + dw * 16 * 1280, kt * 128, lb);
        };
        auto stage1 = [&](int t, int slot, int p) { stage1w(t, slot, p, 0); };
#pragma unroll
        for (int i = 0; i < 4; ++i) stage1(0, 0, i);
        u4v w2a, w2b;
        {
            const auto rw2 = __builtin_amdgcn_make_buffer_rsrc((void*)(u64)w2, 0, 1728 * 1280, 0x00020000);
            const unsigned so2 = ((bid >> 2) * 32 + ((bid >> 1) & 1) * 8 + (w >> 2) * 16 + (w & 3) * 2) * 1280 + (bid & 1) * 640;
            w2a = __builtin_amdgcn_raw_buffer_load_b128(rw2, lane * 16 + (lane >= 40 ? 640 : 0), so2, 0);
            w2b = __builtin_amdgcn_raw_buffer_load_b128(rw2, (1280 + 384 + lane * 16) | oob_if(lane >= 16), so2, 0);
        }
        SB();
        unsigned ix[12];
        {
            const int l0 = 8 * (tid & 255) - 3;
            const float lf = (float)l0;
            const unsigned kb = L_TT + tw * TT_CH, kc = kb - 2u * (0x4300 - 128);
#pragma unroll
            for (int i = 0; i < 6; ++i) {
                const unsigned c2 = pk2(lf + (float)(2 * i), lf + (float)(2 * i + 1));
                asm("v_mad_u32_u16 %0, %1, 2, %2" : "=v"(ix[2 * i]) : "v"(c2), "s"(kc));
                asm("v_mad_u32_u16 %0, %1, 2, %2 op_sel:[1,0,0,0]" : "=v"(ix[2 * i + 1]) : "v"(c2), "s"(kc));
            }
            if (fw == 0) {
#pragma unroll
                for (int i = 0; i < 11; ++i) {
                    const int l = l0 + i;
                    const unsigned lo = kb + 2u * min((unsigned)l, 641u), m = (unsigned)((l - 128) >> 31);
                    ix[i] = (lo & m) | (ix[i] & ~m);
                }
            }
#pragma unroll
            for (int i = 0; i < 11; ++i) asm volatile("" : "+v"(ix[i]));
        }
        if (w == 0) {
            ldsw<unsigned>(L_CNT + (lane & 7) * 4, 0u);
        }
        STAMP(10);
        WAIT_BAR(6);
        STAMP(11);
        const unsigned so = (ch0 * 2) & 15;
        { const u2v tv = ldsr<u2v>(L_SCR + 32); tag = mk64(__builtin_amdgcn_readfirstlane(tv[0]), __builtin_amdgcn_readfirstlane(tv[1])) + 1ull; }
        const unsigned dsk01 = __builtin_amdgcn_readfirstlane(ldsr<u16>(L_SCR + 64 + so) | ((unsigned)ldsr<u16>(L_SCR + 64 + so + 2) << 16));
        const unsigned dsk2 = __builtin_amdgcn_readfirstlane(ldsr<u16>(L_SCR + 64 + so + 4));
        {
            const int hc = w < 6 ? w : w - 2, c = hc >> 1, mode = (hc & 1) * 64 + lane;
            const float ldt = bf2f(ldsr<u16>(L_SCR + so + 2 * c));
            const unsigned pb = L_PRM + c * 256 + mode * 2;
            const float pa = bf2f(ldsr<u16>(pb)), pi = bf2f(ldsr<u16>(pb + 1024));
            const f2v Bc = {bf2f(ldsr<u16>(pb + 2048)), bf2f(ldsr<u16>(pb + 3072))}, Cc = {bf2f(ldsr<u16>(pb + 4096)), bf2f(ldsr<u16>(pb + 5120))};
            lds_count(L_CNT + 12);
            if (w >= 4) {
#pragma unroll
                for (int i = 0; i < 8; ++i) stage1w(1, 1, i & 3, i < 4 ? 0 : -4);
            }
            if (c < nch) {
                const float dt = rbf(__builtin_amdgcn_exp2f(ldt * 1.4426950408889634f));
                const float are = dt * pa * 1.4426950408889634f, aim = dt * pi * 0.15915494309189535f;
                const unsigned tb = L_TBL + c * TBL_CH + mode * 4;
                if (w < 6) {
                    const f2v z8 = cexp_p(are, aim, 8.f), z16 = cexp_p(are, aim, 16.f), z64 = cexp_p(are, aim, 64.f);
                    f2v e[8];
                    e[0] = f2v{1.f, 0.f}; e[1] = z8; e[2] = z16; e[3] = cmul(z16, z8); e[4] = cmul(z16, z16);
                    e[5] = cmul(e[3], z16); e[6] = cmul(e[4], z16); e[7] = cmul(e[4], e[3]);
#pragma unroll
                    for (int i = 0; i < 4; ++i) {
                        f2v t[8];
#pragma unroll
                        for (int j = 0; j < 8; ++j) {
                            if (i < 3) t[j] = cmul_a(e[j], z64);
                            stw(tb, pk2(e[j][0], e[j][1]), (21 + 8 * i + j) * TBL_ROW);
                        }
                        if (i < 3) {
#pragma unroll
                            for (int j = 0; j < 8; ++j) e[j] = cmul_b(e[j], z64, t[j]);
                        }
                    }
                }
                if (w < 4 || w >= 6) {
                    const f2v z1 = cexp_p(are, aim, 1.f), z2 = cexp_p(are, aim, 2.f), z4 = cexp_p(are, aim, 4.f);
                    const f2v z256 = cexp_p(are, aim, 256.f), z1024 = cexp_p(are, aim, 1024.f);
                    const f2v W = cmul(Cc, Bc * dt);
                    auto putx = [&](int row, f2v v) { stw(tb, pk2(v[0], -v[1]), row * TBL_ROW); };
                    const f2v x1 = cmul(W, z1), x2 = cmul(W, z2), x8 = cmul(W, z256), x16 = cmul(W, z1024);
                    const f2v x3 = cmul(x1, z2), x9 = cmul(x8, z2), x12 = cmul(x8, z256), x17 = cmul(x16, z256), x20 = cmul(x16, z1024);
                    const f2v x14 = cmul(x12, z256), x18 = cmul(x17, z256);
                    putx(0, W); putx(1, x1); putx(2, x2); putx(3, x3); putx(8, x8); putx(9, x9); putx(12, x12); putx(14, x14);
                    putx(16, x16); putx(17, x17); putx(18, x18); putx(20, x20);
                    putx(4, cmul(W, z4)); putx(5, cmul(x1, z4)); putx(6, cmul(x2, z4)); putx(7, cmul(x3, z4));
                    putx(10, cmul(x8, z4)); putx(11, cmul(x9, z4)); putx(13, cmul(x12, z4)); putx(15, cmul(x14, z4)); putx(19, cmul(x18, z256));
                }
            }
        }
        lds_wait(L_CNT + 12, 8u);
        ldsw<u4v>(L_W2U + w * 1280 + lane * 16, w2a);
        ldsw<u4v>(lane < 16 ? L_W2U + w * 1280 + 1024 + lane * 16 : L_CNT + 48, w2b);
        STAMP(13);
        BAR_L();
        if (w >= 6) {
            const unsigned tl = L_W2U + (4 * (g & 1) + (l16 >> 2)) * 640 + (lane >> 5) * 32 + (lane & 3) * 8 + (w - 6) * 320;
            bf8v f[5];
#pragma unroll
            for (int k = 0; k < 5; ++k) f[k] = cat(trr(tl + k * 64), trr(tl + k * 64 + 8 * 640));
            lds_count(L_CNT + 16);
#pragma unroll
            for (int k = 0; k < 5; ++k) {
                const int P = 20 * (bid & 1) + 10 * (w - 6) + 2 * k + (lane >> 5), cbb = (P * 52) >> 8, cfr = P - cbb * 5;
                __builtin_amdgcn_raw_buffer_store_b128(__builtin_bit_cast(u4v, f[k]), wsr,
                    (unsigned)(WS_W2T + (bid >> 2) * 5120 + ((bid >> 1) & 1) * 512 + (cbb * 270 + cfr) * 1024 + (lane & 31) * 16) | oob_if(bid >= 216), 0, 16);
            }
        }
        if (w < 2 * nch) {
            const int c = w >> 1, col = 16 * (w & 1) + l16;
            const unsigned ta = L_TBL + c * TBL_CH + l16 * TBL_ROW + g * 16;
            const unsigned te = ta + (21 + 16 * (w & 1)) * TBL_ROW;
            u4v fa[8], fb[8], fe[8];
#pragma unroll
            for (int s = 0; s < 8; ++s) { fe[s] = ldsr<u4v>(te + s * 64); fa[s] = ldsr<u4v>(ta + s * 64); fb[s] = ldsr<u4v>(ta + 16 * TBL_ROW + s * 64); }
            const unsigned tt = L_TT + c * TT_CH;
            const unsigned ta0 = tt + ((col << (3 - max(g - 1, 0))) + ((0x30200100u >> (8 * g)) & 0xffu) * (g == 1 ? 4u : 8u)) * 2;
            const unsigned ta1 = ta0 + (g < 3 ? 4 : 128);
            const unsigned tb0 = tt + (g == 0 ? 512 + col : 644) * 2, st1 = g == 0 ? 64 : 0;
            const unsigned tb1 = g == 1 && col < 2 ? tt + (640 + col) * 2 : tb0;
            SB();
            f4v k0 = {}, k1 = {};
#pragma unroll
            for (int s = 0; s < 8; ++s) {
                k0 = __builtin_amdgcn_mfma_f32_16x16x32_bf16(asbf(fa[s]), asbf(fe[s]), k0, 0, 0, 0);
                k1 = __builtin_amdgcn_mfma_f32_16x16x32_bf16(asbf(fb[s]), asbf(fe[s]), k1, 0, 0, 0);
            }
            lds_wait(L_CNT + 16, 2u);
            ldsw<unsigned>(ta0, pk2(k0[0], k0[1]));
            ldsw<unsigned>(ta1, pk2(k0[2], k0[3]));
            const unsigned q01 = pk2(k1[0], k1[1]), q23 = pk2(k1[2], k1[3]);
            ldsw<u16>(tb1, g == 1 && col == 1 ? (u16)0 : (u16)q01);
            ldsw<u16>(tb0 + st1, (u16)(q01 >> 16));
            ldsw<u16>(tb0 + 2 * st1, (u16)q23);
            ldsw<u16>(tb0 + 3 * st1, (u16)(q23 >> 16));
        }
        BAR_L();
        STAMP(14);
        {
            const bool two = w < 4 && nch == 3;
            const int t = tid & 255;
#pragma unroll
            for (int k = 0; k < 2; ++k) {
                if (k == 0 || two) {
                    unsigned pe[6], po[5];
#pragma unroll
                    for (int i = 0; i < 5; ++i) pe[i] = __builtin_bit_cast(unsigned, h2v{ldsr<u16>(ix[2 * i] + k * 2 * TT_CH), ldsr<u16>(ix[2 * i + 1] + k * 2 * TT_CH)});
                    pe[5] = ldsr<u16>(ix[10] + k * 2 * TT_CH);
                    if (k == 0) { stage1(2, 2, 0); stage1(2, 2, 1); stage1(2, 2, 2); stage1(2, 2, 3); }
#pragma unroll
                    for (int i = 0; i < 5; ++i) po[i] = __builtin_amdgcn_alignbit(pe[i + 1], pe[i], 16);
                    const unsigned cbase = L_KC + (tw + 2 * k) * KC_CH, d = cbase + 32 + t * 16;
                    ldsw<u4v>(d, u4v{po[1], po[2], po[3], po[4]});
                    ldsw<u4v>(d + KC_CP, u4v{pe[1], pe[2], pe[3], pe[4]});
                    ldsw<u4v>(d + 2 * KC_CP, u4v{po[0], po[1], po[2], po[3]});
                    ldsw<u4v>(d + 3 * KC_CP, u4v{pe[0], pe[1], pe[2], pe[3]});
                }
            }
            if (w == 7) ldsw<u4v>(L_KC + min(lane >> 1, 11) * KC_CP + (lane & 1) * 16, u4v{0u, 0u, 0u, 0u});
        }
        STAMP(1);
        f4v acc1[2][4];
#pragma unroll
        for (int i = 0; i < 8; ++i) acc1[i >> 2][i & 3] = f4v{};
        unsigned bx[2], aw[2];
        {
            const int q4 = l16 >> 2, p4 = lane & 3;
#pragma unroll
            for (int i = 0; i < 2; ++i) {
                bx[i] = (64 * tw + l16) * 128 + (((4 * i + g) ^ (l16 >> 1)) << 4);
                aw[i] = 16384 + (8 * g + q4) * 256 + (((2 * fw + i) ^ q4 ^ ((g & 1) << 2)) << 5) + p4 * 8;
            }
        }
        bf8v fa[2][2][2], fb[2][2][4];
        auto rd = [&](int buf, int i) {
            const int ks = i >> 1, h = i & 1;
            fa[buf][ks][h] = cat(trr(aw[h] + ks * 8192), trr(aw[h] + ks * 8192 + 1024));
            fb[buf][ks][2 * h] = asbf(ldsr<u4v>(bx[ks] + h * 4096));
            fb[buf][ks][2 * h + 1] = asbf(ldsr<u4v>(bx[ks] + h * 4096 + 2048));
        };
        unsigned sb = 0;
        int kt = rot + 2; kt = kt >= 10 ? kt - 10 : kt;
        auto tile = [&](int cur, bool more, bool stage, auto&& side) {
            const unsigned d = sb == 2 * SLOT ? (unsigned)(-2 * (int)SLOT) : SLOT, lb = sb + w * 2048;
            kt = kt == 9 ? 0 : kt + 1;
            sb += d; aw[0] += d; aw[1] += d; bx[0] += d; bx[1] += d;
#pragma unroll
            for (int i = 0; i < 4; ++i) {
                SB();
                if (more) rd(cur ^ 1, i);
                SB();
#pragma unroll
                for (int j = 0; j < 4; ++j) acc1[i & 1][j] = __builtin_amdgcn_mfma_f32_16x16x32_bf16(fa[cur][i >> 1][i & 1], fb[cur][i >> 1][j], acc1[i & 1][j], 0, 0, 0);
                SB();
                if (stage) {
                    if (i & 1) dma16(rw, vw, kt * (64 * 3456) + (i >> 1) * 4 * 3456, lb + (i >> 1) * 1024 + 16384);
                    else dma16(rx, vx[i >> 1], kt * 128, lb + (i >> 1) * 1024);
                }
                side(i);
            }
            SB();
        };
        auto none = [&](int) {};
        if (w >= 6) {
            WAIT_V(4);
            arrive_publish(L_CNT + 8, ws, WS_FW + min(bid, 216) * 128, tag, 1u);
        }
        BAR_L();
        STAMP(15);
#pragma unroll
        for (int i = 0; i < 4; ++i) rd(0, i);
        u4v uld[2], cbv;
        f2v sv[4];
        u64 fpoll = 0;
        const unsigned f1a = WS_FS + 2 * rb * 128, f1b = WS_FC + 2 * cb * 128;
        const unsigned f1_off = min(((lane & 2) ? f1b : f1a) + (lane & 1) * 128, WS_FC + 26 * 128);
        const unsigned f2_off = w == 2 ? WS_FA + (lane & 31) * 128 : WS_FW + (2 * min(54 * w + lane, 107) + (cb2 >> 2)) * 128;
        WAIT_BAR(4); tile(0, true, true, none);
        WAIT_BAR(4); tile(1, true, true, none);
        WAIT_BAR(4); tile(0, true, true, [&](int i) { if (i == 0 && w < 2) fpoll = ld_flag(ws, f2_off); });
        if (w < 2 && !__all(fpoll == tag)) wait_flags(ws, f2_off, tag);
        WAIT_BAR(4);
        STAMP(12);
        tile(1, true, true, [&](int i) { if (i == 0) loadb(0); if (i == 2) loadb(1); });
        WAIT_BAR(6); tile(0, true, true, [&](int i) { if (i == 0) loadb(2); if (i == 2) loadb(3); });
        WAIT_BAR(6); tile(1, true, true, [&](int i) { if (i == 0) loadb(4); if (i == 2) loadb(5); });
        WAIT_BAR(6); tile(0, true, true, [&](int i) { if (i == 0) loadb(6); if (i == 2) loadb(7); });
        WAIT_BAR(6); tile(1, true, false, [&](int i) { if (i == 0 && w == 0) fpoll = ld_flag(ws, f1_off); loadb(8 + i); });
        if (w == 0 && !__all(fpoll == tag)) wait_flags(ws, f1_off, tag);
        WAIT_BAR(4);
        STAMP(3);
        tile(0, true, false, [&](int i) {
            sv[i] = __builtin_bit_cast(f2v, __builtin_amdgcn_raw_buffer_load_b64(wsr, l16 * 8, WS_STATS + (rb * 128 + 64 * tw + 16 * i) * 8, 16));
            if (i == 0 && w == 1) cbv = __builtin_amdgcn_raw_buffer_load_b128(wsr, (lane >> 5) * (WS_B1 - WS_CS) + (lane & 31) * 16, WS_CS + cb * 512, 16);
            if (i == 0 && w == 2) fpoll = ld_flag(ws, f2_off);
            loadb(12 + i);
        });
        tile(1, false, false, [&](int i) { loadb(16 + i); });
        if (w == 1) ldsw<u4v>(L_CB + lane * 16, cbv);
        if (w >= 2 && w < 5) ldsw<u4v>(L_UA + (w - 2) * UA_CH + min(lane, 29) * 16, u4v{0u, 0u, 0u, 0u});
        if (w == 2 && !__all(fpoll == tag)) wait_flags(ws, f2_off, tag);
        BAR_L();
        STAMP(2);
        stage_x();
        {
            f4v cs[2], bb[2];
#pragma unroll
            for (int u = 0; u < 2; ++u) { cs[u] = ldsr<f4v>(L_CB + (32 * fw + 16 * u + 4 * g) * 4); bb[u] = ldsr<f4v>(L_CB + 512 + (32 * fw + 16 * u + 4 * g) * 4); }
            const int s = 4 * cb + fw;
            const unsigned ho = (unsigned)(WS_HA + ((2 * rb + tw) * 54 + s) * 4096 + lane * 16) | oob_if(s >= 54);
            u4v hp[4];
#pragma unroll
            for (int ti = 0; ti < 4; ++ti) {
                const f2v rs2 = {sv[ti][0], sv[ti][0]}, nm2 = {-sv[ti][1], -sv[ti][1]};
#pragma unroll
                for (int i = 0; i < 4; ++i) {
                    const int slot = 4 * ti + i, u = i >> 1, e = 2 * (i & 1);
                    if (slot < 2) {
                        const int q = w + 8 * slot, c = q >> 2;
                        uld[slot] = __builtin_amdgcn_raw_buffer_load_b128(wsr, lane * 16 | oob_if(c >= nch), WS_XT + (ch0 + c) * 4096 + (q & 3) * 1024, 16);
                    }
                    if (slot >= 2 && slot < 12) loadb(18 + slot);
                    SB();
                    const f2v v = __builtin_elementwise_fma(rs2, f2v{acc1[u][ti][e], acc1[u][ti][e + 1]}, __builtin_elementwise_fma(nm2, f2v{cs[u][e], cs[u][e + 1]}, f2v{bb[u][e], bb[u][e + 1]}));
                    const f2v z = v * __builtin_elementwise_fma(v * v, f2v{-0.10294324f, -0.10294324f}, f2v{-2.30220820f, -2.30220820f});
                    const f2v d = f2v{__builtin_amdgcn_exp2f(z[0]), __builtin_amdgcn_exp2f(z[1])} + f2v{1.f, 1.f};
                    const f2v gl = v * f2v{__builtin_amdgcn_rcpf(d[0]), __builtin_amdgcn_rcpf(d[1])};
                    hp[ti][i] = pk2(gl[0], gl[1]);
                    asm volatile("" : "+v"(hp[ti][i]));
                }
            }
            SB();
#pragma unroll
            for (int ti = 0; ti < 4; ++ti)
                __builtin_amdgcn_raw_buffer_store_b128(hp[ti], wsr, ho + 1024 * ti, 0, 16);
        }
#pragma unroll
        for (int i = 0; i < 2; ++i) {
            const int q = w + 8 * i, c = q >> 2;
            if (q < 12) ldsw<u4v>(L_UA + c * UA_CH + UA_PAD + (q & 3) * 1024 + lane * 16, uld[i]);
        }
        STAMP(4);
        BAR_L();
        {
            const int typ = w < 3 ? 0 : w < 6 ? 1 : 2;
            const int c1 = w < 3 ? w : w < 6 ? w - 3 : 2 * (w - 6), c2 = w == 6 ? 1 : c1;
            const unsigned kl = L_KC + (l16 >> 2) * KC_CP + 56 - 16 * g + 8 * (lane & 3);
            const unsigned ul = L_UA + UA_PAD + 32 * l16 - 32 + 16 * g;
            unsigned ka[3], ua[3];
            ka[0] = kl + c1 * KC_CH;
            ka[1] = typ == 2 ? kl + c2 * KC_CH - 4 * 512 : ka[0];
            ka[2] = typ == 1 ? ka[0] - 6 * 512 : ka[1];
            ua[0] = ul + c1 * UA_CH - typ * 1024;
            ua[2] = ul + c2 * UA_CH;
            ua[1] = typ == 2 ? ua[2] : ua[0];
            bf8v uf[8], kf[8];
            auto rdk = [&](int sl) { const unsigned ad = ka[sl < 4 ? 0 : sl < 6 ? 1 : 2] + 512u * sl; kf[sl] = cat(trr(ad + 8u), trr(ad)); };
            auto rdu = [&](int j) { uf[j] = asbf(ldsr<u4v>(ua[j >= 4 ? 0 : j >= 2 ? 1 : 2] + 512u * j)); };
#pragma unroll
            for (int sl = 0; sl < 8; ++sl) { rdk(sl); rdu(7 - sl); }
            f4v A[3] = {}, B[5] = {};
            const unsigned fo = WS_FH + (rb * 14 + cb) * 128;
#pragma nounroll
            for (int rho = 0; rho < 7; ++rho) {
#pragma unroll
                for (int i = 0; i < 3; ++i) { ka[i] += 64u; ua[i] -= 64u; asm volatile("" : "+v"(ka[i]), "+v"(ua[i])); }
#pragma unroll
                for (int sl = 0; sl < 8; ++sl) {
                    SB();
                    const int ia = sl < 4 ? 0 : sl < 6 ? 1 : 2, ib = sl < 3 ? 0 : sl - 2;
                    A[ia] = __builtin_amdgcn_mfma_f32_16x16x32_bf16(kf[sl], uf[7 - sl], A[ia], 0, 0, 0);
                    if (sl < 7) B[ib] = __builtin_amdgcn_mfma_f32_16x16x32_bf16(kf[sl], uf[6 - sl], B[ib], 0, 0, 0);
                    SB();
                    rdk(sl); rdu(7 - sl);
                }
                SB();
                if (rho == PUB_RHO) {
                    WAIT_V(0);
                    arrive_publish(L_CNT, ws, fo, tag);
                }
            }
#pragma unroll
            for (int sl = 0; sl < 8; ++sl) {
                const int ia = sl < 4 ? 0 : sl < 6 ? 1 : 2, ib = sl < 3 ? 0 : sl - 2;
                A[ia] = __builtin_amdgcn_mfma_f32_16x16x32_bf16(kf[sl], uf[7 - sl], A[ia], 0, 0, 0);
                if (sl < 7) B[ib] = __builtin_amdgcn_mfma_f32_16x16x32_bf16(kf[sl], uf[6 - sl], B[ib], 0, 0, 0);
            }
            SB();
            f4v y[4];
            if (typ == 0) { y[0] = A[0] + A[1] + A[2]; y[1] = (B[0] + B[1]) + (B[2] + B[3]) + B[4]; y[2] = y[0]; y[3] = y[1]; }
            else if (typ == 1) { y[0] = A[0] + A[1]; y[1] = B[0] + B[1] + B[2]; y[2] = A[2]; y[3] = B[4]; }
            else { y[0] = A[0]; y[1] = B[0]; y[2] = A[1] + A[2]; y[3] = B[2] + B[3] + B[4]; }
#pragma unroll
            for (int i = 0; i < 4; ++i) {
                const int c = i < 2 ? c1 : c2, tau = (i < 2 ? 7 - 2 * typ : 2 * typ - 1) - (i & 1);
                const bool on = c < nch && (i < 2 || (typ != 0 && w != 7));
                const u2v uv = ldsr<u2v>(L_UA + c * UA_CH + UA_PAD + 512 * max(tau, 0) + 32 * l16 + 8 * g);
                const float dk = c == 0 ? bflo(dsk01) : c == 1 ? bfhi(dsk01) : bflo(dsk2);
                const u2v o = {pk2(y[i][0] + dk * bflo(uv[0]), y[i][1] + dk * bfhi(uv[0])), pk2(y[i][2] + dk * bflo(uv[1]), y[i][3] + dk * bfhi(uv[1]))};
                __builtin_amdgcn_raw_buffer_store_b64(o, wsr, 32 * l16 + 8 * g, (WS_YT + (ch0 + c) * 4096 + 512 * max(tau, 0)) | oob_if(!on), 16);
            }
        }
        STAMP(5);
    } else {
        const int a = (r - 28) * 8 + xcd;
        const u4v rx = mkrsrc(x, 2048 * 1280);
        {
            unsigned vo[5];
#pragma unroll
            for (int i = 0; i < 5; ++i) {
                const int n = i * 64 + lane, rl = n / 80, cp = n - rl * 80;
                vo[i] = (a * 64 + 8 * w + rl) * 1280 + ((cp ^ (2 * w) ^ ((rl >> 1) << 3)) << 4);
                dma16(rx, vo[i], 0, L_AX + (w * 10 + i) * 1024);
            }
#pragma unroll
            for (int i = 5; i < 10; ++i) dma16(rx, vo[i - 5], 4 * 1280, L_AX + (w * 10 + i) * 1024);
        }
        const u64 e0 = ld_flag(ws, WS_E);
        u4v cv[10];
        {
            const auto rw1 = __builtin_amdgcn_make_buffer_rsrc((void*)(u64)w1, 0, 640 * 3456, 0x00020000);
#pragma unroll
            for (int i = 0; i < 10; ++i) cv[i] = __builtin_amdgcn_raw_buffer_load_b128(rw1, ((lane >> 3) * 3456 + (lane & 7) * 16) | oob_if(a >= 27), (80 * w + 8 * i) * 3456 + a * 128, 0);
        }
        if (w == 0) ldsw<unsigned>(L_ACNT + (lane & 7) * 4, 0u);
        STAMP(10);
        WAIT_V(11);
        STAMP(11);
        {
            const int row = 8 * w + (lane >> 3), sw = (2 * w) ^ (((row >> 1) & 1) << 3);
            u4v v[10];
#pragma unroll
            for (int i = 0; i < 10; ++i) v[i] = ldsr<u4v>(L_AX + row * 1280 + (((lane & 7) + 8 * i) ^ sw) * 16);
            SB();
            float s1[2] = {0.f, 0.f}, s2[2] = {0.f, 0.f};
#pragma unroll
            for (int i = 0; i < 10; ++i)
#pragma unroll
                for (int j = 0; j < 4; ++j) { s1[j & 1] = dot2(v[i][j], 0x3f803f80u, s1[j & 1]); s2[j & 1] = dot2(v[i][j], v[i][j], s2[j & 1]); }
            const f2v sv = ln_stats(sum8(s1[0] + s1[1]), sum8(s2[0] + s2[1]));
            ldsw<f2v>(L_AST + row * 8, sv);
            __builtin_amdgcn_raw_buffer_store_b64(__builtin_bit_cast(u2v, sv), wsr, (WS_STATS + (a * 64 + row) * 8) | oob_if(lane & 7), 0, 16);
        }
        BAR_L();
        STAMP(14);
        {
            const int o = lane & 7, cq = lane >> 3;
            f2v rs2[4], nm2[4];
#pragma unroll
            for (int i = 0; i < 4; ++i) { const f4v q = ldsr<f4v>(L_AST + o * 64 + i * 16); rs2[i] = f2v{q[0], q[2]}; nm2[i] = f2v{-q[1], -q[3]}; }
            u2v v[3][8];
#pragma unroll
            for (int k = 0; k < 3; ++k)
#pragma unroll
                for (int t = 0; t < 8; ++t) v[k][t] = ldsr<u2v>(L_AX + (8 * o + t) * 1280 + ((4 * min(w + 8 * k, 19) + (cq >> 1)) ^ ((2 * o) ^ (((t >> 1) & 1) << 3))) * 16 + (cq & 1) * 8);
            SB();
#pragma unroll
            for (int k = 0; k < 3; ++k) {
                if (k < 2 || w < 4) {
#pragma unroll
                    for (int e = 0; e < 4; ++e) {
                        u4v ov;
#pragma unroll
                        for (int p = 0; p < 4; ++p) {
                            const unsigned va = v[k][2 * p][e >> 1], vb = v[k][2 * p + 1][e >> 1];
                            const f2v n2 = __builtin_elementwise_fma((e & 1) ? f2v{bfhi(va), bfhi(vb)} : f2v{bflo(va), bflo(vb)}, rs2[p], nm2[p]);
                            ov[p] = pk2(n2[0], n2[1]);
                        }
                        __builtin_amdgcn_raw_buffer_store_b128(ov, wsr, WS_XT + (32 * (w + 8 * k) + 4 * cq + e) * 4096 + a * 128 + o * 16, 0, 16);
                        SB();
                    }
                }
                if (k == 0) {
                    WAIT_V(4);
                    tag = mk64(__builtin_amdgcn_readfirstlane((unsigned)e0), __builtin_amdgcn_readfirstlane((unsigned)(e0 >> 32))) + 1ull;
                    arrive_publish(L_ACNT + 8, ws, WS_FS + a * 128, tag);
                }
            }
        }
        STAMP(15);
        {
            unsigned selo = 0x00003f80u, sehi = 0x3f800000u;
            asm volatile("" : "+v"(selo), "+v"(sehi));
            float sm[8] = {0.f, 0.f, 0.f, 0.f, 0.f, 0.f, 0.f, 0.f};
#pragma unroll
            for (int i = 0; i < 10; ++i)
#pragma unroll
                for (int j = 0; j < 4; ++j) { sm[2 * j] = dot2(cv[i][j], selo, sm[2 * j]); sm[2 * j + 1] = dot2(cv[i][j], sehi, sm[2 * j + 1]); }
#pragma unroll
            for (int j = 0; j < 8; ++j) sm[j] = dpp_add<0x128>(sm[j]);
            ldsw<f4v>(L_ACS + (4 * w + g) * 256 + (lane & 7) * 32 + (lane & 8) * 2, (lane & 8) ? f4v{sm[4], sm[5], sm[6], sm[7]} : f4v{sm[0], sm[1], sm[2], sm[3]});
        }
        BAR_L();
        if (w != 7 || a >= 27) {
            WAIT_V(0);
            arrive_publish(L_ACNT + 12, ws, WS_FA + a * 128, tag);
        } else {
            const float bf = bf2f(b1[64 * a + lane]);
            float p[32], sum = 0.f;
#pragma unroll
            for (int i = 0; i < 32; ++i) p[i] = ldsr<float>(L_ACS + i * 256 + lane * 4);
#pragma unroll
            for (int i = 0; i < 32; i += 4) sum += (p[i] + p[i + 1]) + (p[i + 2] + p[i + 3]);
            __builtin_amdgcn_raw_buffer_store_b32(__builtin_bit_cast(unsigned, sum), wsr, WS_CS + (64 * a + lane) * 4, 0, 16);
            __builtin_amdgcn_raw_buffer_store_b32(__builtin_bit_cast(unsigned, bf), wsr, WS_B1 + (64 * a + lane) * 4, 0, 16);
            WAIT_V(0);
            raise_flag(ws, WS_FC + a * 128, tag);
            arrive_publish(L_ACNT + 12, ws, WS_FA + a * 128, tag);
        }
        STAMP(2);
        if (w < 2) wait_flags(ws, WS_FW + (2 * min(54 * w + lane, 107) + (cb2 >> 2)) * 128, tag);
        BAR_L();
        STAMP(3);
        stage_x();
#pragma unroll
        for (int n = 0; n < 30; ++n) loadb(n);
        __builtin_amdgcn_s_waitcnt(0x0F70);
    }

    asm volatile("" : "+s"(rot2));
    if (w == 0) wait_flags(ws, WS_FH + ((rb2 >> 1) * 14 + min(lane, 13)) * 128, tag);
    BAR_L();
    STAMP(6);
    u4v ha[7][4];
    auto loada = [&](int i, int t) { ha[i][t] = __builtin_amdgcn_raw_buffer_load_b128(wsr, lane * 16, WS_HA + ((rb2 * 54 + sidx(i)) * 4 + t) * 1024, 16); };
#pragma unroll
    for (int i = 0; i < 8; ++i) loada(i >> 2, i & 3);
    if (is_gemm) {
        WAIT_V(8);
        arrive_publish(L_CNT + 4, ws, WS_FY + bid * 128, tag);
    }
    const int c0 = 80 * cb2;
    int lz = lane;
    asm volatile("" : "+v"(lz));
    const int cA = c0 + 10 * w, cB = cA + 9;
    const int own0 = max((cA * 0xaaab) >> 17, (cA - 192) >> 1), own1 = max((cB * 0xaaab) >> 17, (cB - 192) >> 1);
    u64 ypoll = tag;
    u4v y0, y1;
    f4v acc2[4][5];
#pragma unroll
    for (int i = 0; i < 20; ++i) acc2[i / 5][i % 5] = f4v{};
#pragma unroll
    for (int i = 0; i < 5; ++i) {
#pragma unroll
        for (int t = 0; t < 4; ++t) {
            SB();
#pragma unroll
            for (int c = 0; c < 5; ++c) acc2[t][c] = __builtin_amdgcn_mfma_f32_16x16x32_bf16(asbf(ha[i][t]), asbf(bw[5 * i + c]), acc2[t][c], 0, 0, 0);
            SB();
            if (i > 0) loada(i + 2, t);
            else if (t == 3) { loada(2, 0); loada(2, 1); loada(2, 2); loada(2, 3); }
            if (i == 1 || (i == 2 && t == 0)) loadb(30 + 4 * (i - 1) + t);
            if (i == 3 && t == 0) ypoll = ld_flag(ws, WS_FY + min(own0 + lz, own1) * 128);
            if (i == 4 && t == 3) {
                if (!__all(ypoll == tag)) wait_flags(ws, WS_FY + min(own0 + lz, own1) * 128, tag);
                y0 = __builtin_amdgcn_raw_buffer_load_b128(wsr, (lz >> 3) * 4096 + (lz & 7) * 16, WS_YT + cA * 4096 + rb2 * 128, 16);
                y1 = __builtin_amdgcn_raw_buffer_load_b128(wsr, (unsigned)((8 + (lz >> 3)) * 4096 + (lz & 7) * 16) | oob_if(lz >= 16), WS_YT + cA * 4096 + rb2 * 128, 16);
            }
        }
    }
    STAMP(7);
    auto dump = [&](int t) {
        unsigned p[10];
#pragma unroll
        for (int c = 0; c < 5; ++c) { p[2 * c] = pk2(acc2[t][c][0], acc2[t][c][1]); p[2 * c + 1] = pk2(acc2[t][c][2], acc2[t][c][3]); }
        addtid10(L_RED + w * RED_W + t * 5 * RED_T, p);
    };
#pragma unroll
    for (int t = 0; t < 4; ++t) {
        SB();
#pragma unroll
        for (int c = 0; c < 5; ++c) acc2[t][c] = __builtin_amdgcn_mfma_f32_16x16x32_bf16(asbf(ha[5][t]), asbf(bw[25 + c]), acc2[t][c], 0, 0, 0);
        SB();
        if (t > 0) dump(t - 1);
        SB();
#pragma unroll
        for (int c = 0; c < 5; ++c) acc2[t][c] = __builtin_amdgcn_mfma_f32_16x16x32_bf16(asbf(ha[6][t]), asbf(bw[30 + c]), acc2[t][c], 0, 0, 0);
    }
    SB();
    ldsw<u4v>(L_YS + (10 * w + (lz >> 3)) * YS_ROW + (lz & 7) * 16, y0);
    ldsw<u4v>(L_YS + min(10 * w + 8 + (lz >> 3) + (lz >= 16 ? 80 : 0), 80) * YS_ROW + (lz & 7) * 16, y1);
    dump(3);
    BAR_L();
    STAMP(8);
    {
        unsigned selo = 0x00003f80u, sehi = 0x3f800000u;
        asm volatile("" : "+v"(selo), "+v"(sehi));
#pragma unroll
        for (int k = 0; k < 3; ++k) {
            if (k < 2 || w < 4) {
                const int id = w * 64 + lz + 512 * k, tp = id / 40, c2 = id - tp * 40;
                const unsigned ra = L_RED + ((tp >> 3) * 5 + (c2 >> 3)) * RED_T + (tp & 1) * RED_ROW + ((tp >> 1) & 3) * 64 + (c2 & 7) * 8;
                u2v p[8];
#pragma unroll
                for (int j = 0; j < 8; ++j) p[j] = ldsr<u2v>(ra + j * RED_W);
                const unsigned ya = ldsr<unsigned>(L_YS + 2 * c2 * YS_ROW + tp * 4), yb = ldsr<unsigned>(L_YS + (2 * c2 + 1) * YS_ROW + tp * 4);
                const unsigned xa = ldsr<unsigned>(L_XS + tp * 320 + c2 * 4), xb = ldsr<unsigned>(L_XS + tp * 320 + 160 + c2 * 4), bv = ldsr<unsigned>(L_XS + 10240 + c2 * 4);
                float o00 = bflo(ya) + bflo(xa) + bflo(bv), o01 = bflo(yb) + bfhi(xa) + bfhi(bv);
                float o10 = bfhi(ya) + bflo(xb) + bflo(bv), o11 = bfhi(yb) + bfhi(xb) + bfhi(bv);
#pragma unroll
                for (int j = 0; j < 8; ++j) {
                    o00 = dot2(p[j][0], selo, o00); o10 = dot2(p[j][0], sehi, o10);
                    o01 = dot2(p[j][1], selo, o01); o11 = dot2(p[j][1], sehi, o11);
                }
                GP(unsigned) op = (GP(unsigned))(out + (rb2 * 64 + 2 * tp) * 640 + c0 + 2 * c2);
                op[0] = pk2(o00, o01);
                op[320] = pk2(o10, o11);
            }
        }
    }
    if (bid == 0 && w == 5) {
#pragma unroll
        for (int i = 0; i < 4; ++i) wait_flags(ws, WS_FH + (i * 64 + lz) * 128, tag);
        raise_flag(ws, WS_E, tag);
    }
    STAMP(9);
    if (w == 0 && bid == 5) { u64 c1 = __builtin_amdgcn_s_memtime(); *(u64*)(ws + WS_STAMP + 400000) = c1 - cyc0; *(u64*)(ws + WS_STAMP + 400008) = st[9] - st[0]; }
    for (int i = 0; i < NSTAMP; ++i) *(u64*)(ws + WS_STAMP + ((bid * 8 + w) * NSTAMP + i) * 8) = st[i];
}

__global__ void k_spin(const u64* st, int which, int mode) {
  u64 mn=~0ull; for(int i=0;i<2048;i++){ u64 a=st[i*NSTAMP]; mn=a<mn?a:mn; }
  u64 mx=0,sum=0; int n=0;
  for(int b=0;b<256;b++){ bool aux=(b>>3)>=28; u64 h=0; for(int w=0;w<8;w++){ u64 v=st[(b*8+w)*NSTAMP+which]; h=v>h?v:h; } if(h){ h-=mn; mx=h>mx?h:mx; if(aux==(mode==2)){ sum+=h; n++; } } }
  u64 d = (mode==0? mx : sum/(n?n:1))*100ull;
  if(d>1000000ull) d=1000000ull;
  u64 t0=__builtin_amdgcn_s_memrealtime();
  while(__builtin_amdgcn_s_memrealtime()-t0<d){ __builtin_amdgcn_s_sleep(8); }
}

extern "C" void kernel_launch(void* const* d_in, const int* in_sizes, int n_in, void* d_out, int out_size, void* d_ws, size_t ws_size, hipStream_t stream) {
    (void)in_sizes; (void)n_in; (void)out_size; (void)ws_size;
    (void)hipFuncSetAttribute((const void*)s4ffn_fused, hipFuncAttributeMaxDynamicSharedMemorySize, LDS_BYTES);
    s4ffn_fused<<<256, 512, LDS_BYTES, stream>>>((const u16*)d_in[0], (const u16*)d_in[1], (const u16*)d_in[2], (const u16*)d_in[3], (const u16*)d_in[4],
                                                 (const u16*)d_in[5], (const u16*)d_in[6], (const u16*)d_in[7], (const u16*)d_in[8], (const u16*)d_in[10],
                                                 (const u16*)d_in[11], (const u16*)d_in[12], (const u16*)d_in[13], (u16*)d_out, (char*)d_ws);
    k_spin<<<1,1,0,stream>>>((const u64*)((char*)d_ws+WS_STAMP),0,0);
}
```

```cpp
#define STAMPS
#include <hip/hip_runtime.h>
#include <stdint.h>
#define PUB_RHO 4
#define CSUM_ON 1
#define CODE_CHUNKS 4
#define CODE_BYTES 28672u
#define ARR 0

typedef unsigned short u16;
typedef unsigned long long u64;
typedef short s4v __attribute__((ext_vector_type(4)));
typedef __bf16 bf8v __attribute__((ext_vector_type(8)));
typedef __bf16 bf2v __attribute__((ext_vector_type(2)));
typedef float f2v __attribute__((ext_vector_type(2)));
typedef float f4v __attribute__((ext_vector_type(4)));
typedef float f16v __attribute__((ext_vector_type(16)));
typedef unsigned u2v __attribute__((ext_vector_type(2)));
typedef unsigned short h2v __attribute__((ext_vector_type(2)));
typedef unsigned u4v __attribute__((ext_vector_type(4)));
typedef __attribute__((address_space(1))) u64 gu64;
#define LDSP(T) __attribute__((address_space(3))) T*
#define DI __device__ __forceinline__
#define RLX_AGENT __ATOMIC_RELAXED, __HIP_MEMORY_SCOPE_AGENT

constexpr unsigned WS_E = 0;
constexpr unsigned WS_FH = 4096;
constexpr unsigned WS_FA = 32768;
constexpr unsigned WS_FW = 81920;
constexpr unsigned WS_FS = 40960;
constexpr unsigned WS_FC = 45056;
constexpr unsigned WS_FY = 49152;
constexpr unsigned WS_STATS = 131072;
constexpr unsigned WS_CS = 196608, WS_B1 = 204800;
constexpr unsigned WS_STAMP = 262144;
constexpr unsigned WS_XT = 1u << 20;
constexpr unsigned WS_W2T = 4u << 20;
constexpr unsigned WS_YT = 8u << 20;
constexpr unsigned WS_HA = 12u << 20;
constexpr unsigned WS_BYTES = 20u << 20;
constexpr unsigned SLOT = 32768;
constexpr unsigned L_KC = 98304, KC_CH = 16640, KC_CP = 4160;
constexpr unsigned L_UA = 148224, UA_CH = 4576, UA_PAD = 480;
constexpr unsigned L_MISC = 162368, L_CB = L_MISC, L_CNT = L_MISC + 1024;
constexpr unsigned L_TBL = 65536, TBL_ROW = 544, TBL_CH = 53 * TBL_ROW;
constexpr unsigned L_PRM = 152064, L_TT = 158464, TT_CH = 1296, L_W2U = L_PRM;
constexpr unsigned L_SCR = L_CB;
constexpr unsigned L_XS = 0, L_RED = 16384, RED_ROW = 256, RED_T = 576, RED_W = 20 * RED_T, L_YS = L_RED + 8 * RED_W, YS_ROW = 144;
constexpr unsigned L_AX = 0, L_AST = 81920, L_ACNT = L_AST + 512, L_ACS = L_AST + 1024;
static_assert(L_TT + 3 * TT_CH <= L_MISC && L_CNT + 64 <= 163840 && L_W2U + 10240 <= L_MISC, "LDS map");
constexpr unsigned LDS_BYTES = 163840;
constexpr int SPIN_LIMIT = 400000;

constexpr int NSTAMP = 16;
#define STAMP(i) do { st[i] = __builtin_amdgcn_s_memrealtime(); } while (0)

DI float bf2f(unsigned v) { return __uint_as_float(v << 16); }
DI float bflo(unsigned v) { return __uint_as_float(v << 16); }
DI float bfhi(unsigned v) { return __uint_as_float(v & 0xffff0000u); }
DI unsigned pk2(float a, float b) { bf2v v; v[0] = (__bf16)a; v[1] = (__bf16)b; return __builtin_bit_cast(unsigned, v); }
DI float rbf(float a) { return bflo(pk2(a, a)); }
template <typename P> DI u4v mkrsrc(P p, unsigned bytes) { u64 a = (u64)p; u4v r; r[0] = (unsigned)a; r[1] = (unsigned)(a >> 32); r[2] = bytes; r[3] = 0x00020000u; return r; }
DI void dma16(u4v rs, unsigned voff, unsigned soff, unsigned lds) {
    asm volatile("s_mov_b32 m0, %3\n\ts_nop 0\n\tbuffer_load_dwordx4 %0, %1, %2 offen lds" ::"v"(voff), "s"(rs), "s"(soff), "s"(lds) : "memory");
}
DI void dma16g(const void* p, unsigned lds) {
    asm volatile("s_mov_b32 m0, %1\n\ts_nop 0\n\tglobal_load_lds_dwordx4 %0, off" ::"v"(p), "s"(lds) : "memory");
}
DI void dma16g_lanes3(const void* p, unsigned lds) {
    u64 ex;
    asm volatile("s_mov_b64 %0, exec\n\ts_mov_b64 exec, 7\n\ts_mov_b32 m0, %2\n\ts_nop 0\n\tglobal_load_lds_dwordx4 %1, off\n\ts_mov_b64 exec, %0" : "=&s"(ex) : "v"(p), "s"(lds) : "memory");
}
DI void dma16_lanes16(u4v rs, unsigned voff, unsigned soff, unsigned lds) {
    u64 ex;
    asm volatile("s_mov_b64 %0, exec\n\ts_mov_b64 exec, 0xffff\n\ts_mov_b32 m0, %4\n\ts_nop 0\n\tbuffer_load_dwordx4 %1, %2, %3 offen lds\n\ts_mov_b64 exec, %0"
                 : "=&s"(ex) : "v"(voff), "s"(rs), "s"(soff), "s"(lds) : "memory");
}
DI void touch_code(unsigned chunk, int lane, unsigned lds) {
    u64 pc;
    asm volatile("s_getpc_b64 %0" : "=s"(pc));
    const u64 p = (pc & ~127ull) + min(chunk * 8192u + lane * 128u, CODE_BYTES - 128u);
    asm volatile("s_mov_b32 m0, %1\n\ts_nop 0\n\tglobal_load_lds_dword %0, off" ::"v"(p), "s"(lds) : "memory");
}
#define SB() __builtin_amdgcn_sched_barrier(0)
#define WAIT_BAR(n) asm volatile("s_waitcnt vmcnt(" #n ") lgkmcnt(0)\n\ts_barrier" ::: "memory")
#define BAR_L() asm volatile("s_waitcnt lgkmcnt(0)\n\ts_barrier" ::: "memory")
#define WAIT_V(n) asm volatile("s_waitcnt vmcnt(" #n ")" ::: "memory")
template <typename T> DI T ldsr(unsigned a) { return *(LDSP(T))a; }
template <typename T> DI void ldsw(unsigned a, T v) { *(LDSP(T))a = v; }
DI void stw(unsigned a, unsigned v, int off) { asm volatile("ds_write_b32 %0, %1 offset:%2" ::"v"(a), "v"(v), "i"(off) : "memory"); }
DI s4v trr(unsigned a) { return __builtin_amdgcn_ds_read_tr16_b64_v4i16((LDSP(s4v))a); }
DI bf8v cat(s4v a, s4v b) { return __builtin_bit_cast(bf8v, __builtin_shufflevector(a, b, 0, 1, 2, 3, 4, 5, 6, 7)); }
DI bf8v asbf(u4v a) { return __builtin_bit_cast(bf8v, a); }
template <int CTRL> DI float dpp_add(float v) { return v + __builtin_bit_cast(float, __builtin_amdgcn_update_dpp(0, __builtin_bit_cast(int, v), CTRL, 0xf, 0xf, true)); }
DI float sum8(float v) { return dpp_add<0x141>(dpp_add<0x4e>(dpp_add<0xb1>(v))); }
DI float dot2(unsigned a, unsigned b, float c) { return __builtin_amdgcn_fdot2_f32_bf16(__builtin_bit_cast(bf2v, a), __builtin_bit_cast(bf2v, b), c, false); }
DI u64 ld_flag(const char* ws, unsigned off) { return __hip_atomic_load((gu64*)(ws + off), RLX_AGENT); }
DI void wait_flags(const char* ws, unsigned off, u64 tag) {
#pragma nounroll
    for (int it = 0; it < SPIN_LIMIT; ++it) {
        if (__all(ld_flag(ws, off) == tag)) break;
        __builtin_amdgcn_s_sleep(1);
    }
    asm volatile("" ::: "memory");
}
DI unsigned lds_count(unsigned cnt_lds) {
    unsigned old, one = 1u;
    u64 ex;
    asm volatile("s_mov_b64 %1, exec\n\ts_mov_b64 exec, 1\n\ts_waitcnt lgkmcnt(0)\n\tds_add_rtn_u32 %0, %2, %3\n\ts_waitcnt lgkmcnt(0)\n\ts_mov_b64 exec, %1"
                 : "=&v"(old), "=&s"(ex) : "v"(cnt_lds), "v"(one) : "memory");
    return __builtin_amdgcn_readfirstlane(old);
}
DI void raise_flag(char* ws, unsigned flag_off, u64 tag) { __hip_atomic_store((gu64*)(ws + flag_off), tag, RLX_AGENT); }
DI void arrive_publish(unsigned cnt_lds, char* ws, unsigned flag_off, u64 tag, unsigned last = 7u) {
    if (lds_count(cnt_lds) == last) raise_flag(ws, flag_off, tag);
}
DI void lds_wait(unsigned cnt_lds, unsigned n) {
#pragma nounroll
    for (int it = 0; it < SPIN_LIMIT; ++it) {
        if (__builtin_amdgcn_readfirstlane(*(volatile LDSP(unsigned))cnt_lds) >= n) break;
        __builtin_amdgcn_s_sleep(1);
    }
    asm volatile("" ::: "memory");
}
DI unsigned oob_if(bool c) { return c ? 0x80000000u : 0u; }
DI f2v ln_stats(float s1, float s2) {
    const float mean = s1 * (1.f / 640.f), var = fmaxf(__builtin_fmaf(-mean, mean, s2 * (1.f / 640.f)), 0.f);
    const float rs = __builtin_amdgcn_rcpf(rbf(__builtin_amdgcn_sqrtf(rbf(rbf(var) + 1e-5f))));
    return f2v{rs, rbf(mean) * rs};
}
DI f2v cmul_a(f2v a, f2v b) { f2v t; asm("v_pk_mul_f32 %0, %1, %2 op_sel_hi:[0,1]" : "=v"(t) : "v"(a), "v"(b)); return t; }
DI f2v cmul_b(f2v a, f2v b, f2v t) {
    f2v o;
    asm("v_pk_fma_f32 %0, %1, %2, %3 op_sel:[1,1,0] op_sel_hi:[1,0,1] neg_lo:[0,1,0]" : "=v"(o) : "v"(a), "v"(b), "v"(t));
    return o;
}
DI f2v cmul(f2v a, f2v b) { return cmul_b(a, b, cmul_a(a, b)); }
DI f2v cexp_p(float are, float aim, float p) {
    const float m = __builtin_amdgcn_exp2f(are * p), an = __builtin_amdgcn_fractf(aim * p);
    return f2v{m * __builtin_amdgcn_cosf(an), m * __builtin_amdgcn_sinf(an)};
}

DI void addtid10(unsigned m0v, const unsigned (&p)[10]) {
    asm volatile("s_mov_b32 m0, %10\n\ts_nop 0\n\t"
                 "ds_write_addtid_b32 %0 offset:%11\n\tds_write_addtid_b32 %1 offset:%12\n\tds_write_addtid_b32 %2 offset:%13\n\tds_write_addtid_b32 %3 offset:%14\n\t"
                 "ds_write_addtid_b32 %4 offset:%15\n\tds_write_addtid_b32 %5 offset:%16\n\tds_write_addtid_b32 %6 offset:%17\n\tds_write_addtid_b32 %7 offset:%18\n\t"
                 "ds_write_addtid_b32 %8 offset:%19\n\tds_write_addtid_b32 %9 offset:%20"
                 ::"v"(p[0]), "v"(p[1]), "v"(p[2]), "v"(p[3]), "v"(p[4]), "v"(p[5]), "v"(p[6]), "v"(p[7]), "v"(p[8]), "v"(p[9]), "s"(m0v),
                   "i"(0), "i"(RED_ROW), "i"(RED_T), "i"(RED_T + RED_ROW), "i"(2 * RED_T), "i"(2 * RED_T + RED_ROW), "i"(3 * RED_T), "i"(3 * RED_T + RED_ROW),
                   "i"(4 * RED_T), "i"(4 * RED_T + RED_ROW) : "memory");
}

#define GP(T) __attribute__((address_space(1))) T*
typedef unsigned u8v __attribute__((ext_vector_type(8)));
DI u64 mk64(unsigned lo, unsigned hi) { return ((u64)hi << 32) | lo; }
__global__ __launch_bounds__(512, 2) void s4ffn_fused(const u16*, const u16*, const u16*, const u16*, const u16*, const u16*, const u16*, const u16*, const u16*,
                                                      const u16*, const u16*, const u16*, const u16*, u16*, char*) {
    const int tid = threadIdx.x, lane = tid & 63, w = __builtin_amdgcn_readfirstlane(tid >> 6);
    const int xcd = blockIdx.x & 7, r = ((blockIdx.x >> 3) + 28) & 31, bid = r * 8 + xcd;
    const bool is_gemm = r < 28;
    u64 st[NSTAMP];
    for (int i = 0; i < NSTAMP; ++i) st[i] = 0;
    u64 cyc0 = __builtin_amdgcn_s_memtime();
    STAMP(0);
    if (w < CODE_CHUNKS && ((r + w) & 3) == 0) touch_code(w, lane, 163840 - 256);
    u64 a_x, a_prm;
    u8v a_m;
    u4v a_t;
    asm volatile("s_load_dwordx2 %0, %4, 0x0\n\ts_load_dwordx2 %1, %4, %5 offset:0x8\n\ts_load_dwordx8 %2, %4, 0x48\n\ts_load_dwordx4 %3, %4, 0x68\n\ts_waitcnt lgkmcnt(0)"
                 : "=&s"(a_x), "=&s"(a_prm), "=&s"(a_m), "=&s"(a_t) : "s"(__builtin_amdgcn_kernarg_segment_ptr()), "s"(8 * w));
    const GP(u16) x = (const GP(u16))a_x;
    const char* prm = (const char*)a_prm;
    const GP(u16) w1 = (const GP(u16))mk64(a_m[0], a_m[1]);
    const GP(u16) b1 = (const GP(u16))mk64(a_m[2], a_m[3]);
    const GP(u16) w2 = (const GP(u16))mk64(a_m[4], a_m[5]);
    const GP(u16) b2 = (const GP(u16))mk64(a_m[6], a_m[7]);
    GP(u16) out = (GP(u16))mk64(a_t[0], a_t[1]);
    char* ws = (char*)mk64(a_t[2], a_t[3]);
    u64 tag;
    const auto wsr = __builtin_amdgcn_make_buffer_rsrc((void*)ws, 0, WS_BYTES, 0x00020000);
    const int nch = bid < 192 ? 3 : 2, ch0 = 3 * bid - max(bid - 192, 0);
    const int g = lane >> 4, l16 = lane & 15;
    const int rb2 = xcd * 4 + (r & 3), cb2 = r >> 2;
    const bool has7 = w < 6;
    u4v bw[35];
    int rot2 = 7 * cb2 + 14 * (r & 3) + w;
    rot2 = rot2 >= 54 ? rot2 - 54 : rot2;
    auto sidx = [&](int i) { const int s = rot2 + 8 * i; return s >= 54 ? s - 54 : s; };
    auto loadb = [&](int n) {
        bw[n] = __builtin_amdgcn_raw_buffer_load_b128(wsr, lane * 16 | oob_if(n >= 30 && !has7), WS_W2T + ((cb2 * 54 + sidx(n / 5)) * 5 + n % 5) * 1024, 16);
    };

    auto stage_x = [&]() {
        const u4v rx = mkrsrc(x, 2048 * 1280);
        const int n1 = 64 + (lane & 15);
        dma16(rx, (lane / 10) * 1280 + (lane % 10) * 16, (rb2 * 64 + 8 * w) * 1280 + cb2 * 160, L_XS + w * 1280);
        dma16_lanes16(rx, (n1 / 10) * 1280 + (n1 % 10) * 16, (rb2 * 64 + 8 * w) * 1280 + cb2 * 160, L_XS + w * 1280 + 1024);
        if (w == 0) dma16_lanes16(mkrsrc(b2, 1280), min(lane, 9) * 16, cb2 * 160, L_XS + 10240);
    };
    if (__builtin_expect(is_gemm, 1)) {
        if (w < 6) dma16g(prm + (ch0 + min(g, nch - 1)) * 256 + l16 * 16, L_PRM + w * 1024);
        else dma16g_lanes3(lane == 2 ? (const char*)ws + WS_E : prm + min(((ch0 * 2) & ~15) + lane * 16, 1264), L_SCR + (w - 6) * 64);
        SB();
        const int gi = r & 3, gj = r >> 2, rb = (xcd >> 1) * 4 + gi, cb = (xcd & 1) * 7 + gj;
        const int fw = w & 3, tw = w >> 2;
        const u4v rx = mkrsrc(x, 2048 * 1280), rw = mkrsrc(w1, 640 * 3456);
        unsigned vx[2];
#pragma unroll
        for (int u = 0; u < 2; ++u) vx[u] = (rb * 128 + 16 * w + 8 * u + (lane >> 3)) * 1280 + (((lane & 7) ^ (4 * u + g)) << 4);
        const int wc = l16 ^ (2 * g) ^ ((w & 1) << 3);
        const unsigned vw = (unsigned)((8 * w + g) * 3456 + cb * 256 + wc * 16) | (cb == 13 ? (unsigned)(wc & 8) << 28 : 0u);
        int rot = gj + 3 * gi + xcd;
        rot = rot >= 20 ? rot - 20 : rot >= 10 ? rot - 10 : rot;
        auto stage1w = [&](int t, int slot, int p, int dw) {
            int kt = t + rot; kt = kt >= 10 ? kt - 10 : kt;
            const unsigned lb = slot * SLOT + (w + dw) * 2048 + (p >> 1) * 1024;
            if (p & 1) dma16(rw, vw + dw * 8 * 3456, kt * (64 * 3456) + (p >> 1) * 4 * 3456, lb + 16384);
            else dma16(rx, vx[p >> 1] + dw * 16 * 1280, kt * 128, lb);
        };
        auto stage1 = [&](int t, int slot, int p) { stage1w(t, slot, p, 0); };
#pragma unroll
        for (int i = 0; i < 4; ++i) stage1(0, 0, i);
        u4v w2a, w2b;
        {
            const auto rw2 = __builtin_amdgcn_make_buffer_rsrc((void*)(u64)w2, 0, 1728 * 1280, 0x00020000);
            const unsigned so2 = ((bid >> 2) * 32 + ((bid >> 1) & 1) * 8 + (w >> 2) * 16 + (w & 3) * 2) * 1280 + (bid & 1) * 640;
            w2a = __builtin_amdgcn_raw_buffer_load_b128(rw2, lane * 16 + (lane >= 40 ? 640 : 0), so2, 0);
            w2b = __builtin_amdgcn_raw_buffer_load_b128(rw2, (1280 + 384 + lane * 16) | oob_if(lane >= 16), so2, 0);
        }
        SB();
        unsigned ix[12];
        {
            const int l0 = 8 * (tid & 255) - 3;
            const float lf = (float)l0;
            const unsigned kb = L_TT + tw * TT_CH, kc = kb - 2u * (0x4300 - 128);
#pragma unroll
            for (int i = 0; i < 6; ++i) {
                const unsigned c2 = pk2(lf + (float)(2 * i), lf + (float)(2 * i + 1));
                asm("v_mad_u32_u16 %0, %1, 2, %2" : "=v"(ix[2 * i]) : "v"(c2), "s"(kc));
                asm("v_mad_u32_u16 %0, %1, 2, %2 op_sel:[1,0,0,0]" : "=v"(ix[2 * i + 1]) : "v"(c2), "s"(kc));
            }
            if (fw == 0) {
#pragma unroll
                for (int i = 0; i < 11; ++i) {
                    const int l = l0 + i;
                    const unsigned lo = kb + 2u * min((unsigned)l, 641u), m = (unsigned)((l - 128) >> 31);
                    ix[i] = (lo & m) | (ix[i] & ~m);
                }
            }
#pragma unroll
            for (int i = 0; i < 11; ++i) asm volatile("" : "+v"(ix[i]));
        }
        if (w == 0) {
            ldsw<unsigned>(L_CNT + (lane & 7) * 4, 0u);
        }
        STAMP(10);
        WAIT_BAR(6);
        STAMP(11);
        const unsigned so = (ch0 * 2) & 15;
        { const u2v tv = ldsr<u2v>(L_SCR + 32); tag = mk64(__builtin_amdgcn_readfirstlane(tv[0]), __builtin_amdgcn_readfirstlane(tv[1])) + 1ull; }
        const unsigned dsk01 = __builtin_amdgcn_readfirstlane(ldsr<u16>(L_SCR + 64 + so) | ((unsigned)ldsr<u16>(L_SCR + 64 + so + 2) << 16));
        const unsigned dsk2 = __builtin_amdgcn_readfirstlane(ldsr<u16>(L_SCR + 64 + so + 4));
        {
            const int hc = w < 6 ? w : w - 2, c = hc >> 1, mode = (hc & 1) * 64 + lane;
            const float ldt = bf2f(ldsr<u16>(L_SCR + so + 2 * c));
            const unsigned pb = L_PRM + c * 256 + mode * 2;
            const float pa = bf2f(ldsr<u16>(pb)), pi = bf2f(ldsr<u16>(pb + 1024));
            const f2v Bc = {bf2f(ldsr<u16>(pb + 2048)), bf2f(ldsr<u16>(pb + 3072))}, Cc = {bf2f(ldsr<u16>(pb + 4096)), bf2f(ldsr<u16>(pb + 5120))};
            lds_count(L_CNT + 12);
            if (w >= 4) {
#pragma unroll
                for (int i = 0; i < 8; ++i) stage1w(1, 1, i & 3, i < 4 ? 0 : -4);
            }
            if (c < nch) {
                const float dt = rbf(__builtin_amdgcn_exp2f(ldt * 1.4426950408889634f));
                const float are = dt * pa * 1.4426950408889634f, aim = dt * pi * 0.15915494309189535f;
                const unsigned tb = L_TBL + c * TBL_CH + mode * 4;
                if (w < 6) {
                    const f2v z8 = cexp_p(are, aim, 8.f), z16 = cexp_p(are, aim, 16.f), z64 = cexp_p(are, aim, 64.f);
                    f2v e[8];
                    e[0] = f2v{1.f, 0.f}; e[1] = z8; e[2] = z16; e[3] = cmul(z16, z8); e[4] = cmul(z16, z16);
                    e[5] = cmul(e[3], z16); e[6] = cmul(e[4], z16); e[7] = cmul(e[4], e[3]);
#pragma unroll
                    for (int i = 0; i < 4; ++i) {
                        f2v t[8];
#pragma unroll
                        for (int j = 0; j < 8; ++j) {
                            if (i < 3) t[j] = cmul_a(e[j], z64);
                            stw(tb, pk2(e[j][0], e[j][1]), (21 + 8 * i + j) * TBL_ROW);
                        }
                        if (i < 3) {
#pragma unroll
                            for (int j = 0; j < 8; ++j) e[j] = cmul_b(e[j], z64, t[j]);
                        }
                    }
                }
                if (w < 4 || w >= 6) {
                    const f2v z1 = cexp_p(are, aim, 1.f), z2 = cexp_p(are, aim, 2.f), z4 = cexp_p(are, aim, 4.f);
                    const f2v z256 = cexp_p(are, aim, 256.f), z1024 = cexp_p(are, aim, 1024.f);
                    const f2v W = cmul(Cc, Bc * dt);
                    auto putx = [&](int row, f2v v) { stw(tb, pk2(v[0], -v[1]), row * TBL_ROW); };
                    const f2v x1 = cmul(W, z1), x2 = cmul(W, z2), x8 = cmul(W, z256), x16 = cmul(W, z1024);
                    const f2v x3 = cmul(x1, z2), x9 = cmul(x8, z2), x12 = cmul(x8, z256), x17 = cmul(x16, z256), x20 = cmul(x16, z1024);
                    const f2v x14 = cmul(x12, z256), x18 = cmul(x17, z256);
                    putx(0, W); putx(1, x1); putx(2, x2); putx(3, x3); putx(8, x8); putx(9, x9); putx(12, x12); putx(14, x14);
                    putx(16, x16); putx(17, x17); putx(18, x18); putx(20, x20);
                    putx(4, cmul(W, z4)); putx(5, cmul(x1, z4)); putx(6, cmul(x2, z4)); putx(7, cmul(x3, z4));
                    putx(10, cmul(x8, z4)); putx(11, cmul(x9, z4)); putx(13, cmul(x12, z4)); putx(15, cmul(x14, z4)); putx(19, cmul(x18, z256));
                }
            }
        }
        lds_wait(L_CNT + 12, 8u);
        ldsw<u4v>(L_W2U + w * 1280 + lane * 16, w2a);
        ldsw<u4v>(lane < 16 ? L_W2U + w * 1280 + 1024 + lane * 16 : L_CNT + 48, w2b);
        STAMP(13);
        BAR_L();
        if (w >= 6) {
            const unsigned tl = L_W2U + (4 * (g & 1) + (l16 >> 2)) * 640 + (lane >> 5) * 32 + (lane & 3) * 8 + (w - 6) * 320;
            bf8v f[5];
#pragma unroll
            for (int k = 0; k < 5; ++k) f[k] = cat(trr(tl + k * 64), trr(tl + k * 64 + 8 * 640));
            lds_count(L_CNT + 16);
#pragma unroll
            for (int k = 0; k < 5; ++k) {
                const int P = 20 * (bid & 1) + 10 * (w - 6) + 2 * k + (lane >> 5), cbb = (P * 52) >> 8, cfr = P - cbb * 5;
                __builtin_amdgcn_raw_buffer_store_b128(__builtin_bit_cast(u4v, f[k]), wsr,
                    (unsigned)(WS_W2T + (bid >> 2) * 5120 + ((bid >> 1) & 1) * 512 + (cbb * 270 + cfr) * 1024 + (lane & 31) * 16) | oob_if(bid >= 216), 0, 16);
            }
        }
        if (w < 2 * nch) {
            const int c = w >> 1, col = 16 * (w & 1) + l16;
            const unsigned ta = L_TBL + c * TBL_CH + l16 * TBL_ROW + g * 16;
            const unsigned te = ta + (21 + 16 * (w & 1)) * TBL_ROW;
            u4v fa[8], fb[8], fe[8];
#pragma unroll
            for (int s = 0; s < 8; ++s) { fe[s] = ldsr<u4v>(te + s * 64); fa[s] = ldsr<u4v>(ta + s * 64); fb[s] = ldsr<u4v>(ta + 16 * TBL_ROW + s * 64); }
            const unsigned tt = L_TT + c * TT_CH;
            const unsigned ta0 = tt + ((col << (3 - max(g - 1, 0))) + ((0x30200100u >> (8 * g)) & 0xffu) * (g == 1 ? 4u : 8u)) * 2;
            const unsigned ta1 = ta0 + (g < 3 ? 4 : 128);
            const unsigned tb0 = tt + (g == 0 ? 512 + col : 644) * 2, st1 = g == 0 ? 64 : 0;
            const unsigned tb1 = g == 1 && col < 2 ? tt + (640 + col) * 2 : tb0;
            SB();
            f4v k0 = {}, k1 = {};
#pragma unroll
            for (int s = 0; s < 8; ++s) {
                k0 = __builtin_amdgcn_mfma_f32_16x16x32_bf16(asbf(fa[s]), asbf(fe[s]), k0, 0, 0, 0);
                k1 = __builtin_amdgcn_mfma_f32_16x16x32_bf16(asbf(fb[s]), asbf(fe[s]), k1, 0, 0, 0);
            }
            lds_wait(L_CNT + 16, 2u);
            ldsw<unsigned>(ta0, pk2(k0[0], k0[1]));
            ldsw<unsigned>(ta1, pk2(k0[2], k0[3]));
            const unsigned q01 = pk2(k1[0], k1[1]), q23 = pk2(k1[2], k1[3]);
            ldsw<u16>(tb1, g == 1 && col == 1 ? (u16)0 : (u16)q01);
            ldsw<u16>(tb0 + st1, (u16)(q01 >> 16));
            ldsw<u16>(tb0 + 2 * st1, (u16)q23);
            ldsw<u16>(tb0 + 3 * st1, (u16)(q23 >> 16));
        }
        BAR_L();
        STAMP(14);
        {
            const bool two = w < 4 && nch == 3;
            const int t = tid & 255;
#pragma unroll
            for (int k = 0; k < 2; ++k) {
                if (k == 0 || two) {
                    unsigned pe[6], po[5];
#pragma unroll
                    for (int i = 0; i < 5; ++i) pe[i] = __builtin_bit_cast(unsigned, h2v{ldsr<u16>(ix[2 * i] + k * 2 * TT_CH), ldsr<u16>(ix[2 * i + 1] + k * 2 * TT_CH)});
                    pe[5] = ldsr<u16>(ix[10] + k * 2 * TT_CH);
                    if (k == 0) { stage1(2, 2, 0); stage1(2, 2, 1); stage1(2, 2, 2); stage1(2, 2, 3); }
#pragma unroll
                    for (int i = 0; i < 5; ++i) po[i] = __builtin_amdgcn_alignbit(pe[i + 1], pe[i], 16);
                    const unsigned cbase = L_KC + (tw + 2 * k) * KC_CH, d = cbase + 32 + t * 16;
                    ldsw<u4v>(d, u4v{po[1], po[2], po[3], po[4]});
                    ldsw<u4v>(d + KC_CP, u4v{pe[1], pe[2], pe[3], pe[4]});
                    ldsw<u4v>(d + 2 * KC_CP, u4v{po[0], po[1], po[2], po[3]});
                    ldsw<u4v>(d + 3 * KC_CP, u4v{pe[0], pe[1], pe[2], pe[3]});
                }
            }
            if (w == 7) ldsw<u4v>(L_KC + min(lane >> 1, 11) * KC_CP + (lane & 1) * 16, u4v{0u, 0u, 0u, 0u});
        }
        STAMP(1);
        f4v acc1[2][4];
#pragma unroll
        for (int i = 0; i < 8; ++i) acc1[i >> 2][i & 3] = f4v{};
        unsigned bx[2], aw[2];
        {
            const int q4 = l16 >> 2, p4 = lane & 3;
#pragma unroll
            for (int i = 0; i < 2; ++i) {
                bx[i] = (64 * tw + l16) * 128 + (((4 * i + g) ^ (l16 >> 1)) << 4);
                aw[i] = 16384 + (8 * g + q4) * 256 + (((2 * fw + i) ^ q4 ^ ((g & 1) << 2)) << 5) + p4 * 8;
            }
        }
        bf8v fa[2][2][2], fb[2][2][4];
        auto rd = [&](int buf, int i) {
            const int ks = i >> 1, h = i & 1;
            fa[buf][ks][h] = cat(trr(aw[h] + ks * 8192), trr(aw[h] + ks * 8192 + 1024));
            fb[buf][ks][2 * h] = asbf(ldsr<u4v>(bx[ks] + h * 4096));
            fb[buf][ks][2 * h + 1] = asbf(ldsr<u4v>(bx[ks] + h * 4096 + 2048));
        };
        unsigned sb = 0;
        int kt = rot + 2; kt = kt >= 10 ? kt - 10 : kt;
        auto tile = [&](int cur, bool more, bool stage, auto&& side) {
            const unsigned d = sb == 2 * SLOT ? (unsigned)(-2 * (int)SLOT) : SLOT, lb = sb + w * 2048;
            kt = kt == 9 ? 0 : kt + 1;
            sb += d; aw[0] += d; aw[1] += d; bx[0] += d; bx[1] += d;
#pragma unroll
            for (int i = 0; i < 4; ++i) {
                SB();
                if (more) rd(cur ^ 1, i);
                SB();
#pragma unroll
                for (int j = 0; j < 4; ++j) acc1[i & 1][j] = __builtin_amdgcn_mfma_f32_16x16x32_bf16(fa[cur][i >> 1][i & 1], fb[cur][i >> 1][j], acc1[i & 1][j], 0, 0, 0);
                SB();
                if (stage) {
                    if (i & 1) dma16(rw, vw, kt * (64 * 3456) + (i >> 1) * 4 * 3456, lb + (i >> 1) * 1024 + 16384);
                    else dma16(rx, vx[i >> 1], kt * 128, lb + (i >> 1) * 1024);
                }
                side(i);
            }
            SB();
        };
        auto none = [&](int) {};
        if (w >= 6) {
            WAIT_V(4);
            arrive_publish(L_CNT + 8, ws, WS_FW + min(bid, 216) * 128, tag, 1u);
        }
        BAR_L();
        STAMP(15);
#pragma unroll
        for (int i = 0; i < 4; ++i) rd(0, i);
        u4v uld[2], cbv;
        f2v sv[4];
        u64 fpoll = 0;
        const unsigned f1a = WS_FS + 2 * rb * 128, f1b = WS_FC + 2 * cb * 128;
        const unsigned f1_off = min(((lane & 2) ? f1b : f1a) + (lane & 1) * 128, WS_FC + 26 * 128);
        const unsigned f2_off = w == 2 ? WS_FA + (lane & 31) * 128 : WS_FW + (2 * min(54 * w + lane, 107) + (cb2 >> 2)) * 128;
        WAIT_BAR(4); tile(0, true, true, none);
        WAIT_BAR(4); tile(1, true, true, none);
        WAIT_BAR(4); tile(0, true, true, [&](int i) { if (i == 0 && w < 2) fpoll = ld_flag(ws, f2_off); });
        if (w < 2 && !__all(fpoll == tag)) wait_flags(ws, f2_off, tag);
        WAIT_BAR(4);
        STAMP(12);
        tile(1, true, true, [&](int i) { if (i == 0) loadb(0); if (i == 2) loadb(1); });
        WAIT_BAR(6); tile(0, true, true, [&](int i) { if (i == 0) loadb(2); if (i == 2) loadb(3); });
        WAIT_BAR(6); tile(1, true, true, [&](int i) { if (i == 0) loadb(4); if (i == 2) loadb(5); });
        WAIT_BAR(6); tile(0, true, true, [&](int i) { if (i == 0) loadb(6); if (i == 2) loadb(7); });
        WAIT_BAR(6); tile(1, true, false, [&](int i) { if (i == 0 && w == 0) fpoll = ld_flag(ws, f1_off); loadb(8 + i); });
        if (w == 0 && !__all(fpoll == tag)) wait_flags(ws, f1_off, tag);
        WAIT_BAR(4);
        STAMP(3);
        tile(0, true, false, [&](int i) {
            sv[i] = __builtin_bit_cast(f2v, __builtin_amdgcn_raw_buffer_load_b64(wsr, l16 * 8, WS_STATS + (rb * 128 + 64 * tw + 16 * i) * 8, 16));
            if (i == 0 && w == 1) cbv = __builtin_amdgcn_raw_buffer_load_b128(wsr, (lane >> 5) * (WS_B1 - WS_CS) + (lane & 31) * 16, WS_CS + cb * 512, 16);
            if (i == 0 && w == 2) fpoll = ld_flag(ws, f2_off);
            loadb(12 + i);
        });
        tile(1, false, false, [&](int i) { loadb(16 + i); });
        if (w == 1) ldsw<u4v>(L_CB + lane * 16, cbv);
        if (w >= 2 && w < 5) ldsw<u4v>(L_UA + (w - 2) * UA_CH + min(lane, 29) * 16, u4v{0u, 0u, 0u, 0u});
        if (w == 2 && !__all(fpoll == tag)) wait_flags(ws, f2_off, tag);
        BAR_L();
        STAMP(2);
        stage_x();
        {
            f4v cs[2], bb[2];
#pragma unroll
            for (int u = 0; u < 2; ++u) { cs[u] = ldsr<f4v>(L_CB + (32 * fw + 16 * u + 4 * g) * 4); bb[u] = ldsr<f4v>(L_CB + 512 + (32 * fw + 16 * u + 4 * g) * 4); }
            const int s = 4 * cb + fw;
            const unsigned ho = (unsigned)(WS_HA + ((2 * rb + tw) * 54 + s) * 4096 + lane * 16) | oob_if(s >= 54);
            u4v hp[4];
#pragma unroll
            for (int ti = 0; ti < 4; ++ti) {
                const f2v rs2 = {sv[ti][0], sv[ti][0]}, nm2 = {-sv[ti][1], -sv[ti][1]};
#pragma unroll
                for (int i = 0; i < 4; ++i) {
                    const int slot = 4 * ti + i, u = i >> 1, e = 2 * (i & 1);
                    if (slot < 2) {
                        const int q = w + 8 * slot, c = q >> 2;
                        uld[slot] = __builtin_amdgcn_raw_buffer_load_b128(wsr, lane * 16 | oob_if(c >= nch), WS_XT + (ch0 + c) * 4096 + (q & 3) * 1024, 16);
                    }
                    if (slot >= 2 && slot < 12) loadb(18 + slot);
                    SB();
                    const f2v v = __builtin_elementwise_fma(rs2, f2v{acc1[u][ti][e], acc1[u][ti][e + 1]}, __builtin_elementwise_fma(nm2, f2v{cs[u][e], cs[u][e + 1]}, f2v{bb[u][e], bb[u][e + 1]}));
                    const f2v z = v * __builtin_elementwise_fma(v * v, f2v{-0.10294324f, -0.10294324f}, f2v{-2.30220820f, -2.30220820f});
                    const f2v d = f2v{__builtin_amdgcn_exp2f(z[0]), __builtin_amdgcn_exp2f(z[1])} + f2v{1.f, 1.f};
                    const f2v gl = v * f2v{__builtin_amdgcn_rcpf(d[0]), __builtin_amdgcn_rcpf(d[1])};
                    hp[ti][i] = pk2(gl[0], gl[1]);
                    asm volatile("" : "+v"(hp[ti][i]));
                }
            }
            SB();
#pragma unroll
            for (int ti = 0; ti < 4; ++ti)
                __builtin_amdgcn_raw_buffer_store_b128(hp[ti], wsr, ho + 1024 * ti, 0, 16);
        }
#pragma unroll
        for (int i = 0; i < 2; ++i) {
            const int q = w + 8 * i, c = q >> 2;
            if (q < 12) ldsw<u4v>(L_UA + c * UA_CH + UA_PAD + (q & 3) * 1024 + lane * 16, uld[i]);
        }
        STAMP(4);
        BAR_L();
        {
            const int typ = w < 3 ? 0 : w < 6 ? 1 : 2;
            const int c1 = w < 3 ? w : w < 6 ? w - 3 : 2 * (w - 6), c2 = w == 6 ? 1 : c1;
            const unsigned kl = L_KC + (l16 >> 2) * KC_CP + 56 - 16 * g + 8 * (lane & 3);
            const unsigned ul = L_UA + UA_PAD + 32 * l16 - 32 + 16 * g;
            unsigned ka[3], ua[3];
            ka[0] = kl + c1 * KC_CH;
            ka[1] = typ == 2 ? kl + c2 * KC_CH - 4 * 512 : ka[0];
            ka[2] = typ == 1 ? ka[0] - 6 * 512 : ka[1];
            ua[0] = ul + c1 * UA_CH - typ * 1024;
            ua[2] = ul + c2 * UA_CH;
            ua[1] = typ == 2 ? ua[2] : ua[0];
            bf8v uf[8], kf[8];
            auto rdk = [&](int sl) { const unsigned ad = ka[sl < 4 ? 0 : sl < 6 ? 1 : 2] + 512u * sl; kf[sl] = cat(trr(ad + 8u), trr(ad)); };
            auto rdu = [&](int j) { uf[j] = asbf(ldsr<u4v>(ua[j >= 4 ? 0 : j >= 2 ? 1 : 2] + 512u * j)); };
#pragma unroll
            for (int sl = 0; sl < 8; ++sl) { rdk(sl); rdu(7 - sl); }
            f4v A[3] = {}, B[5] = {};
            const unsigned fo = WS_FH + (rb * 14 + cb) * 128;
#pragma nounroll
            for (int rho = 0; rho < 7; ++rho) {
#pragma unroll
                for (int i = 0; i < 3; ++i) { ka[i] += 64u; ua[i] -= 64u; asm volatile("" : "+v"(ka[i]), "+v"(ua[i])); }
#pragma unroll
                for (int sl = 0; sl < 8; ++sl) {
                    SB();
                    const int ia = sl < 4 ? 0 : sl < 6 ? 1 : 2, ib = sl < 3 ? 0 : sl - 2;
                    A[ia] = __builtin_amdgcn_mfma_f32_16x16x32_bf16(kf[sl], uf[7 - sl], A[ia], 0, 0, 0);
                    if (sl < 7) B[ib] = __builtin_amdgcn_mfma_f32_16x16x32_bf16(kf[sl], uf[6 - sl], B[ib], 0, 0, 0);
                    SB();
                    rdk(sl); rdu(7 - sl);
                }
                SB();
                if (rho == PUB_RHO) {
                    WAIT_V(0);
                    arrive_publish(L_CNT, ws, fo, tag);
                }
            }
#pragma unroll
            for (int sl = 0; sl < 8; ++sl) {
                const int ia = sl < 4 ? 0 : sl < 6 ? 1 : 2, ib = sl < 3 ? 0 : sl - 2;
                A[ia] = __builtin_amdgcn_mfma_f32_16x16x32_bf16(kf[sl], uf[7 - sl], A[ia], 0, 0, 0);
                if (sl < 7) B[ib] = __builtin_amdgcn_mfma_f32_16x16x32_bf16(kf[sl], uf[6 - sl], B[ib], 0, 0, 0);
            }
            SB();
            f4v y[4];
            if (typ == 0) { y[0] = A[0] + A[1] + A[2]; y[1] = (B[0] + B[1]) + (B[2] + B[3]) + B[4]; y[2] = y[0]; y[3] = y[1]; }
            else if (typ == 1) { y[0] = A[0] + A[1]; y[1] = B[0] + B[1] + B[2]; y[2] = A[2]; y[3] = B[4]; }
            else { y[0] = A[0]; y[1] = B[0]; y[2] = A[1] + A[2]; y[3] = B[2] + B[3] + B[4]; }
#pragma unroll
            for (int i = 0; i < 4; ++i) {
                const int c = i < 2 ? c1 : c2, tau = (i < 2 ? 7 - 2 * typ : 2 * typ - 1) - (i & 1);
                const bool on = c < nch && (i < 2 || (typ != 0 && w != 7));
                const u2v uv = ldsr<u2v>(L_UA + c * UA_CH + UA_PAD + 512 * max(tau, 0) + 32 * l16 + 8 * g);
                const float dk = c == 0 ? bflo(dsk01) : c == 1 ? bfhi(dsk01) : bflo(dsk2);
                const u2v o = {pk2(y[i][0] + dk * bflo(uv[0]), y[i][1] + dk * bfhi(uv[0])), pk2(y[i][2] + dk * bflo(uv[1]), y[i][3] + dk * bfhi(uv[1]))};
                __builtin_amdgcn_raw_buffer_store_b64(o, wsr, 32 * l16 + 8 * g, (WS_YT + (ch0 + c) * 4096 + 512 * max(tau, 0)) | oob_if(!on), 16);
            }
        }
        STAMP(5);
    } else {
        const int a = (r - 28) * 8 + xcd;
        const u4v rx = mkrsrc(x, 2048 * 1280);
        {
            unsigned vo[5];
#pragma unroll
            for (int i = 0; i < 5; ++i) {
                const int n = i * 64 + lane, rl = n / 80, cp = n - rl * 80;
                vo[i] = (a * 64 + 8 * w + rl) * 1280 + ((cp ^ (2 * w) ^ ((rl >> 1) << 3)) << 4);
                dma16(rx, vo[i], 0, L_AX + (w * 10 + i) * 1024);
            }
#pragma unroll
            for (int i = 5; i < 10; ++i) dma16(rx, vo[i - 5], 4 * 1280, L_AX + (w * 10 + i) * 1024);
        }
        const u64 e0 = ld_flag(ws, WS_E);
        u4v cv[10];
        {
            const auto rw1 = __builtin_amdgcn_make_buffer_rsrc((void*)(u64)w1, 0, 640 * 3456, 0x00020000);
#pragma unroll
            for (int i = 0; i < 10; ++i) cv[i] = __builtin_amdgcn_raw_buffer_load_b128(rw1, ((lane >> 3) * 3456 + (lane & 7) * 16) | oob_if(a >= 27), (80 * w + 8 * i) * 3456 + a * 128, 0);
        }
        if (w == 0) ldsw<unsigned>(L_ACNT + (lane & 7) * 4, 0u);
        STAMP(10);
        WAIT_V(11);
        STAMP(11);
        {
            const int row = 8 * w + (lane >> 3), sw = (2 * w) ^ (((row >> 1) & 1) << 3);
            u4v v[10];
#pragma unroll
            for (int i = 0; i < 10; ++i) v[i] = ldsr<u4v>(L_AX + row * 1280 + (((lane & 7) + 8 * i) ^ sw) * 16);
            SB();
            float s1[2] = {0.f, 0.f}, s2[2] = {0.f, 0.f};
#pragma unroll
            for (int i = 0; i < 10; ++i)
#pragma unroll
                for (int j = 0; j < 4; ++j) { s1[j & 1] = dot2(v[i][j], 0x3f803f80u, s1[j & 1]); s2[j & 1] = dot2(v[i][j], v[i][j], s2[j & 1]); }
            const f2v sv = ln_stats(sum8(s1[0] + s1[1]), sum8(s2[0] + s2[1]));
            ldsw<f2v>(L_AST + row * 8, sv);
            __builtin_amdgcn_raw_buffer_store_b64(__builtin_bit_cast(u2v, sv), wsr, (WS_STATS + (a * 64 + row) * 8) | oob_if(lane & 7), 0, 16);
        }
        BAR_L();
        STAMP(14);
        {
            const int o = lane & 7, cq = lane >> 3;
            f2v rs2[4], nm2[4];
#pragma unroll
            for (int i = 0; i < 4; ++i) { const f4v q = ldsr<f4v>(L_AST + o * 64 + i * 16); rs2[i] = f2v{q[0], q[2]}; nm2[i] = f2v{-q[1], -q[3]}; }
            u2v v[3][8];
#pragma unroll
            for (int k = 0; k < 3; ++k)
#pragma unroll
                for (int t = 0; t < 8; ++t) v[k][t] = ldsr<u2v>(L_AX + (8 * o + t) * 1280 + ((4 * min(w + 8 * k, 19) + (cq >> 1)) ^ ((2 * o) ^ (((t >> 1) & 1) << 3))) * 16 + (cq & 1) * 8);
            SB();
#pragma unroll
            for (int k = 0; k < 3; ++k) {
                if (k < 2 || w < 4) {
#pragma unroll
                    for (int e = 0; e < 4; ++e) {
                        u4v ov;
#pragma unroll
                        for (int p = 0; p < 4; ++p) {
                            const unsigned va = v[k][2 * p][e >> 1], vb = v[k][2 * p + 1][e >> 1];
                            const f2v n2 = __builtin_elementwise_fma((e & 1) ? f2v{bfhi(va), bfhi(vb)} : f2v{bflo(va), bflo(vb)}, rs2[p], nm2[p]);
                            ov[p] = pk2(n2[0], n2[1]);
                        }
                        __builtin_amdgcn_raw_buffer_store_b128(ov, wsr, WS_XT + (32 * (w + 8 * k) + 4 * cq + e) * 4096 + a * 128 + o * 16, 0, 16);
                        SB();
                    }
                }
                if (k == 0) {
                    WAIT_V(4);
                    tag = mk64(__builtin_amdgcn_readfirstlane((unsigned)e0), __builtin_amdgcn_readfirstlane((unsigned)(e0 >> 32))) + 1ull;
                    arrive_publish(L_ACNT + 8, ws, WS_FS + a * 128, tag);
                }
            }
        }
        STAMP(15);
        {
            unsigned selo = 0x00003f80u, sehi = 0x3f800000u;
            asm volatile("" : "+v"(selo), "+v"(sehi));
            float sm[8] = {0.f, 0.f, 0.f, 0.f, 0.f, 0.f, 0.f, 0.f};
#pragma unroll
            for (int i = 0; i < 10; ++i)
#pragma unroll
                for (int j = 0; j < 4; ++j) { sm[2 * j] = dot2(cv[i][j], selo, sm[2 * j]); sm[2 * j + 1] = dot2(cv[i][j], sehi, sm[2 * j + 1]); }
#pragma unroll
            for (int j = 0; j < 8; ++j) sm[j] = dpp_add<0x128>(sm[j]);
            ldsw<f4v>(L_ACS + (4 * w + g) * 256 + (lane & 7) * 32 + (lane & 8) * 2, (lane & 8) ? f4v{sm[4], sm[5], sm[6], sm[7]} : f4v{sm[0], sm[1], sm[2], sm[3]});
        }
        BAR_L();
        if (w != 7 || a >= 27) {
            WAIT_V(0);
            arrive_publish(L_ACNT + 12, ws, WS_FA + a * 128, tag);
        } else {
            const float bf = bf2f(b1[64 * a + lane]);
            float p[32], sum = 0.f;
#pragma unroll
            for (int i = 0; i < 32; ++i) p[i] = ldsr<float>(L_ACS + i * 256 + lane * 4);
#pragma unroll
            for (int i = 0; i < 32; i += 4) sum += (p[i] + p[i + 1]) + (p[i + 2] + p[i + 3]);
            __builtin_amdgcn_raw_buffer_store_b32(__builtin_bit_cast(unsigned, sum), wsr, WS_CS + (64 * a + lane) * 4, 0, 16);
            __builtin_amdgcn_raw_buffer_store_b32(__builtin_bit_cast(unsigned, bf), wsr, WS_B1 + (64 * a + lane) * 4, 0, 16);
            WAIT_V(0);
            raise_flag(ws, WS_FC + a * 128, tag);
            arrive_publish(L_ACNT + 12, ws, WS_FA + a * 128, tag);
        }
        STAMP(2);
        if (w < 2) wait_flags(ws, WS_FW + (2 * min(54 * w + lane, 107) + (cb2 >> 2)) * 128, tag);
        BAR_L();
        STAMP(3);
        stage_x();
#pragma unroll
        for (int n = 0; n < 30; ++n) loadb(n);
        __builtin_amdgcn_s_waitcnt(0x0F70);
    }

    asm volatile("" : "+s"(rot2));
    if (w == 0) wait_flags(ws, WS_FH + ((rb2 >> 1) * 14 + min(lane, 13)) * 128, tag);
    BAR_L();
    STAMP(6);
    u4v ha[7][4];
    auto loada = [&](int i, int t) { ha[i][t] = __builtin_amdgcn_raw_buffer_load_b128(wsr, lane * 16, WS_HA + ((rb2 * 54 + sidx(i)) * 4 + t) * 1024, 16); };
#pragma unroll
    for (int i = 0; i < 8; ++i) loada(i >> 2, i & 3);
    if (is_gemm) {
        WAIT_V(8);
        arrive_publish(L_CNT + 4, ws, WS_FY + bid * 128, tag);
    }
    const int c0 = 80 * cb2;
    int lz = lane;
    asm volatile("" : "+v"(lz));
    const int cA = c0 + 10 * w, cB = cA + 9;
    const int own0 = max((cA * 0xaaab) >> 17, (cA - 192) >> 1), own1 = max((cB * 0xaaab) >> 17, (cB - 192) >> 1);
    u64 ypoll = tag;
    u4v y0, y1;
    f4v acc2[4][5];
#pragma unroll
    for (int i = 0; i < 20; ++i) acc2[i / 5][i % 5] = f4v{};
#pragma unroll
    for (int i = 0; i < 5; ++i) {
#pragma unroll
        for (int t = 0; t < 4; ++t) {
            SB();
#pragma unroll
            for (int c = 0; c < 5; ++c) acc2[t][c] = __builtin_amdgcn_mfma_f32_16x16x32_bf16(asbf(ha[i][t]), asbf(bw[5 * i + c]), acc2[t][c], 0, 0, 0);
            SB();
            if (i > 0) loada(i + 2, t);
            else if (t == 3) { loada(2, 0); loada(2, 1); loada(2, 2); loada(2, 3); }
            if (i == 1 || (i == 2 && t == 0)) loadb(30 + 4 * (i - 1) + t);
            if (i == 3 && t == 0) ypoll = ld_flag(ws, WS_FY + min(own0 + lz, own1) * 128);
            if (i == 4 && t == 3) {
                if (!__all(ypoll == tag)) wait_flags(ws, WS_FY + min(own0 + lz, own1) * 128, tag);
                y0 = __builtin_amdgcn_raw_buffer_load_b128(wsr, (lz >> 3) * 4096 + (lz & 7) * 16, WS_YT + cA * 4096 + rb2 * 128, 16);
                y1 = __builtin_amdgcn_raw_buffer_load_b128(wsr, (unsigned)((8 + (lz >> 3)) * 4096 + (lz & 7) * 16) | oob_if(lz >= 16), WS_YT + cA * 4096 + rb2 * 128, 16);
            }
        }
    }
    STAMP(7);
    auto dump = [&](int t) {
        unsigned p[10];
#pragma unroll
        for (int c = 0; c < 5; ++c) { p[2 * c] = pk2(acc2[t][c][0], acc2[t][c][1]); p[2 * c + 1] = pk2(acc2[t][c][2], acc2[t][c][3]); }
        addtid10(L_RED + w * RED_W + t * 5 * RED_T, p);
    };
#pragma unroll
    for (int t = 0; t < 4; ++t) {
        SB();
#pragma unroll
        for (int c = 0; c < 5; ++c) acc2[t][c] = __builtin_amdgcn_mfma_f32_16x16x32_bf16(asbf(ha[5][t]), asbf(bw[25 + c]), acc2[t][c], 0, 0, 0);
        SB();
        if (t > 0) dump(t - 1);
        SB();
#pragma unroll
        for (int c = 0; c < 5; ++c) acc2[t][c] = __builtin_amdgcn_mfma_f32_16x16x32_bf16(asbf(ha[6][t]), asbf(bw[30 + c]), acc2[t][c], 0, 0, 0);
    }
    SB();
    ldsw<u4v>(L_YS + (10 * w + (lz >> 3)) * YS_ROW + (lz & 7) * 16, y0);
    ldsw<u4v>(L_YS + min(10 * w + 8 + (lz >> 3) + (lz >= 16 ? 80 : 0), 80) * YS_ROW + (lz & 7) * 16, y1);
    dump(3);
    BAR_L();
    STAMP(8);
    {
        unsigned selo = 0x00003f80u, sehi = 0x3f800000u;
        asm volatile("" : "+v"(selo), "+v"(sehi));
#pragma unroll
        for (int k = 0; k < 3; ++k) {
            if (k < 2 || w < 4) {
                const int id = w * 64 + lz + 512 * k, tp = id / 40, c2 = id - tp * 40;
                const unsigned ra = L_RED + ((tp >> 3) * 5 + (c2 >> 3)) * RED_T + (tp & 1) * RED_ROW + ((tp >> 1) & 3) * 64 + (c2 & 7) * 8;
                u2v p[8];
#pragma unroll
                for (int j = 0; j < 8; ++j) p[j] = ldsr<u2v>(ra + j * RED_W);
                const unsigned ya = ldsr<unsigned>(L_YS + 2 * c2 * YS_ROW + tp * 4), yb = ldsr<unsigned>(L_YS + (2 * c2 + 1) * YS_ROW + tp * 4);
                const unsigned xa = ldsr<unsigned>(L_XS + tp * 320 + c2 * 4), xb = ldsr<unsigned>(L_XS + tp * 320 + 160 + c2 * 4), bv = ldsr<unsigned>(L_XS + 10240 + c2 * 4);
                float o00 = bflo(ya) + bflo(xa) + bflo(bv), o01 = bflo(yb) + bfhi(xa) + bfhi(bv);
                float o10 = bfhi(ya) + bflo(xb) + bflo(bv), o11 = bfhi(yb) + bfhi(xb) + bfhi(bv);
#pragma unroll
                for (int j = 0; j < 8; ++j) {
                    o00 = dot2(p[j][0], selo, o00); o10 = dot2(p[j][0], sehi, o10);
                    o01 = dot2(p[j][1], selo, o01); o11 = dot2(p[j][1], sehi, o11);
                }
                GP(unsigned) op = (GP(unsigned))(out + (rb2 * 64 + 2 * tp) * 640 + c0 + 2 * c2);
                op[0] = pk2(o00, o01);
                op[320] = pk2(o10, o11);
            }
        }
    }
    if (bid == 0 && w == 5) {
#pragma unroll
        for (int i = 0; i < 4; ++i) wait_flags(ws, WS_FH + (i * 64 + lz) * 128, tag);
        raise_flag(ws, WS_E, tag);
    }
    STAMP(9);
    if (w == 0 && bid == 5) { u64 c1 = __builtin_amdgcn_s_memtime(); *(u64*)(ws + WS_STAMP + 400000) = c1 - cyc0; *(u64*)(ws + WS_STAMP + 400008) = st[9] - st[0]; }
    for (int i = 0; i < NSTAMP; ++i) *(u64*)(ws + WS_STAMP + ((bid * 8 + w) * NSTAMP + i) * 8) = st[i];
}

__global__ void k_pre(u64* p) { p[0] = __builtin_amdgcn_s_memrealtime(); }
__global__ void k_null(u64* p) { if (threadIdx.x == 0 && blockIdx.x == 0) p[1] = __builtin_amdgcn_s_memrealtime(); }
__global__ void k_spin(const u64* st, const u64* pre, int mode) {
  u64 tnow=__builtin_amdgcn_s_memrealtime();
  u64 mn=~0ull,mx=0; for(int i=0;i<2048;i++){ u64 a=st[i*NSTAMP], b=st[i*NSTAMP+9]; mn=a<mn?a:mn; mx=b>mx?b:mx; }
  u64 d = mode==0 ? mn-pre[0] : mode==1 ? tnow-mx : mode==2 ? pre[1]-pre[0] : mode==3 ? mn-pre[1] : 0;
  d*=100ull;
  if(d>1000000ull) d=1000000ull;
  u64 t0=__builtin_amdgcn_s_memrealtime();
  while(__builtin_amdgcn_s_memrealtime()-t0<d){ __builtin_amdgcn_s_sleep(8); }
}

extern "C" void kernel_launch(void* const* d_in, const int* in_sizes, int n_in, void* d_out, int out_size, void* d_ws, size_t ws_size, hipStream_t stream) {
    (void)in_sizes; (void)n_in; (void)out_size; (void)ws_size;
    (void)hipFuncSetAttribute((const void*)s4ffn_fused, hipFuncAttributeMaxDynamicSharedMemorySize, LDS_BYTES);
    k_pre<<<1,64,0,stream>>>((u64*)((char*)d_ws+WS_STAMP+500000));
    s4ffn_fused<<<256, 512, LDS_BYTES, stream>>>((const u16*)d_in[0], (const u16*)d_in[1], (const u16*)d_in[2], (const u16*)d_in[3], (const u16*)d_in[4],
                                                 (const u16*)d_in[5], (const u16*)d_in[6], (const u16*)d_in[7], (const u16*)d_in[8], (const u16*)d_in[10],
                                                 (const u16*)d_in[11], (const u16*)d_in[12], (const u16*)d_in[13], (u16*)d_out, (char*)d_ws);
    k_spin<<<1,1,0,stream>>>((const u64*)((char*)d_ws+WS_STAMP),(const u64*)((char*)d_ws+WS_STAMP+500000),4);
}
```

```cpp
#define STAMPS
#include <hip/hip_runtime.h>
#include <stdint.h>
#define PUB_RHO 4
#define CSUM_ON 1
#define CODE_CHUNKS 4
#define CODE_BYTES 28672u
#define ARR 0

typedef unsigned short u16;
typedef unsigned long long u64;
typedef short s4v __attribute__((ext_vector_type(4)));
typedef __bf16 bf8v __attribute__((ext_vector_type(8)));
typedef __bf16 bf2v __attribute__((ext_vector_type(2)));
typedef float f2v __attribute__((ext_vector_type(2)));
typedef float f4v __attribute__((ext_vector_type(4)));
typedef float f16v __attribute__((ext_vector_type(16)));
typedef unsigned u2v __attribute__((ext_vector_type(2)));
typedef unsigned short h2v __attribute__((ext_vector_type(2)));
typedef unsigned u4v __attribute__((ext_vector_type(4)));
typedef __attribute__((address_space(1))) u64 gu64;
#define LDSP(T) __attribute__((address_space(3))) T*
#define DI __device__ __forceinline__
#define RLX_AGENT __ATOMIC_RELAXED, __HIP_MEMORY_SCOPE_AGENT

constexpr unsigned WS_E = 0;
constexpr unsigned WS_FH = 4096;
constexpr unsigned WS_FA = 32768;
constexpr unsigned WS_FW = 81920;
constexpr unsigned WS_FS = 40960;
constexpr unsigned WS_FC = 45056;
constexpr unsigned WS_FY = 49152;
constexpr unsigned WS_STATS = 131072;
constexpr unsigned WS_CS = 196608, WS_B1 = 204800;
constexpr unsigned WS_STAMP = 262144;
constexpr unsigned WS_XT = 1u << 20;
constexpr unsigned WS_W2T = 4u << 20;
constexpr unsigned WS_YT = 8u << 20;
constexpr unsigned WS_HA = 12u << 20;
constexpr unsigned WS_BYTES = 20u << 20;
constexpr unsigned SLOT = 32768;
constexpr unsigned L_KC = 98304, KC_CH = 16640, KC_CP = 4160;
constexpr unsigned L_UA = 148224, UA_CH = 4576, UA_PAD = 480;
constexpr unsigned L_MISC = 162368, L_CB = L_MISC, L_CNT = L_MISC + 1024;
constexpr unsigned L_TBL = 65536, TBL_ROW = 544, TBL_CH = 53 * TBL_ROW;
constexpr unsigned L_PRM = 152064, L_TT = 158464, TT_CH = 1296, L_W2U = L_PRM;
constexpr unsigned L_SCR = L_CB;
constexpr unsigned L_XS = 0, L_RED = 16384, RED_ROW = 256, RED_T = 576, RED_W = 20 * RED_T, L_YS = L_RED + 8 * RED_W, YS_ROW = 144;
constexpr unsigned L_AX = 0, L_AST = 81920, L_ACNT = L_AST + 512, L_ACS = L_AST + 1024;
static_assert(L_TT + 3 * TT_CH <= L_MISC && L_CNT + 64 <= 163840 && L_W2U + 10240 <= L_MISC, "LDS map");
constexpr unsigned LDS_BYTES = 163840;
constexpr int SPIN_LIMIT = 400000;

constexpr int NSTAMP = 16;
#define STAMP(i) do { st[i] = __builtin_amdgcn_s_memrealtime(); } while (0)

DI float bf2f(unsigned v) { return __uint_as_float(v << 16); }
DI float bflo(unsigned v) { return __uint_as_float(v << 16); }
DI float bfhi(unsigned v) { return __uint_as_float(v & 0xffff0000u); }
DI unsigned pk2(float a, float b) { bf2v v; v[0] = (__bf16)a; v[1] = (__bf16)b; return __builtin_bit_cast(unsigned, v); }
DI float rbf(float a) { return bflo(pk2(a, a)); }
template <typename P> DI u4v mkrsrc(P p, unsigned bytes) { u64 a = (u64)p; u4v r; r[0] = (unsigned)a; r[1] = (unsigned)(a >> 32); r[2] = bytes; r[3] = 0x00020000u; return r; }
DI void dma16(u4v rs, unsigned voff, unsigned soff, unsigned lds) {
    asm volatile("s_mov_b32 m0, %3\n\ts_nop 0\n\tbuffer_load_dwordx4 %0, %1, %2 offen lds" ::"v"(voff), "s"(rs), "s"(soff), "s"(lds) : "memory");
}
DI void dma16g(const void* p, unsigned lds) {
    asm volatile("s_mov_b32 m0, %1\n\ts_nop 0\n\tglobal_load_lds_dwordx4 %0, off" ::"v"(p), "s"(lds) : "memory");
}
DI void dma16g_lanes3(const void* p, unsigned lds) {
    u64 ex;
    asm volatile("s_mov_b64 %0, exec\n\ts_mov_b64 exec, 7\n\ts_mov_b32 m0, %2\n\ts_nop 0\n\tglobal_load_lds_dwordx4 %1, off\n\ts_mov_b64 exec, %0" : "=&s"(ex) : "v"(p), "s"(lds) : "memory");
}
DI void dma16_lanes16(u4v rs, unsigned voff, unsigned soff, unsigned lds) {
    u64 ex;
    asm volatile("s_mov_b64 %0, exec\n\ts_mov_b64 exec, 0xffff\n\ts_mov_b32 m0, %4\n\ts_nop 0\n\tbuffer_load_dwordx4 %1, %2, %3 offen lds\n\ts_mov_b64 exec, %0"
                 : "=&s"(ex) : "v"(voff), "s"(rs), "s"(soff), "s"(lds) : "memory");
}
DI void touch_code(unsigned chunk, int lane, unsigned lds) {
    u64 pc;
    asm volatile("s_getpc_b64 %0" : "=s"(pc));
    const u64 p = (pc & ~127ull) + min(chunk * 8192u + lane * 128u, CODE_BYTES - 128u);
    asm volatile("s_mov_b32 m0, %1\n\ts_nop 0\n\tglobal_load_lds_dword %0, off" ::"v"(p), "s"(lds) : "memory");
}
#define SB() __builtin_amdgcn_sched_barrier(0)
#define WAIT_BAR(n) asm volatile("s_waitcnt vmcnt(" #n ") lgkmcnt(0)\n\ts_barrier" ::: "memory")
#define BAR_L() asm volatile("s_waitcnt lgkmcnt(0)\n\ts_barrier" ::: "memory")
#define WAIT_V(n) asm volatile("s_waitcnt vmcnt(" #n ")" ::: "memory")
template <typename T> DI T ldsr(unsigned a) { return *(LDSP(T))a; }
template <typename T> DI void ldsw(unsigned a, T v) { *(LDSP(T))a = v; }
DI void stw(unsigned a, unsigned v, int off) { asm volatile("ds_write_b32 %0, %1 offset:%2" ::"v"(a), "v"(v), "i"(off) : "memory"); }
DI s4v trr(unsigned a) { return __builtin_amdgcn_ds_read_tr16_b64_v4i16((LDSP(s4v))a); }
DI bf8v cat(s4v a, s4v b) { return __builtin_bit_cast(bf8v, __builtin_shufflevector(a, b, 0, 1, 2, 3, 4, 5, 6, 7)); }
DI bf8v asbf(u4v a) { return __builtin_bit_cast(bf8v, a); }
template <int CTRL> DI float dpp_add(float v) { return v + __builtin_bit_cast(float, __builtin_amdgcn_update_dpp(0, __builtin_bit_cast(int, v), CTRL, 0xf, 0xf, true)); }
DI float sum8(float v) { return dpp_add<0x141>(dpp_add<0x4e>(dpp_add<0xb1>(v))); }
DI float dot2(unsigned a, unsigned b, float c) { return __builtin_amdgcn_fdot2_f32_bf16(__builtin_bit_cast(bf2v, a), __builtin_bit_cast(bf2v, b), c, false); }
DI u64 ld_flag(const char* ws, unsigned off) { return __hip_atomic_load((gu64*)(ws + off), RLX_AGENT); }
DI void wait_flags(const char* ws, unsigned off, u64 tag) {
#pragma nounroll
    for (int it = 0; it < SPIN_LIMIT; ++it) {
        if (__all(ld_flag(ws, off) == tag)) break;
        __builtin_amdgcn_s_sleep(1);
    }
    asm volatile("" ::: "memory");
}
DI unsigned lds_count(unsigned cnt_lds) {
    unsigned old, one = 1u;
    u64 ex;
    asm volatile("s_mov_b64 %1, exec\n\ts_mov_b64 exec, 1\n\ts_waitcnt lgkmcnt(0)\n\tds_add_rtn_u32 %0, %2, %3\n\ts_waitcnt lgkmcnt(0)\n\ts_mov_b64 exec, %1"
                 : "=&v"(old), "=&s"(ex) : "v"(cnt_lds), "v"(one) : "memory");
    return __builtin_amdgcn_readfirstlane(old);
}
DI void raise_flag(char* ws, unsigned flag_off, u64 tag) { __hip_atomic_store((gu64*)(ws + flag_off), tag, RLX_AGENT); }
DI void arrive_publish(unsigned cnt_lds, char* ws, unsigned flag_off, u64 tag, unsigned last = 7u) {
    if (lds_count(cnt_lds) == last) raise_flag(ws, flag_off, tag);
}
DI void lds_wait(unsigned cnt_lds, unsigned n) {
#pragma nounroll
    for (int it = 0; it < SPIN_LIMIT; ++it) {
        if (__builtin_amdgcn_readfirstlane(*(volatile LDSP(unsigned))cnt_lds) >= n) break;
        __builtin_amdgcn_s_sleep(1);
    }
    asm volatile("" ::: "memory");
}
DI unsigned oob_if(bool c) { return c ? 0x80000000u : 0u; }
DI f2v ln_stats(float s1, float s2) {
    const float mean = s1 * (1.f / 640.f), var = fmaxf(__builtin_fmaf(-mean, mean, s2 * (1.f / 640.f)), 0.f);
    const float rs = __builtin_amdgcn_rcpf(rbf(__builtin_amdgcn_sqrtf(rbf(rbf(var) + 1e-5f))));
    return f2v{rs, rbf(mean) * rs};
}
DI f2v cmul_a(f2v a, f2v b) { f2v t; asm("v_pk_mul_f32 %0, %1, %2 op_sel_hi:[0,1]" : "=v"(t) : "v"(a), "v"(b)); return t; }
DI f2v cmul_b(f2v a, f2v b, f2v t) {
    f2v o;
    asm("v_pk_fma_f32 %0, %1, %2, %3 op_sel:[1,1,0] op_sel_hi:[1,0,1] neg_lo:[0,1,0]" : "=v"(o) : "v"(a), "v"(b), "v"(t));
    return o;
}
DI f2v cmul(f2v a, f2v b) { return cmul_b(a, b, cmul_a(a, b)); }
DI f2v cexp_p(float are, float aim, float p) {
    const float m = __builtin_amdgcn_exp2f(are * p), an = __builtin_amdgcn_fractf(aim * p);
    return f2v{m * __builtin_amdgcn_cosf(an), m * __builtin_amdgcn_sinf(an)};
}

DI void addtid10(unsigned m0v, const unsigned (&p)[10]) {
    asm volatile("s_mov_b32 m0, %10\n\ts_nop 0\n\t"
                 "ds_write_addtid_b32 %0 offset:%11\n\tds_write_addtid_b32 %1 offset:%12\n\tds_write_addtid_b32 %2 offset:%13\n\tds_write_addtid_b32 %3 offset:%14\n\t"
                 "ds_write_addtid_b32 %4 offset:%15\n\tds_write_addtid_b32 %5 offset:%16\n\tds_write_addtid_b32 %6 offset:%17\n\tds_write_addtid_b32 %7 offset:%18\n\t"
                 "ds_write_addtid_b32 %8 offset:%19\n\tds_write_addtid_b32 %9 offset:%20"
                 ::"v"(p[0]), "v"(p[1]), "v"(p[2]), "v"(p[3]), "v"(p[4]), "v"(p[5]), "v"(p[6]), "v"(p[7]), "v"(p[8]), "v"(p[9]), "s"(m0v),
                   "i"(0), "i"(RED_ROW), "i"(RED_T), "i"(RED_T + RED_ROW), "i"(2 * RED_T), "i"(2 * RED_T + RED_ROW), "i"(3 * RED_T), "i"(3 * RED_T + RED_ROW),
                   "i"(4 * RED_T), "i"(4 * RED_T + RED_ROW) : "memory");
}

#define GP(T) __attribute__((address_space(1))) T*
typedef unsigned u8v __attribute__((ext_vector_type(8)));
DI u64 mk64(unsigned lo, unsigned hi) { return ((u64)hi << 32) | lo; }
__global__ __launch_bounds__(512, 2) void s4ffn_fused(const u16*, const u16*, const u16*, const u16*, const u16*, const u16*, const u16*, const u16*, const u16*,
                                                      const u16*, const u16*, const u16*, const u16*, u16*, char*) {
    const int tid = threadIdx.x, lane = tid & 63, w = __builtin_amdgcn_readfirstlane(tid >> 6);
    const int xcd = blockIdx.x & 7, r = ((blockIdx.x >> 3) + 28) & 31, bid = r * 8 + xcd;
    const bool is_gemm = r < 28;
    u64 st[NSTAMP];
    for (int i = 0; i < NSTAMP; ++i) st[i] = 0;
    u64 cyc0 = __builtin_amdgcn_s_memtime();
    STAMP(0);
    if (w < CODE_CHUNKS && ((r + w) & 3) == 0) touch_code(w, lane, 163840 - 256);
    u64 a_x, a_prm;
    u8v a_m;
    u4v a_t;
    asm volatile("s_load_dwordx2 %0, %4, 0x0\n\ts_load_dwordx2 %1, %4, %5 offset:0x8\n\ts_load_dwordx8 %2, %4, 0x48\n\ts_load_dwordx4 %3, %4, 0x68\n\ts_waitcnt lgkmcnt(0)"
                 : "=&s"(a_x), "=&s"(a_prm), "=&s"(a_m), "=&s"(a_t) : "s"(__builtin_amdgcn_kernarg_segment_ptr()), "s"(8 * w));
    const GP(u16) x = (const GP(u16))a_x;
    const char* prm = (const char*)a_prm;
    const GP(u16) w1 = (const GP(u16))mk64(a_m[0], a_m[1]);
    const GP(u16) b1 = (const GP(u16))mk64(a_m[2], a_m[3]);
    const GP(u16) w2 = (const GP(u16))mk64(a_m[4], a_m[5]);
    const GP(u16) b2 = (const GP(u16))mk64(a_m[6], a_m[7]);
    GP(u16) out = (GP(u16))mk64(a_t[0], a_t[1]);
    char* ws = (char*)mk64(a_t[2], a_t[3]);
    u64 tag;
    const auto wsr = __builtin_amdgcn_make_buffer_rsrc((void*)ws, 0, WS_BYTES, 0x00020000);
    const int nch = bid < 192 ? 3 : 2, ch0 = 3 * bid - max(bid - 192, 0);
    const int g = lane >> 4, l16 = lane & 15;
    const int rb2 = xcd * 4 + (r & 3), cb2 = r >> 2;
    const bool has7 = w < 6;
    u4v bw[35];
    int rot2 = 7 * cb2 + 14 * (r & 3) + w;
    rot2 = rot2 >= 54 ? rot2 - 54 : rot2;
    auto sidx = [&](int i) { const int s = rot2 + 8 * i; return s >= 54 ? s - 54 : s; };
    auto loadb = [&](int n) {
        bw[n] = __builtin_amdgcn_raw_buffer_load_b128(wsr, lane * 16 | oob_if(n >= 30 && !has7), WS_W2T + ((cb2 * 54 + sidx(n / 5)) * 5 + n % 5) * 1024, 16);
    };

    auto stage_x = [&]() {
        const u4v rx = mkrsrc(x, 2048 * 1280);
        const int n1 = 64 + (lane & 15);
        dma16(rx, (lane / 10) * 1280 + (lane % 10) * 16, (rb2 * 64 + 8 * w) * 1280 + cb2 * 160, L_XS + w * 1280);
        dma16_lanes16(rx, (n1 / 10) * 1280 + (n1 % 10) * 16, (rb2 * 64 + 8 * w) * 1280 + cb2 * 160, L_XS + w * 1280 + 1024);
        if (w == 0) dma16_lanes16(mkrsrc(b2, 1280), min(lane, 9) * 16, cb2 * 160, L_XS + 10240);
    };
    if (__builtin_expect(is_gemm, 1)) {
        if (w < 6) dma16g(prm + (ch0 + min(g, nch - 1)) * 256 + l16 * 16, L_PRM + w * 1024);
        else dma16g_lanes3(lane == 2 ? (const char*)ws + WS_E : prm + min(((ch0 * 2) & ~15) + lane * 16, 1264), L_SCR + (w - 6) * 64);
        SB();
        const int gi = r & 3, gj = r >> 2, rb = (xcd >> 1) * 4 + gi, cb = (xcd & 1) * 7 + gj;
        const int fw = w & 3, tw = w >> 2;
        const u4v rx = mkrsrc(x, 2048 * 1280), rw = mkrsrc(w1, 640 * 3456);
        unsigned vx[2];
#pragma unroll
        for (int u = 0; u < 2; ++u) vx[u] = (rb * 128 + 16 * w + 8 * u + (lane >> 3)) * 1280 + (((lane & 7) ^ (4 * u + g)) << 4);
        const int wc = l16 ^ (2 * g) ^ ((w & 1) << 3);
        const unsigned vw = (unsigned)((8 * w + g) * 3456 + cb * 256 + wc * 16) | (cb == 13 ? (unsigned)(wc & 8) << 28 : 0u);
        int rot = gj + 3 * gi + xcd;
        rot = rot >= 20 ? rot - 20 : rot >= 10 ? rot - 10 : rot;
        auto stage1w = [&](int t, int slot, int p, int dw) {
            int kt = t + rot; kt = kt >= 10 ? kt - 10 : kt;
            const unsigned lb = slot * SLOT + (w + dw) * 2048 + (p >> 1) * 1024;
            if (p & 1) dma16(rw, vw + dw * 8 * 3456, kt * (64 * 3456) + (p >> 1) * 4 * 3456, lb + 16384);
            else dma16(rx, vx[p >> 1] + dw * 16 * 1280, kt * 128, lb);
        };
        auto stage1 = [&](int t, int slot, int p) { stage1w(t, slot, p, 0); };
#pragma unroll
        for (int i = 0; i < 4; ++i) stage1(0, 0, i);
        u4v w2a, w2b;
        {
            const auto rw2 = __builtin_amdgcn_make_buffer_rsrc((void*)(u64)w2, 0, 1728 * 1280, 0x00020000);
            const unsigned so2 = ((bid >> 2) * 32 + ((bid >> 1) & 1) * 8 + (w >> 2) * 16 + (w & 3) * 2) * 1280 + (bid & 1) * 640;
            w2a = __builtin_amdgcn_raw_buffer_load_b128(rw2, lane * 16 + (lane >= 40 ? 640 : 0), so2, 0);
            w2b = __builtin_amdgcn_raw_buffer_load_b128(rw2, (1280 + 384 + lane * 16) | oob_if(lane >= 16), so2, 0);
        }
        SB();
        unsigned ix[12];
        {
            const int l0 = 8 * (tid & 255) - 3;
            const float lf = (float)l0;
            const unsigned kb = L_TT + tw * TT_CH, kc = kb - 2u * (0x4300 - 128);
#pragma unroll
            for (int i = 0; i < 6; ++i) {
                const unsigned c2 = pk2(lf + (float)(2 * i), lf + (float)(2 * i + 1));
                asm("v_mad_u32_u16 %0, %1, 2, %2" : "=v"(ix[2 * i]) : "v"(c2), "s"(kc));
                asm("v_mad_u32_u16 %0, %1, 2, %2 op_sel:[1,0,0,0]" : "=v"(ix[2 * i + 1]) : "v"(c2), "s"(kc));
            }
            if (fw == 0) {
#pragma unroll
                for (int i = 0; i < 11; ++i) {
                    const int l = l0 + i;
                    const unsigned lo = kb + 2u * min((unsigned)l, 641u), m = (unsigned)((l - 128) >> 31);
                    ix[i] = (lo & m) | (ix[i] & ~m);
                }
            }
#pragma unroll
            for (int i = 0; i < 11; ++i) asm volatile("" : "+v"(ix[i]));
        }
        if (w == 0) {
            ldsw<unsigned>(L_CNT + (lane & 7) * 4, 0u);
        }
        STAMP(10);
        WAIT_BAR(6);
        STAMP(11);
        const unsigned so = (ch0 * 2) & 15;
        { const u2v tv = ldsr<u2v>(L_SCR + 32); tag = mk64(__builtin_amdgcn_readfirstlane(tv[0]), __builtin_amdgcn_readfirstlane(tv[1])) + 1ull; }
        const unsigned dsk01 = __builtin_amdgcn_readfirstlane(ldsr<u16>(L_SCR + 64 + so) | ((unsigned)ldsr<u16>(L_SCR + 64 + so + 2) << 16));
        const unsigned dsk2 = __builtin_amdgcn_readfirstlane(ldsr<u16>(L_SCR + 64 + so + 4));
        {
            const int hc = w < 6 ? w : w - 2, c = hc >> 1, mode = (hc & 1) * 64 + lane;
            const float ldt = bf2f(ldsr<u16>(L_SCR + so + 2 * c));
            const unsigned pb = L_PRM + c * 256 + mode * 2;
            const float pa = bf2f(ldsr<u16>(pb)), pi = bf2f(ldsr<u16>(pb + 1024));
            const f2v Bc = {bf2f(ldsr<u16>(pb + 2048)), bf2f(ldsr<u16>(pb + 3072))}, Cc = {bf2f(ldsr<u16>(pb + 4096)), bf2f(ldsr<u16>(pb + 5120))};
            lds_count(L_CNT + 12);
            if (w >= 4) {
#pragma unroll
                for (int i = 0; i < 8; ++i) stage1w(1, 1, i & 3, i < 4 ? 0 : -4);
            }
            if (c < nch) {
                const float dt = rbf(__builtin_amdgcn_exp2f(ldt * 1.4426950408889634f));
                const float are = dt * pa * 1.4426950408889634f, aim = dt * pi * 0.15915494309189535f;
                const unsigned tb = L_TBL + c * TBL_CH + mode * 4;
                if (w < 6) {
                    const f2v z8 = cexp_p(are, aim, 8.f), z16 = cexp_p(are, aim, 16.f), z64 = cexp_p(are, aim, 64.f);
                    f2v e[8];
                    e[0] = f2v{1.f, 0.f}; e[1] = z8; e[2] = z16; e[3] = cmul(z16, z8); e[4] = cmul(z16, z16);
                    e[5] = cmul(e[3], z16); e[6] = cmul(e[4], z16); e[7] = cmul(e[4], e[3]);
#pragma unroll
                    for (int i = 0; i < 4; ++i) {
                        f2v t[8];
#pragma unroll
                        for (int j = 0; j < 8; ++j) {
                            if (i < 3) t[j] = cmul_a(e[j], z64);
                            stw(tb, pk2(e[j][0], e[j][1]), (21 + 8 * i + j) * TBL_ROW);
                        }
                        if (i < 3) {
#pragma unroll
                            for (int j = 0; j < 8; ++j) e[j] = cmul_b(e[j], z64, t[j]);
                        }
                    }
                }
                if (w < 4 || w >= 6) {
                    const f2v z1 = cexp_p(are, aim, 1.f), z2 = cexp_p(are, aim, 2.f), z4 = cexp_p(are, aim, 4.f);
                    const f2v z256 = cexp_p(are, aim, 256.f), z1024 = cexp_p(are, aim, 1024.f);
                    const f2v W = cmul(Cc, Bc * dt);
                    auto putx = [&](int row, f2v v) { stw(tb, pk2(v[0], -v[1]), row * TBL_ROW); };
                    const f2v x1 = cmul(W, z1), x2 = cmul(W, z2), x8 = cmul(W, z256), x16 = cmul(W, z1024);
                    const f2v x3 = cmul(x1, z2), x9 = cmul(x8, z2), x12 = cmul(x8, z256), x17 = cmul(x16, z256), x20 = cmul(x16, z1024);
                    const f2v x14 = cmul(x12, z256), x18 = cmul(x17, z256);
                    putx(0, W); putx(1, x1); putx(2, x2); putx(3, x3); putx(8, x8); putx(9, x9); putx(12, x12); putx(14, x14);
                    putx(16, x16); putx(17, x17); putx(18, x18); putx(20, x20);
                    putx(4, cmul(W, z4)); putx(5, cmul(x1, z4)); putx(6, cmul(x2, z4)); putx(7, cmul(x3, z4));
                    putx(10, cmul(x8, z4)); putx(11, cmul(x9, z4)); putx(13, cmul(x12, z4)); putx(15, cmul(x14, z4)); putx(19, cmul(x18, z256));
                }
            }
        }
        lds_wait(L_CNT + 12, 8u);
        ldsw<u4v>(L_W2U + w * 1280 + lane * 16, w2a);
        ldsw<u4v>(lane < 16 ? L_W2U + w * 1280 + 1024 + lane * 16 : L_CNT + 48, w2b);
        STAMP(13);
        BAR_L();
        if (w >= 6) {
            const unsigned tl = L_W2U + (4 * (g & 1) + (l16 >> 2)) * 640 + (lane >> 5) * 32 + (lane & 3) * 8 + (w - 6) * 320;
            bf8v f[5];
#pragma unroll
            for (int k = 0; k < 5; ++k) f[k] = cat(trr(tl + k * 64), trr(tl + k * 64 + 8 * 640));
            lds_count(L_CNT + 16);
#pragma unroll
            for (int k = 0; k < 5; ++k) {
                const int P = 20 * (bid & 1) + 10 * (w - 6) + 2 * k + (lane >> 5), cbb = (P * 52) >> 8, cfr = P - cbb * 5;
                __builtin_amdgcn_raw_buffer_store_b128(__builtin_bit_cast(u4v, f[k]), wsr,
                    (unsigned)(WS_W2T + (bid >> 2) * 5120 + ((bid >> 1) & 1) * 512 + (cbb * 270 + cfr) * 1024 + (lane & 31) * 16) | oob_if(bid >= 216), 0, 16);
            }
        }
        if (w < 2 * nch) {
            const int c = w >> 1, col = 16 * (w & 1) + l16;
            const unsigned ta = L_TBL + c * TBL_CH + l16 * TBL_ROW + g * 16;
            const unsigned te = ta + (21 + 16 * (w & 1)) * TBL_ROW;
            u4v fa[8], fb[8], fe[8];
#pragma unroll
            for (int s = 0; s < 8; ++s) { fe[s] = ldsr<u4v>(te + s * 64); fa[s] = ldsr<u4v>(ta + s * 64); fb[s] = ldsr<u4v>(ta + 16 * TBL_ROW + s * 64); }
            const unsigned tt = L_TT + c * TT_CH;
            const unsigned ta0 = tt + ((col << (3 - max(g - 1, 0))) + ((0x30200100u >> (8 * g)) & 0xffu) * (g == 1 ? 4u : 8u)) * 2;
            const unsigned ta1 = ta0 + (g < 3 ? 4 : 128);
            const unsigned tb0 = tt + (g == 0 ? 512 + col : 644) * 2, st1 = g == 0 ? 64 : 0;
            const unsigned tb1 = g == 1 && col < 2 ? tt + (640 + col) * 2 : tb0;
            SB();
            f4v k0 = {}, k1 = {};
#pragma unroll
            for (int s = 0; s < 8; ++s) {
                k0 = __builtin_amdgcn_mfma_f32_16x16x32_bf16(asbf(fa[s]), asbf(fe[s]), k0, 0, 0, 0);
                k1 = __builtin_amdgcn_mfma_f32_16x16x32_bf16(asbf(fb[s]), asbf(fe[s]), k1, 0, 0, 0);
            }
            lds_wait(L_CNT + 16, 2u);
            ldsw<unsigned>(ta0, pk2(k0[0], k0[1]));
            ldsw<unsigned>(ta1, pk2(k0[2], k0[3]));
            const unsigned q01 = pk2(k1[0], k1[1]), q23 = pk2(k1[2], k1[3]);
            ldsw<u16>(tb1, g == 1 && col == 1 ? (u16)0 : (u16)q01);
            ldsw<u16>(tb0 + st1, (u16)(q01 >> 16));
            ldsw<u16>(tb0 + 2 * st1, (u16)q23);
            ldsw<u16>(tb0 + 3 * st1, (u16)(q23 >> 16));
        }
        BAR_L();
        STAMP(14);
        {
            const bool two = w < 4 && nch == 3;
            const int t = tid & 255;
#pragma unroll
            for (int k = 0; k < 2; ++k) {
                if (k == 0 || two) {
                    unsigned pe[6], po[5];
#pragma unroll
                    for (int i = 0; i < 5; ++i) pe[i] = __builtin_bit_cast(unsigned, h2v{ldsr<u16>(ix[2 * i] + k * 2 * TT_CH), ldsr<u16>(ix[2 * i + 1] + k * 2 * TT_CH)});
                    pe[5] = ldsr<u16>(ix[10] + k * 2 * TT_CH);
                    if (k == 0) { stage1(2, 2, 0); stage1(2, 2, 1); stage1(2, 2, 2); stage1(2, 2, 3); }
#pragma unroll
                    for (int i = 0; i < 5; ++i) po[i] = __builtin_amdgcn_alignbit(pe[i + 1], pe[i], 16);
                    const unsigned cbase = L_KC + (tw + 2 * k) * KC_CH, d = cbase + 32 + t * 16;
                    ldsw<u4v>(d, u4v{po[1], po[2], po[3], po[4]});
                    ldsw<u4v>(d + KC_CP, u4v{pe[1], pe[2], pe[3], pe[4]});
                    ldsw<u4v>(d + 2 * KC_CP, u4v{po[0], po[1], po[2], po[3]});
                    ldsw<u4v>(d + 3 * KC_CP, u4v{pe[0], pe[1], pe[2], pe[3]});
                }
            }
            if (w == 7) ldsw<u4v>(L_KC + min(lane >> 1, 11) * KC_CP + (lane & 1) * 16, u4v{0u, 0u, 0u, 0u});
        }
        STAMP(1);
        f4v acc1[2][4];
#pragma unroll
        for (int i = 0; i < 8; ++i) acc1[i >> 2][i & 3] = f4v{};
        unsigned bx[2], aw[2];
        {
            const int q4 = l16 >> 2, p4 = lane & 3;
#pragma unroll
            for (int i = 0; i < 2; ++i) {
                bx[i] = (64 * tw + l16) * 128 + (((4 * i + g) ^ (l16 >> 1)) << 4);
                aw[i] = 16384 + (8 * g + q4) * 256 + (((2 * fw + i) ^ q4 ^ ((g & 1) << 2)) << 5) + p4 * 8;
            }
        }
        bf8v fa[2][2][2], fb[2][2][4];
        auto rd = [&](int buf, int i) {
            const int ks = i >> 1, h = i & 1;
            fa[buf][ks][h] = cat(trr(aw[h] + ks * 8192), trr(aw[h] + ks * 8192 + 1024));
            fb[buf][ks][2 * h] = asbf(ldsr<u4v>(bx[ks] + h * 4096));
            fb[buf][ks][2 * h + 1] = asbf(ldsr<u4v>(bx[ks] + h * 4096 + 2048));
        };
        unsigned sb = 0;
        int kt = rot + 2; kt = kt >= 10 ? kt - 10 : kt;
        auto tile = [&](int cur, bool more, bool stage, auto&& side) {
            const unsigned d = sb == 2 * SLOT ? (unsigned)(-2 * (int)SLOT) : SLOT, lb = sb + w * 2048;
            kt = kt == 9 ? 0 : kt + 1;
            sb += d; aw[0] += d; aw[1] += d; bx[0] += d; bx[1] += d;
#pragma unroll
            for (int i = 0; i < 4; ++i) {
                SB();
                if (more) rd(cur ^ 1, i);
                SB();
#pragma unroll
                for (int j = 0; j < 4; ++j) acc1[i & 1][j] = __builtin_amdgcn_mfma_f32_16x16x32_bf16(fa[cur][i >> 1][i & 1], fb[cur][i >> 1][j], acc1[i & 1][j], 0, 0, 0);
                SB();
                if (stage) {
                    if (i & 1) dma16(rw, vw, kt * (64 * 3456) + (i >> 1) * 4 * 3456, lb + (i >> 1) * 1024 + 16384);
                    else dma16(rx, vx[i >> 1], kt * 128, lb + (i >> 1) * 1024);
                }
                side(i);
            }
            SB();
        };
        auto none = [&](int) {};
        if (w >= 6) {
            WAIT_V(4);
            arrive_publish(L_CNT + 8, ws, WS_FW + min(bid, 216) * 128, tag, 1u);
        }
        BAR_L();
        STAMP(15);
#pragma unroll
        for (int i = 0; i < 4; ++i) rd(0, i);
        u4v uld[2], cbv;
        f2v sv[4];
        u64 fpoll = 0;
        const unsigned f1a = WS_FS + 2 * rb * 128, f1b = WS_FC + 2 * cb * 128;
        const unsigned f1_off = min(((lane & 2) ? f1b : f1a) + (lane & 1) * 128, WS_FC + 26 * 128);
        const unsigned f2_off = w == 2 ? WS_FA + (lane & 31) * 128 : WS_FW + (2 * min(54 * w + lane, 107) + (cb2 >> 2)) * 128;
        WAIT_BAR(4); tile(0, true, true, none);
        WAIT_BAR(4); tile(1, true, true, none);
        WAIT_BAR(4); tile(0, true, true, [&](int i) { if (i == 0 && w < 2) fpoll = ld_flag(ws, f2_off); });
        if (w < 2 && !__all(fpoll == tag)) wait_flags(ws, f2_off, tag);
        WAIT_BAR(4);
        STAMP(12);
        tile(1, true, true, [&](int i) { if (i == 0) loadb(0); if (i == 2) loadb(1); });
        WAIT_BAR(6); tile(0, true, true, [&](int i) { if (i == 0) loadb(2); if (i == 2) loadb(3); });
        WAIT_BAR(6); tile(1, true, true, [&](int i) { if (i == 0) loadb(4); if (i == 2) loadb(5); });
        WAIT_BAR(6); tile(0, true, true, [&](int i) { if (i == 0) loadb(6); if (i == 2) loadb(7); });
        WAIT_BAR(6); tile(1, true, false, [&](int i) { if (i == 0 && w == 0) fpoll = ld_flag(ws, f1_off); loadb(8 + i); });
        if (w == 0 && !__all(fpoll == tag)) wait_flags(ws, f1_off, tag);
        WAIT_BAR(4);
        STAMP(3);
        tile(0, true, false, [&](int i) {
            sv[i] = __builtin_bit_cast(f2v, __builtin_amdgcn_raw_buffer_load_b64(wsr, l16 * 8, WS_STATS + (rb * 128 + 64 * tw + 16 * i) * 8, 16));
            if (i == 0 && w == 1) cbv = __builtin_amdgcn_raw_buffer_load_b128(wsr, (lane >> 5) * (WS_B1 - WS_CS) + (lane & 31) * 16, WS_CS + cb * 512, 16);
            if (i == 0 && w == 2) fpoll = ld_flag(ws, f2_off);
            loadb(12 + i);
        });
        tile(1, false, false, [&](int i) { loadb(16 + i); });
        if (w == 1) ldsw<u4v>(L_CB + lane * 16, cbv);
        if (w >= 2 && w < 5) ldsw<u4v>(L_UA + (w - 2) * UA_CH + min(lane, 29) * 16, u4v{0u, 0u, 0u, 0u});
        if (w == 2 && !__all(fpoll == tag)) wait_flags(ws, f2_off, tag);
        BAR_L();
        STAMP(2);
        stage_x();
        {
            f4v cs[2], bb[2];
#pragma unroll
            for (int u = 0; u < 2; ++u) { cs[u] = ldsr<f4v>(L_CB + (32 * fw + 16 * u + 4 * g) * 4); bb[u] = ldsr<f4v>(L_CB + 512 + (32 * fw + 16 * u + 4 * g) * 4); }
            const int s = 4 * cb + fw;
            const unsigned ho = (unsigned)(WS_HA + ((2 * rb + tw) * 54 + s) * 4096 + lane * 16) | oob_if(s >= 54);
            u4v hp[4];
#pragma unroll
            for (int ti = 0; ti < 4; ++ti) {
                const f2v rs2 = {sv[ti][0], sv[ti][0]}, nm2 = {-sv[ti][1], -sv[ti][1]};
#pragma unroll
                for (int i = 0; i < 4; ++i) {
                    const int slot = 4 * ti + i, u = i >> 1, e = 2 * (i & 1);
                    if (slot < 2) {
                        const int q = w + 8 * slot, c = q >> 2;
                        uld[slot] = __builtin_amdgcn_raw_buffer_load_b128(wsr, lane * 16 | oob_if(c >= nch), WS_XT + (ch0 + c) * 4096 + (q & 3) * 1024, 16);
                    }
                    if (slot >= 2 && slot < 12) loadb(18 + slot);
                    SB();
                    const f2v v = __builtin_elementwise_fma(rs2, f2v{acc1[u][ti][e], acc1[u][ti][e + 1]}, __builtin_elementwise_fma(nm2, f2v{cs[u][e], cs[u][e + 1]}, f2v{bb[u][e], bb[u][e + 1]}));
                    const f2v z = v * __builtin_elementwise_fma(v * v, f2v{-0.10294324f, -0.10294324f}, f2v{-2.30220820f, -2.30220820f});
                    const f2v d = f2v{__builtin_amdgcn_exp2f(z[0]), __builtin_amdgcn_exp2f(z[1])} + f2v{1.f, 1.f};
                    const f2v gl = v * f2v{__builtin_amdgcn_rcpf(d[0]), __builtin_amdgcn_rcpf(d[1])};
                    hp[ti][i] = pk2(gl[0], gl[1]);
                    asm volatile("" : "+v"(hp[ti][i]));
                }
            }
            SB();
#pragma unroll
            for (int ti = 0; ti < 4; ++ti)
                __builtin_amdgcn_raw_buffer_store_b128(hp[ti], wsr, ho + 1024 * ti, 0, 16);
        }
#pragma unroll
        for (int i = 0; i < 2; ++i) {
            const int q = w + 8 * i, c = q >> 2;
            if (q < 12) ldsw<u4v>(L_UA + c * UA_CH + UA_PAD + (q & 3) * 1024 + lane * 16, uld[i]);
        }
        STAMP(4);
        BAR_L();
        {
            const int typ = w < 3 ? 0 : w < 6 ? 1 : 2;
            const int c1 = w < 3 ? w : w < 6 ? w - 3 : 2 * (w - 6), c2 = w == 6 ? 1 : c1;
            const unsigned kl = L_KC + (l16 >> 2) * KC_CP + 56 - 16 * g + 8 * (lane & 3);
            const unsigned ul = L_UA + UA_PAD + 32 * l16 - 32 + 16 * g;
            unsigned ka[3], ua[3];
            ka[0] = kl + c1 * KC_CH;
            ka[1] = typ == 2 ? kl + c2 * KC_CH - 4 * 512 : ka[0];
            ka[2] = typ == 1 ? ka[0] - 6 * 512 : ka[1];
            ua[0] = ul + c1 * UA_CH - typ * 1024;
            ua[2] = ul + c2 * UA_CH;
            ua[1] = typ == 2 ? ua[2] : ua[0];
            bf8v uf[8], kf[8];
            auto rdk = [&](int sl) { const unsigned ad = ka[sl < 4 ? 0 : sl < 6 ? 1 : 2] + 512u * sl; kf[sl] = cat(trr(ad + 8u), trr(ad)); };
            auto rdu = [&](int j) { uf[j] = asbf(ldsr<u4v>(ua[j >= 4 ? 0 : j >= 2 ? 1 : 2] + 512u * j)); };
#pragma unroll
            for (int sl = 0; sl < 8; ++sl) { rdk(sl); rdu(7 - sl); }
            f4v A[3] = {}, B[5] = {};
            const unsigned fo = WS_FH + (rb * 14 + cb) * 128;
#pragma nounroll
            for (int rho = 0; rho < 7; ++rho) {
#pragma unroll
                for (int i = 0; i < 3; ++i) { ka[i] += 64u; ua[i] -= 64u; asm volatile("" : "+v"(ka[i]), "+v"(ua[i])); }
#pragma unroll
                for (int sl = 0; sl < 8; ++sl) {
                    SB();
                    const int ia = sl < 4 ? 0 : sl < 6 ? 1 : 2, ib = sl < 3 ? 0 : sl - 2;
                    A[ia] = __builtin_amdgcn_mfma_f32_16x16x32_bf16(kf[sl], uf[7 - sl], A[ia], 0, 0, 0);
                    if (sl < 7) B[ib] = __builtin_amdgcn_mfma_f32_16x16x32_bf16(kf[sl], uf[6 - sl], B[ib], 0, 0, 0);
                    SB();
                    rdk(sl); rdu(7 - sl);
                }
                SB();
                if (rho == PUB_RHO) {
                    WAIT_V(0);
                    arrive_publish(L_CNT, ws, fo, tag);
                }
            }
#pragma unroll
            for (int sl = 0; sl < 8; ++sl) {
                const int ia = sl < 4 ? 0 : sl < 6 ? 1 : 2, ib = sl < 3 ? 0 : sl - 2;
                A[ia] = __builtin_amdgcn_mfma_f32_16x16x32_bf16(kf[sl], uf[7 - sl], A[ia], 0, 0, 0);
                if (sl < 7) B[ib] = __builtin_amdgcn_mfma_f32_16x16x32_bf16(kf[sl], uf[6 - sl], B[ib], 0, 0, 0);
            }
            SB();
            f4v y[4];
            if (typ == 0) { y[0] = A[0] + A[1] + A[2]; y[1] = (B[0] + B[1]) + (B[2] + B[3]) + B[4]; y[2] = y[0]; y[3] = y[1]; }
            else if (typ == 1) { y[0] = A[0] + A[1]; y[1] = B[0] + B[1] + B[2]; y[2] = A[2]; y[3] = B[4]; }
            else { y[0] = A[0]; y[1] = B[0]; y[2] = A[1] + A[2]; y[3] = B[2] + B[3] + B[4]; }
#pragma unroll
            for (int i = 0; i < 4; ++i) {
                const int c = i < 2 ? c1 : c2, tau = (i < 2 ? 7 - 2 * typ : 2 * typ - 1) - (i & 1);
                const bool on = c < nch && (i < 2 || (typ != 0 && w != 7));
                const u2v uv = ldsr<u2v>(L_UA + c * UA_CH + UA_PAD + 512 * max(tau, 0) + 32 * l16 + 8 * g);
                const float dk = c == 0 ? bflo(dsk01) : c == 1 ? bfhi(dsk01) : bflo(dsk2);
                const u2v o = {pk2(y[i][0] + dk * bflo(uv[0]), y[i][1] + dk * bfhi(uv[0])), pk2(y[i][2] + dk * bflo(uv[1]), y[i][3] + dk * bfhi(uv[1]))};
                __builtin_amdgcn_raw_buffer_store_b64(o, wsr, 32 * l16 + 8 * g, (WS_YT + (ch0 + c) * 4096 + 512 * max(tau, 0)) | oob_if(!on), 16);
            }
        }
        STAMP(5);
    } else {
        const int a = (r - 28) * 8 + xcd;
        const u4v rx = mkrsrc(x, 2048 * 1280);
        {
            unsigned vo[5];
#pragma unroll
            for (int i = 0; i < 5; ++i) {
                const int n = i * 64 + lane, rl = n / 80, cp = n - rl * 80;
                vo[i] = (a * 64 + 8 * w + rl) * 1280 + ((cp ^ (2 * w) ^ ((rl >> 1) << 3)) << 4);
                dma16(rx, vo[i], 0, L_AX + (w * 10 + i) * 1024);
            }
#pragma unroll
            for (int i = 5; i < 10; ++i) dma16(rx, vo[i - 5], 4 * 1280, L_AX + (w * 10 + i) * 1024);
        }
        const u64 e0 = ld_flag(ws, WS_E);
        u4v cv[10];
        {
            const auto rw1 = __builtin_amdgcn_make_buffer_rsrc((void*)(u64)w1, 0, 640 * 3456, 0x00020000);
#pragma unroll
            for (int i = 0; i < 10; ++i) cv[i] = __builtin_amdgcn_raw_buffer_load_b128(rw1, ((lane >> 3) * 3456 + (lane & 7) * 16) | oob_if(a >= 27), (80 * w + 8 * i) * 3456 + a * 128, 0);
        }
        if (w == 0) ldsw<unsigned>(L_ACNT + (lane & 7) * 4, 0u);
        STAMP(10);
        WAIT_V(11);
        STAMP(11);
        {
            const int row = 8 * w + (lane >> 3), sw = (2 * w) ^ (((row >> 1) & 1) << 3);
            u4v v[10];
#pragma unroll
            for (int i = 0; i < 10; ++i) v[i] = ldsr<u4v>(L_AX + row * 1280 + (((lane & 7) + 8 * i) ^ sw) * 16);
            SB();
            float s1[2] = {0.f, 0.f}, s2[2] = {0.f, 0.f};
#pragma unroll
            for (int i = 0; i < 10; ++i)
#pragma unroll
                for (int j = 0; j < 4; ++j) { s1[j & 1] = dot2(v[i][j], 0x3f803f80u, s1[j & 1]); s2[j & 1] = dot2(v[i][j], v[i][j], s2[j & 1]); }
            const f2v sv = ln_stats(sum8(s1[0] + s1[1]), sum8(s2[0] + s2[1]));
            ldsw<f2v>(L_AST + row * 8, sv);
            __builtin_amdgcn_raw_buffer_store_b64(__builtin_bit_cast(u2v, sv), wsr, (WS_STATS + (a * 64 + row) * 8) | oob_if(lane & 7), 0, 16);
        }
        BAR_L();
        STAMP(14);
        {
            const int o = lane & 7, cq = lane >> 3;
            f2v rs2[4], nm2[4];
#pragma unroll
            for (int i = 0; i < 4; ++i) { const f4v q = ldsr<f4v>(L_AST + o * 64 + i * 16); rs2[i] = f2v{q[0], q[2]}; nm2[i] = f2v{-q[1], -q[3]}; }
            u2v v[3][8];
#pragma unroll
            for (int k = 0; k < 3; ++k)
#pragma unroll
                for (int t = 0; t < 8; ++t) v[k][t] = ldsr<u2v>(L_AX + (8 * o + t) * 1280 + ((4 * min(w + 8 * k, 19) + (cq >> 1)) ^ ((2 * o) ^ (((t >> 1) & 1) << 3))) * 16 + (cq & 1) * 8);
            SB();
#pragma unroll
            for (int k = 0; k < 3; ++k) {
                if (k < 2 || w < 4) {
#pragma unroll
                    for (int e = 0; e < 4; ++e) {
                        u4v ov;
#pragma unroll
                        for (int p = 0; p < 4; ++p) {
                            const unsigned va = v[k][2 * p][e >> 1], vb = v[k][2 * p + 1][e >> 1];
                            const f2v n2 = __builtin_elementwise_fma((e & 1) ? f2v{bfhi(va), bfhi(vb)} : f2v{bflo(va), bflo(vb)}, rs2[p], nm2[p]);
                            ov[p] = pk2(n2[0], n2[1]);
                        }
                        __builtin_amdgcn_raw_buffer_store_b128(ov, wsr, WS_XT + (32 * (w + 8 * k) + 4 * cq + e) * 4096 + a * 128 + o * 16, 0, 16);
                        SB();
                    }
                }
                if (k == 0) {
                    WAIT_V(4);
                    tag = mk64(__builtin_amdgcn_readfirstlane((unsigned)e0), __builtin_amdgcn_readfirstlane((unsigned)(e0 >> 32))) + 1ull;
                    arrive_publish(L_ACNT + 8, ws, WS_FS + a * 128, tag);
                }
            }
        }
        STAMP(15);
        {
            unsigned selo = 0x00003f80u, sehi = 0x3f800000u;
            asm volatile("" : "+v"(selo), "+v"(sehi));
            float sm[8] = {0.f, 0.f, 0.f, 0.f, 0.f, 0.f, 0.f, 0.f};
#pragma unroll
            for (int i = 0; i < 10; ++i)
#pragma unroll
                for (int j = 0; j < 4; ++j) { sm[2 * j] = dot2(cv[i][j], selo, sm[2 * j]); sm[2 * j + 1] = dot2(cv[i][j], sehi, sm[2 * j + 1]); }
#pragma unroll
            for (int j = 0; j < 8; ++j) sm[j] = dpp_add<0x128>(sm[j]);
            ldsw<f4v>(L_ACS + (4 * w + g) * 256 + (lane & 7) * 32 + (lane & 8) * 2, (lane & 8) ? f4v{sm[4], sm[5], sm[6], sm[7]} : f4v{sm[0], sm[1], sm[2], sm[3]});
        }
        BAR_L();
        if (w != 7 || a >= 27) {
            WAIT_V(0);
            arrive_publish(L_ACNT + 12, ws, WS_FA + a * 128, tag);
        } else {
            const float bf = bf2f(b1[64 * a + lane]);
            float p[32], sum = 0.f;
#pragma unroll
            for (int i = 0; i < 32; ++i) p[i] = ldsr<float>(L_ACS + i * 256 + lane * 4);
#pragma unroll
            for (int i = 0; i < 32; i += 4) sum += (p[i] + p[i + 1]) + (p[i + 2] + p[i + 3]);
            __builtin_amdgcn_raw_buffer_store_b32(__builtin_bit_cast(unsigned, sum), wsr, WS_CS + (64 * a + lane) * 4, 0, 16);
            __builtin_amdgcn_raw_buffer_store_b32(__builtin_bit_cast(unsigned, bf), wsr, WS_B1 + (64 * a + lane) * 4, 0, 16);
            WAIT_V(0);
            raise_flag(ws, WS_FC + a * 128, tag);
            arrive_publish(L_ACNT + 12, ws, WS_FA + a * 128, tag);
        }
        STAMP(2);
        if (w < 2) wait_flags(ws, WS_FW + (2 * min(54 * w + lane, 107) + (cb2 >> 2)) * 128, tag);
        BAR_L();
        STAMP(3);
        stage_x();
#pragma unroll
        for (int n = 0; n < 30; ++n) loadb(n);
        __builtin_amdgcn_s_waitcnt(0x0F70);
    }

    asm volatile("" : "+s"(rot2));
    if (w == 0) wait_flags(ws, WS_FH + ((rb2 >> 1) * 14 + min(lane, 13)) * 128, tag);
    BAR_L();
    STAMP(6);
    u4v ha[7][4];
    auto loada = [&](int i, int t) { ha[i][t] = __builtin_amdgcn_raw_buffer_load_b128(wsr, lane * 16, WS_HA + ((rb2 * 54 + sidx(i)) * 4 + t) * 1024, 16); };
#pragma unroll
    for (int i = 0; i < 8; ++i) loada(i >> 2, i & 3);
    if (is_gemm) {
        WAIT_V(8);
        arrive_publish(L_CNT + 4, ws, WS_FY + bid * 128, tag);
    }
    const int c0 = 80 * cb2;
    int lz = lane;
    asm volatile("" : "+v"(lz));
    const int cA = c0 + 10 * w, cB = cA + 9;
    const int own0 = max((cA * 0xaaab) >> 17, (cA - 192) >> 1), own1 = max((cB * 0xaaab) >> 17, (cB - 192) >> 1);
    u64 ypoll = tag;
    u4v y0, y1;
    f4v acc2[4][5];
#pragma unroll
    for (int i = 0; i < 20; ++i) acc2[i / 5][i % 5] = f4v{};
#pragma unroll
    for (int i = 0; i < 5; ++i) {
#pragma unroll
        for (int t = 0; t < 4; ++t) {
            SB();
#pragma unroll
            for (int c = 0; c < 5; ++c) acc2[t][c] = __builtin_amdgcn_mfma_f32_16x16x32_bf16(asbf(ha[i][t]), asbf(bw[5 * i + c]), acc2[t][c], 0, 0, 0);
            SB();
            if (i > 0) loada(i + 2, t);
            else if (t == 3) { loada(2, 0); loada(2, 1); loada(2, 2); loada(2, 3); }
            if (i == 1 || (i == 2 && t == 0)) loadb(30 + 4 * (i - 1) + t);
            if (i == 3 && t == 0) ypoll = ld_flag(ws, WS_FY + min(own0 + lz, own1) * 128);
            if (i == 4 && t == 3) {
                if (!__all(ypoll == tag)) wait_flags(ws, WS_FY + min(own0 + lz, own1) * 128, tag);
                y0 = __builtin_amdgcn_raw_buffer_load_b128(wsr, (lz >> 3) * 4096 + (lz & 7) * 16, WS_YT + cA * 4096 + rb2 * 128, 16);
                y1 = __builtin_amdgcn_raw_buffer_load_b128(wsr, (unsigned)((8 + (lz >> 3)) * 4096 + (lz & 7) * 16) | oob_if(lz >= 16), WS_YT + cA * 4096 + rb2 * 128, 16);
            }
        }
    }
    STAMP(7);
    auto dump = [&](int t) {
        unsigned p[10];
#pragma unroll
        for (int c = 0; c < 5; ++c) { p[2 * c] = pk2(acc2[t][c][0], acc2[t][c][1]); p[2 * c + 1] = pk2(acc2[t][c][2], acc2[t][c][3]); }
        addtid10(L_RED + w * RED_W + t * 5 * RED_T, p);
    };
#pragma unroll
    for (int t = 0; t < 4; ++t) {
        SB();
#pragma unroll
        for (int c = 0; c < 5; ++c) acc2[t][c] = __builtin_amdgcn_mfma_f32_16x16x32_bf16(asbf(ha[5][t]), asbf(bw[25 + c]), acc2[t][c], 0, 0, 0);
        SB();
        if (t > 0) dump(t - 1);
        SB();
#pragma unroll
        for (int c = 0; c < 5; ++c) acc2[t][c] = __builtin_amdgcn_mfma_f32_16x16x32_bf16(asbf(ha[6][t]), asbf(bw[30 + c]), acc2[t][c], 0, 0, 0);
    }
    SB();
    ldsw<u4v>(L_YS + (10 * w + (lz >> 3)) * YS_ROW + (lz & 7) * 16, y0);
    ldsw<u4v>(L_YS + min(10 * w + 8 + (lz >> 3) + (lz >= 16 ? 80 : 0), 80) * YS_ROW + (lz & 7) * 16, y1);
    dump(3);
    BAR_L();
    STAMP(8);
    {
        unsigned selo = 0x00003f80u, sehi = 0x3f800000u;
        asm volatile("" : "+v"(selo), "+v"(sehi));
#pragma unroll
        for (int k = 0; k < 3; ++k) {
            if (k < 2 || w < 4) {
                const int id = w * 64 + lz + 512 * k, tp = id / 40, c2 = id - tp * 40;
                const unsigned ra = L_RED + ((tp >> 3) * 5 + (c2 >> 3)) * RED_T + (tp & 1) * RED_ROW + ((tp >> 1) & 3) * 64 + (c2 & 7) * 8;
                u2v p[8];
#pragma unroll
                for (int j = 0; j < 8; ++j) p[j] = ldsr<u2v>(ra + j * RED_W);
                const unsigned ya = ldsr<unsigned>(L_YS + 2 * c2 * YS_ROW + tp * 4), yb = ldsr<unsigned>(L_YS + (2 * c2 + 1) * YS_ROW + tp * 4);
                const unsigned xa = ldsr<unsigned>(L_XS + tp * 320 + c2 * 4), xb = ldsr<unsigned>(L_XS + tp * 320 + 160 + c2 * 4), bv = ldsr<unsigned>(L_XS + 10240 + c2 * 4);
                float o00 = bflo(ya) + bflo(xa) + bflo(bv), o01 = bflo(yb) + bfhi(xa) + bfhi(bv);
                float o10 = bfhi(ya) + bflo(xb) + bflo(bv), o11 = bfhi(yb) + bfhi(xb) + bfhi(bv);
#pragma unroll
                for (int j = 0; j < 8; ++j) {
                    o00 = dot2(p[j][0], selo, o00); o10 = dot2(p[j][0], sehi, o10);
                    o01 = dot2(p[j][1], selo, o01); o11 = dot2(p[j][1], sehi, o11);
                }
                GP(unsigned) op = (GP(unsigned))(out + (rb2 * 64 + 2 * tp) * 640 + c0 + 2 * c2);
                op[0] = pk2(o00, o01);
                op[320] = pk2(o10, o11);
            }
        }
    }
    if (bid == 0 && w == 5) {
#pragma unroll
        for (int i = 0; i < 4; ++i) wait_flags(ws, WS_FH + (i * 64 + lz) * 128, tag);
        raise_flag(ws, WS_E, tag);
    }
    STAMP(9);
    if (w == 0 && bid == 5) { u64 c1 = __builtin_amdgcn_s_memtime(); *(u64*)(ws + WS_STAMP + 400000) = c1 - cyc0; *(u64*)(ws + WS_STAMP + 400008) = st[9] - st[0]; }
    for (int i = 0; i < NSTAMP; ++i) *(u64*)(ws + WS_STAMP + ((bid * 8 + w) * NSTAMP + i) * 8) = st[i];
}

extern "C" void kernel_launch(void* const* d_in, const int* in_sizes, int n_in, void* d_out, int out_size, void* d_ws, size_t ws_size, hipStream_t stream) {
    (void)in_sizes; (void)n_in; (void)out_size; (void)ws_size;
    (void)hipFuncSetAttribute((const void*)s4ffn_fused, hipFuncAttributeMaxDynamicSharedMemorySize, LDS_BYTES);
    s4ffn_fused<<<256, 512, LDS_BYTES, stream>>>((const u16*)d_in[0], (const u16*)d_in[1], (const u16*)d_in[2], (const u16*)d_in[3], (const u16*)d_in[4],
                                                 (const u16*)d_in[5], (const u16*)d_in[6], (const u16*)d_in[7], (const u16*)d_in[8], (const u16*)d_in[10],
                                                 (const u16*)d_in[11], (const u16*)d_in[12], (const u16*)d_in[13], (u16*)d_out, (char*)d_ws);
}
```

```cpp
#define OUT_AUX 2
#include <hip/hip_runtime.h>
#include <stdint.h>
#define PUB_RHO 4
#define CSUM_ON 1
#define CODE_CHUNKS 4
#define CODE_BYTES 28672u
#define ARR 0

typedef unsigned short u16;
typedef unsigned long long u64;
typedef short s4v __attribute__((ext_vector_type(4)));
typedef __bf16 bf8v __attribute__((ext_vector_type(8)));
typedef __bf16 bf2v __attribute__((ext_vector_type(2)));
typedef float f2v __attribute__((ext_vector_type(2)));
typedef float f4v __attribute__((ext_vector_type(4)));
typedef float f16v __attribute__((ext_vector_type(16)));
typedef unsigned u2v __attribute__((ext_vector_type(2)));
typedef unsigned short h2v __attribute__((ext_vector_type(2)));
typedef unsigned u4v __attribute__((ext_vector_type(4)));
typedef __attribute__((address_space(1))) u64 gu64;
#define LDSP(T) __attribute__((address_space(3))) T*
#define DI __device__ __forceinline__
#define RLX_AGENT __ATOMIC_RELAXED, __HIP_MEMORY_SCOPE_AGENT

constexpr unsigned WS_E = 0;
constexpr unsigned WS_FH = 4096;
constexpr unsigned WS_FA = 32768;
constexpr unsigned WS_FW = 81920;
constexpr unsigned WS_FS = 40960;
constexpr unsigned WS_FC = 45056;
constexpr unsigned WS_FY = 49152;
constexpr unsigned WS_STATS = 131072;
constexpr unsigned WS_CS = 196608, WS_B1 = 204800;
constexpr unsigned WS_STAMP = 262144;
constexpr unsigned WS_XT = 1u << 20;
constexpr unsigned WS_W2T = 4u << 20;
constexpr unsigned WS_YT = 8u << 20;
constexpr unsigned WS_HA = 12u << 20;
constexpr unsigned WS_BYTES = 20u << 20;
constexpr unsigned SLOT = 32768;
constexpr unsigned L_KC = 98304, KC_CH = 16640, KC_CP = 4160;
constexpr unsigned L_UA = 148224, UA_CH = 4576, UA_PAD = 480;
constexpr unsigned L_MISC = 162368, L_CB = L_MISC, L_CNT = L_MISC + 1024;
constexpr unsigned L_TBL = 65536, TBL_ROW = 544, TBL_CH = 53 * TBL_ROW;
constexpr unsigned L_PRM = 152064, L_TT = 158464, TT_CH = 1296, L_W2U = L_PRM;
constexpr unsigned L_SCR = L_CB;
constexpr unsigned L_XS = 0, L_RED = 16384, RED_ROW = 256, RED_T = 576, RED_W = 20 * RED_T, L_YS = L_RED + 8 * RED_W, YS_ROW = 144;
constexpr unsigned L_AX = 0, L_AST = 81920, L_ACNT = L_AST + 512, L_ACS = L_AST + 1024;
static_assert(L_TT + 3 * TT_CH <= L_MISC && L_CNT + 64 <= 163840 && L_W2U + 10240 <= L_MISC, "LDS map");
constexpr unsigned LDS_BYTES = 163840;
constexpr int SPIN_LIMIT = 400000;

#define STAMP(i)

DI float bf2f(unsigned v) { return __uint_as_float(v << 16); }
DI float bflo(unsigned v) { return __uint_as_float(v << 16); }
DI float bfhi(unsigned v) { return __uint_as_float(v & 0xffff0000u); }
DI unsigned pk2(float a, float b) { bf2v v; v[0] = (__bf16)a; v[1] = (__bf16)b; return __builtin_bit_cast(unsigned, v); }
DI float rbf(float a) { return bflo(pk2(a, a)); }
template <typename P> DI u4v mkrsrc(P p, unsigned bytes) { u64 a = (u64)p; u4v r; r[0] = (unsigned)a; r[1] = (unsigned)(a >> 32); r[2] = bytes; r[3] = 0x00020000u; return r; }
DI void dma16(u4v rs, unsigned voff, unsigned soff, unsigned lds) {
    asm volatile("s_mov_b32 m0, %3\n\ts_nop 0\n\tbuffer_load_dwordx4 %0, %1, %2 offen lds" ::"v"(voff), "s"(rs), "s"(soff), "s"(lds) : "memory");
}
DI void dma16g(const void* p, unsigned lds) {
    asm volatile("s_mov_b32 m0, %1\n\ts_nop 0\n\tglobal_load_lds_dwordx4 %0, off" ::"v"(p), "s"(lds) : "memory");
}
DI void dma16g_lanes3(const void* p, unsigned lds) {
    u64 ex;
    asm volatile("s_mov_b64 %0, exec\n\ts_mov_b64 exec, 7\n\ts_mov_b32 m0, %2\n\ts_nop 0\n\tglobal_load_lds_dwordx4 %1, off\n\ts_mov_b64 exec, %0" : "=&s"(ex) : "v"(p), "s"(lds) : "memory");
}
DI void dma16_lanes16(u4v rs, unsigned voff, unsigned soff, unsigned lds) {
    u64 ex;
    asm volatile("s_mov_b64 %0, exec\n\ts_mov_b64 exec, 0xffff\n\ts_mov_b32 m0, %4\n\ts_nop 0\n\tbuffer_load_dwordx4 %1, %2, %3 offen lds\n\ts_mov_b64 exec, %0"
                 : "=&s"(ex) : "v"(voff), "s"(rs), "s"(soff), "s"(lds) : "memory");
}
DI void touch_code(unsigned chunk, int lane, unsigned lds) {
    u64 pc;
    asm volatile("s_getpc_b64 %0" : "=s"(pc));
    const u64 p = (pc & ~127ull) + min(chunk * 8192u + lane * 128u, CODE_BYTES - 128u);
    asm volatile("s_mov_b32 m0, %1\n\ts_nop 0\n\tglobal_load_lds_dword %0, off" ::"v"(p), "s"(lds) : "memory");
}
#define SB() __builtin_amdgcn_sched_barrier(0)
#define WAIT_BAR(n) asm volatile("s_waitcnt vmcnt(" #n ") lgkmcnt(0)\n\ts_barrier" ::: "memory")
#define BAR_L() asm volatile("s_waitcnt lgkmcnt(0)\n\ts_barrier" ::: "memory")
#define WAIT_V(n) asm volatile("s_waitcnt vmcnt(" #n ")" ::: "memory")
template <typename T> DI T ldsr(unsigned a) { return *(LDSP(T))a; }
template <typename T> DI void ldsw(unsigned a, T v) { *(LDSP(T))a = v; }
DI void stw(unsigned a, unsigned v, int off) { asm volatile("ds_write_b32 %0, %1 offset:%2" ::"v"(a), "v"(v), "i"(off) : "memory"); }
DI s4v trr(unsigned a) { return __builtin_amdgcn_ds_read_tr16_b64_v4i16((LDSP(s4v))a); }
DI bf8v cat(s4v a, s4v b) { return __builtin_bit_cast(bf8v, __builtin_shufflevector(a, b, 0, 1, 2, 3, 4, 5, 6, 7)); }
DI bf8v asbf(u4v a) { return __builtin_bit_cast(bf8v, a); }
template <int CTRL> DI float dpp_add(float v) { return v + __builtin_bit_cast(float, __builtin_amdgcn_update_dpp(0, __builtin_bit_cast(int, v), CTRL, 0xf, 0xf, true)); }
DI float sum8(float v) { return dpp_add<0x141>(dpp_add<0x4e>(dpp_add<0xb1>(v))); }
DI float dot2(unsigned a, unsigned b, float c) { return __builtin_amdgcn_fdot2_f32_bf16(__builtin_bit_cast(bf2v, a), __builtin_bit_cast(bf2v, b), c, false); }
DI u64 ld_flag(const char* ws, unsigned off) { return __hip_atomic_load((gu64*)(ws + off), RLX_AGENT); }
DI void wait_flags(const char* ws, unsigned off, u64 tag) {
#pragma nounroll
    for (int it = 0; it < SPIN_LIMIT; ++it) {
        if (__all(ld_flag(ws, off) == tag)) break;
        __builtin_amdgcn_s_sleep(1);
    }
    asm volatile("" ::: "memory");
}
DI unsigned lds_count(unsigned cnt_lds) {
    unsigned old, one = 1u;
    u64 ex;
    asm volatile("s_mov_b64 %1, exec\n\ts_mov_b64 exec, 1\n\ts_waitcnt lgkmcnt(0)\n\tds_add_rtn_u32 %0, %2, %3\n\ts_waitcnt lgkmcnt(0)\n\ts_mov_b64 exec, %1"
                 : "=&v"(old), "=&s"(ex) : "v"(cnt_lds), "v"(one) : "memory");
    return __builtin_amdgcn_readfirstlane(old);
}
DI void raise_flag(char* ws, unsigned flag_off, u64 tag) { __hip_atomic_store((gu64*)(ws + flag_off), tag, RLX_AGENT); }
DI void arrive_publish(unsigned cnt_lds, char* ws, unsigned flag_off, u64 tag, unsigned last = 7u) {
    if (lds_count(cnt_lds) == last) raise_flag(ws, flag_off, tag);
}
DI void lds_wait(unsigned cnt_lds, unsigned n) {
#pragma nounroll
    for (int it = 0; it < SPIN_LIMIT; ++it) {
        if (__builtin_amdgcn_readfirstlane(*(volatile LDSP(unsigned))cnt_lds) >= n) break;
        __builtin_amdgcn_s_sleep(1);
    }
    asm volatile("" ::: "memory");
}
DI unsigned oob_if(bool c) { return c ? 0x80000000u : 0u; }
DI f2v ln_stats(float s1, float s2) {
    const float mean = s1 * (1.f / 640.f), var = fmaxf(__builtin_fmaf(-mean, mean, s2 * (1.f / 640.f)), 0.f);
    const float rs = __builtin_amdgcn_rcpf(rbf(__builtin_amdgcn_sqrtf(rbf(rbf(var) + 1e-5f))));
    return f2v{rs, rbf(mean) * rs};
}
DI f2v cmul_a(f2v a, f2v b) { f2v t; asm("v_pk_mul_f32 %0, %1, %2 op_sel_hi:[0,1]" : "=v"(t) : "v"(a), "v"(b)); return t; }
DI f2v cmul_b(f2v a, f2v b, f2v t) {
    f2v o;
    asm("v_pk_fma_f32 %0, %1, %2, %3 op_sel:[1,1,0] op_sel_hi:[1,0,1] neg_lo:[0,1,0]" : "=v"(o) : "v"(a), "v"(b), "v"(t));
    return o;
}
DI f2v cmul(f2v a, f2v b) { return cmul_b(a, b, cmul_a(a, b)); }
DI f2v cexp_p(float are, float aim, float p) {
    const float m = __builtin_amdgcn_exp2f(are * p), an = __builtin_amdgcn_fractf(aim * p);
    return f2v{m * __builtin_amdgcn_cosf(an), m * __builtin_amdgcn_sinf(an)};
}

DI void addtid10(unsigned m0v, const unsigned (&p)[10]) {
    asm volatile("s_mov_b32 m0, %10\n\ts_nop 0\n\t"
                 "ds_write_addtid_b32 %0 offset:%11\n\tds_write_addtid_b32 %1 offset:%12\n\tds_write_addtid_b32 %2 offset:%13\n\tds_write_addtid_b32 %3 offset:%14\n\t"
                 "ds_write_addtid_b32 %4 offset:%15\n\tds_write_addtid_b32 %5 offset:%16\n\tds_write_addtid_b32 %6 offset:%17\n\tds_write_addtid_b32 %7 offset:%18\n\t"
                 "ds_write_addtid_b32 %8 offset:%19\n\tds_write_addtid_b32 %9 offset:%20"
                 ::"v"(p[0]), "v"(p[1]), "v"(p[2]), "v"(p[3]), "v"(p[4]), "v"(p[5]), "v"(p[6]), "v"(p[7]), "v"(p[8]), "v"(p[9]), "s"(m0v),
                   "i"(0), "i"(RED_ROW), "i"(RED_T), "i"(RED_T + RED_ROW), "i"(2 * RED_T), "i"(2 * RED_T + RED_ROW), "i"(3 * RED_T), "i"(3 * RED_T + RED_ROW),
                   "i"(4 * RED_T), "i"(4 * RED_T + RED_ROW) : "memory");
}

#define GP(T) __attribute__((address_space(1))) T*
typedef unsigned u8v __attribute__((ext_vector_type(8)));
DI u64 mk64(unsigned lo, unsigned hi) { return ((u64)hi << 32) | lo; }
__global__ __launch_bounds__(512, 2) void s4ffn_fused(const u16*, const u16*, const u16*, const u16*, const u16*, const u16*, const u16*, const u16*, const u16*,
                                                      const u16*, const u16*, const u16*, const u16*, u16*, char*) {
    const int tid = threadIdx.x, lane = tid & 63, w = __builtin_amdgcn_readfirstlane(tid >> 6);
    const int xcd = blockIdx.x & 7, r = ((blockIdx.x >> 3) + 28) & 31, bid = r * 8 + xcd;
    const bool is_gemm = r < 28;
    STAMP(0);
    if (w < CODE_CHUNKS && ((r + w) & 3) == 0) touch_code(w, lane, 163840 - 256);
    u64 a_x, a_prm;
    u8v a_m;
    u4v a_t;
    asm volatile("s_load_dwordx2 %0, %4, 0x0\n\ts_load_dwordx2 %1, %4, %5 offset:0x8\n\ts_load_dwordx8 %2, %4, 0x48\n\ts_load_dwordx4 %3, %4, 0x68\n\ts_waitcnt lgkmcnt(0)"
                 : "=&s"(a_x), "=&s"(a_prm), "=&s"(a_m), "=&s"(a_t) : "s"(__builtin_amdgcn_kernarg_segment_ptr()), "s"(8 * w));
    const GP(u16) x = (const GP(u16))a_x;
    const char* prm = (const char*)a_prm;
    const GP(u16) w1 = (const GP(u16))mk64(a_m[0], a_m[1]);
    const GP(u16) b1 = (const GP(u16))mk64(a_m[2], a_m[3]);
    const GP(u16) w2 = (const GP(u16))mk64(a_m[4], a_m[5]);
    const GP(u16) b2 = (const GP(u16))mk64(a_m[6], a_m[7]);
    GP(u16) out = (GP(u16))mk64(a_t[0], a_t[1]);
    char* ws = (char*)mk64(a_t[2], a_t[3]);
    u64 tag;
    const auto wsr = __builtin_amdgcn_make_buffer_rsrc((void*)ws, 0, WS_BYTES, 0x00020000);
    const int nch = bid < 192 ? 3 : 2, ch0 = 3 * bid - max(bid - 192, 0);
    const int g = lane >> 4, l16 = lane & 15;
    const int rb2 = xcd * 4 + (r & 3), cb2 = r >> 2;
    const bool has7 = w < 6;
    u4v bw[35];
    int rot2 = 7 * cb2 + 14 * (r & 3) + w;
    rot2 = rot2 >= 54 ? rot2 - 54 : rot2;
    auto sidx = [&](int i) { const int s = rot2 + 8 * i; return s >= 54 ? s - 54 : s; };
    auto loadb = [&](int n) {
        bw[n] = __builtin_amdgcn_raw_buffer_load_b128(wsr, lane * 16 | oob_if(n >= 30 && !has7), WS_W2T + ((cb2 * 54 + sidx(n / 5)) * 5 + n % 5) * 1024, 16);
    };

    auto stage_x = [&]() {
        const u4v rx = mkrsrc(x, 2048 * 1280);
        const int n1 = 64 + (lane & 15);
        dma16(rx, (lane / 10) * 1280 + (lane % 10) * 16, (rb2 * 64 + 8 * w) * 1280 + cb2 * 160, L_XS + w * 1280);
        dma16_lanes16(rx, (n1 / 10) * 1280 + (n1 % 10) * 16, (rb2 * 64 + 8 * w) * 1280 + cb2 * 160, L_XS + w * 1280 + 1024);
        if (w == 0) dma16_lanes16(mkrsrc(b2, 1280), min(lane, 9) * 16, cb2 * 160, L_XS + 10240);
    };
    if (__builtin_expect(is_gemm, 1)) {
        if (w < 6) dma16g(prm + (ch0 + min(g, nch - 1)) * 256 + l16 * 16, L_PRM + w * 1024);
        else dma16g_lanes3(lane == 2 ? (const char*)ws + WS_E : prm + min(((ch0 * 2) & ~15) + lane * 16, 1264), L_SCR + (w - 6) * 64);
        SB();
        const int gi = r & 3, gj = r >> 2, rb = (xcd >> 1) * 4 + gi, cb = (xcd & 1) * 7 + gj;
        const int fw = w & 3, tw = w >> 2;
        const u4v rx = mkrsrc(x, 2048 * 1280), rw = mkrsrc(w1, 640 * 3456);
        unsigned vx[2];
#pragma unroll
        for (int u = 0; u < 2; ++u) vx[u] = (rb * 128 + 16 * w + 8 * u + (lane >> 3)) * 1280 + (((lane & 7) ^ (4 * u + g)) << 4);
        const int wc = l16 ^ (2 * g) ^ ((w & 1) << 3);
        const unsigned vw = (unsigned)((8 * w + g) * 3456 + cb * 256 + wc * 16) | (cb == 13 ? (unsigned)(wc & 8) << 28 : 0u);
        int rot = gj + 3 * gi + xcd;
        rot = rot >= 20 ? rot - 20 : rot >= 10 ? rot - 10 : rot;
        auto stage1w = [&](int t, int slot, int p, int dw) {
            int kt = t + rot; kt = kt >= 10 ? kt - 10 : kt;
            const unsigned lb = slot * SLOT + (w + dw) * 2048 + (p >> 1) * 1024;
            if (p & 1) dma16(rw, vw + dw * 8 * 3456, kt * (64 * 3456) + (p >> 1) * 4 * 3456, lb + 16384);
            else dma16(rx, vx[p >> 1] + dw * 16 * 1280, kt * 128, lb);
        };
        auto stage1 = [&](int t, int slot, int p) { stage1w(t, slot, p, 0); };
#pragma unroll
        for (int i = 0; i < 4; ++i) stage1(0, 0, i);
        u4v w2a, w2b;
        {
            const auto rw2 = __builtin_amdgcn_make_buffer_rsrc((void*)(u64)w2, 0, 1728 * 1280, 0x00020000);
            const unsigned so2 = ((bid >> 2) * 32 + ((bid >> 1) & 1) * 8 + (w >> 2) * 16 + (w & 3) * 2) * 1280 + (bid & 1) * 640;
            w2a = __builtin_amdgcn_raw_buffer_load_b128(rw2, lane * 16 + (lane >= 40 ? 640 : 0), so2, 0);
            w2b = __builtin_amdgcn_raw_buffer_load_b128(rw2, (1280 + 384 + lane * 16) | oob_if(lane >= 16), so2, 0);
        }
        SB();
        unsigned ix[12];
        {
            const int l0 = 8 * (tid & 255) - 3;
            const float lf = (float)l0;
            const unsigned kb = L_TT + tw * TT_CH, kc = kb - 2u * (0x4300 - 128);
#pragma unroll
            for (int i = 0; i < 6; ++i) {
                const unsigned c2 = pk2(lf + (float)(2 * i), lf + (float)(2 * i + 1));
                asm("v_mad_u32_u16 %0, %1, 2, %2" : "=v"(ix[2 * i]) : "v"(c2), "s"(kc));
                asm("v_mad_u32_u16 %0, %1, 2, %2 op_sel:[1,0,0,0]" : "=v"(ix[2 * i + 1]) : "v"(c2), "s"(kc));
            }
            if (fw == 0) {
#pragma unroll
                for (int i = 0; i < 11; ++i) {
                    const int l = l0 + i;
                    const unsigned lo = kb + 2u * min((unsigned)l, 641u), m = (unsigned)((l - 128) >> 31);
                    ix[i] = (lo & m) | (ix[i] & ~m);
                }
            }
#pragma unroll
            for (int i = 0; i < 11; ++i) asm volatile("" : "+v"(ix[i]));
        }
        if (w == 0) {
            ldsw<unsigned>(L_CNT + (lane & 7) * 4, 0u);
        }
        STAMP(10);
        WAIT_BAR(6);
        STAMP(11);
        const unsigned so = (ch0 * 2) & 15;
        { const u2v tv = ldsr<u2v>(L_SCR + 32); tag = mk64(__builtin_amdgcn_readfirstlane(tv[0]), __builtin_amdgcn_readfirstlane(tv[1])) + 1ull; }
        const unsigned dsk01 = __builtin_amdgcn_readfirstlane(ldsr<u16>(L_SCR + 64 + so) | ((unsigned)ldsr<u16>(L_SCR + 64 + so + 2) << 16));
        const unsigned dsk2 = __builtin_amdgcn_readfirstlane(ldsr<u16>(L_SCR + 64 + so + 4));
        {
            const int hc = w < 6 ? w : w - 2, c = hc >> 1, mode = (hc & 1) * 64 + lane;
            const float ldt = bf2f(ldsr<u16>(L_SCR + so + 2 * c));
            const unsigned pb = L_PRM + c * 256 + mode * 2;
            const float pa = bf2f(ldsr<u16>(pb)), pi = bf2f(ldsr<u16>(pb + 1024));
            const f2v Bc = {bf2f(ldsr<u16>(pb + 2048)), bf2f(ldsr<u16>(pb + 3072))}, Cc = {bf2f(ldsr<u16>(pb + 4096)), bf2f(ldsr<u16>(pb + 5120))};
            lds_count(L_CNT + 12);
            if (w >= 4) {
#pragma unroll
                for (int i = 0; i < 8; ++i) stage1w(1, 1, i & 3, i < 4 ? 0 : -4);
            }
            if (c < nch) {
                const float dt = rbf(__builtin_amdgcn_exp2f(ldt * 1.4426950408889634f));
                const float are = dt * pa * 1.4426950408889634f, aim = dt * pi * 0.15915494309189535f;
                const unsigned tb = L_TBL + c * TBL_CH + mode * 4;
                if (w < 6) {
                    const f2v z8 = cexp_p(are, aim, 8.f), z16 = cexp_p(are, aim, 16.f), z64 = cexp_p(are, aim, 64.f);
                    f2v e[8];
                    e[0] = f2v{1.f, 0.f}; e[1] = z8; e[2] = z16; e[3] = cmul(z16, z8); e[4] = cmul(z16, z16);
                    e[5] = cmul(e[3], z16); e[6] = cmul(e[4], z16); e[7] = cmul(e[4], e[3]);
#pragma unroll
                    for (int i = 0; i < 4; ++i) {
                        f2v t[8];
#pragma unroll
                        for (int j = 0; j < 8; ++j) {
                            if (i < 3) t[j] = cmul_a(e[j], z64);
                            stw(tb, pk2(e[j][0], e[j][1]), (21 + 8 * i + j) * TBL_ROW);
                        }
                        if (i < 3) {
#pragma unroll
                            for (int j = 0; j < 8; ++j) e[j] = cmul_b(e[j], z64, t[j]);
                        }
                    }
                }
                if (w < 4 || w >= 6) {
                    const f2v z1 = cexp_p(are, aim, 1.f), z2 = cexp_p(are, aim, 2.f), z4 = cexp_p(are, aim, 4.f);
                    const f2v z256 = cexp_p(are, aim, 256.f), z1024 = cexp_p(are, aim, 1024.f);
                    const f2v W = cmul(Cc, Bc * dt);
                    auto putx = [&](int row, f2v v) { stw(tb, pk2(v[0], -v[1]), row * TBL_ROW); };
                    const f2v x1 = cmul(W, z1), x2 = cmul(W, z2), x8 = cmul(W, z256), x16 = cmul(W, z1024);
                    const f2v x3 = cmul(x1, z2), x9 = cmul(x8, z2), x12 = cmul(x8, z256), x17 = cmul(x16, z256), x20 = cmul(x16, z1024);
                    const f2v x14 = cmul(x12, z256), x18 = cmul(x17, z256);
                    putx(0, W); putx(1, x1); putx(2, x2); putx(3, x3); putx(8, x8); putx(9, x9); putx(12, x12); putx(14, x14);
                    putx(16, x16); putx(17, x17); putx(18, x18); putx(20, x20);
                    putx(4, cmul(W, z4)); putx(5, cmul(x1, z4)); putx(6, cmul(x2, z4)); putx(7, cmul(x3, z4));
                    putx(10, cmul(x8, z4)); putx(11, cmul(x9, z4)); putx(13, cmul(x12, z4)); putx(15, cmul(x14, z4)); putx(19, cmul(x18, z256));
                }
            }
        }
        lds_wait(L_CNT + 12, 8u);
        ldsw<u4v>(L_W2U + w * 1280 + lane * 16, w2a);
        ldsw<u4v>(lane < 16 ? L_W2U + w * 1280 + 1024 + lane * 16 : L_CNT + 48, w2b);
        STAMP(13);
        BAR_L();
        if (w >= 6) {
            const unsigned tl = L_W2U + (4 * (g & 1) + (l16 >> 2)) * 640 + (lane >> 5) * 32 + (lane & 3) * 8 + (w - 6) * 320;
            bf8v f[5];
#pragma unroll
            for (int k = 0; k < 5; ++k) f[k] = cat(trr(tl + k * 64), trr(tl + k * 64 + 8 * 640));
            lds_count(L_CNT + 16);
#pragma unroll
            for (int k = 0; k < 5; ++k) {
                const int P = 20 * (bid & 1) + 10 * (w - 6) + 2 * k + (lane >> 5), cbb = (P * 52) >> 8, cfr = P - cbb * 5;
                __builtin_amdgcn_raw_buffer_store_b128(__builtin_bit_cast(u4v, f[k]), wsr,
                    (unsigned)(WS_W2T + (bid >> 2) * 5120 + ((bid >> 1) & 1) * 512 + (cbb * 270 + cfr) * 1024 + (lane & 31) * 16) | oob_if(bid >= 216), 0, 16);
            }
        }
        if (w < 2 * nch) {
            const int c = w >> 1, col = 16 * (w & 1) + l16;
            const unsigned ta = L_TBL + c * TBL_CH + l16 * TBL_ROW + g * 16;
            const unsigned te = ta + (21 + 16 * (w & 1)) * TBL_ROW;
            u4v fa[8], fb[8], fe[8];
#pragma unroll
            for (int s = 0; s < 8; ++s) { fe[s] = ldsr<u4v>(te + s * 64); fa[s] = ldsr<u4v>(ta + s * 64); fb[s] = ldsr<u4v>(ta + 16 * TBL_ROW + s * 64); }
            const unsigned tt = L_TT + c * TT_CH;
            const unsigned ta0 = tt + ((col << (3 - max(g - 1, 0))) + ((0x30200100u >> (8 * g)) & 0xffu) * (g == 1 ? 4u : 8u)) * 2;
            const unsigned ta1 = ta0 + (g < 3 ? 4 : 128);
            const unsigned tb0 = tt + (g == 0 ? 512 + col : 644) * 2, st1 = g == 0 ? 64 : 0;
            const unsigned tb1 = g == 1 && col < 2 ? tt + (640 + col) * 2 : tb0;
            SB();
            f4v k0 = {}, k1 = {};
#pragma unroll
            for (int s = 0; s < 8; ++s) {
                k0 = __builtin_amdgcn_mfma_f32_16x16x32_bf16(asbf(fa[s]), asbf(fe[s]), k0, 0, 0, 0);
                k1 = __builtin_amdgcn_mfma_f32_16x16x32_bf16(asbf(fb[s]), asbf(fe[s]), k1, 0, 0, 0);
            }
            lds_wait(L_CNT + 16, 2u);
            ldsw<unsigned>(ta0, pk2(k0[0], k0[1]));
            ldsw<unsigned>(ta1, pk2(k0[2], k0[3]));
            const unsigned q01 = pk2(k1[0], k1[1]), q23 = pk2(k1[2], k1[3]);
            ldsw<u16>(tb1, g == 1 && col == 1 ? (u16)0 : (u16)q01);
            ldsw<u16>(tb0 + st1, (u16)(q01 >> 16));
            ldsw<u16>(tb0 + 2 * st1, (u16)q23);
            ldsw<u16>(tb0 + 3 * st1, (u16)(q23 >> 16));
        }
        BAR_L();
        STAMP(14);
        {
            const bool two = w < 4 && nch == 3;
            const int t = tid & 255;
#pragma unroll
            for (int k = 0; k < 2; ++k) {
                if (k == 0 || two) {
                    unsigned pe[6], po[5];
#pragma unroll
                    for (int i = 0; i < 5; ++i) pe[i] = __builtin_bit_cast(unsigned, h2v{ldsr<u16>(ix[2 * i] + k * 2 * TT_CH), ldsr<u16>(ix[2 * i + 1] + k * 2 * TT_CH)});
                    pe[5] = ldsr<u16>(ix[10] + k * 2 * TT_CH);
                    if (k == 0) { stage1(2, 2, 0); stage1(2, 2, 1); stage1(2, 2, 2); stage1(2, 2, 3); }
#pragma unroll
                    for (int i = 0; i < 5; ++i) po[i] = __builtin_amdgcn_alignbit(pe[i + 1], pe[i], 16);
                    const unsigned cbase = L_KC + (tw + 2 * k) * KC_CH, d = cbase + 32 + t * 16;
                    ldsw<u4v>(d, u4v{po[1], po[2], po[3], po[4]});
                    ldsw<u4v>(d + KC_CP, u4v{pe[1], pe[2], pe[3], pe[4]});
                    ldsw<u4v>(d + 2 * KC_CP, u4v{po[0], po[1], po[2], po[3]});
                    ldsw<u4v>(d + 3 * KC_CP, u4v{pe[0], pe[1], pe[2], pe[3]});
                }
            }
            if (w == 7) ldsw<u4v>(L_KC + min(lane >> 1, 11) * KC_CP + (lane & 1) * 16, u4v{0u, 0u, 0u, 0u});
        }
        STAMP(1);
        f4v acc1[2][4];
#pragma unroll
        for (int i = 0; i < 8; ++i) acc1[i >> 2][i & 3] = f4v{};
        unsigned bx[2], aw[2];
        {
            const int q4 = l16 >> 2, p4 = lane & 3;
#pragma unroll
            for (int i = 0; i < 2; ++i) {
                bx[i] = (64 * tw + l16) * 128 + (((4 * i + g) ^ (l16 >> 1)) << 4);
                aw[i] = 16384 + (8 * g + q4) * 256 + (((2 * fw + i) ^ q4 ^ ((g & 1) << 2)) << 5) + p4 * 8;
            }
        }
        bf8v fa[2][2][2], fb[2][2][4];
        auto rd = [&](int buf, int i) {
            const int ks = i >> 1, h = i & 1;
            fa[buf][ks][h] = cat(trr(aw[h] + ks * 8192), trr(aw[h] + ks * 8192 + 1024));
            fb[buf][ks][2 * h] = asbf(ldsr<u4v>(bx[ks] + h * 4096));
            fb[buf][ks][2 * h + 1] = asbf(ldsr<u4v>(bx[ks] + h * 4096 + 2048));
        };
        unsigned sb = 0;
        int kt = rot + 2; kt = kt >= 10 ? kt - 10 : kt;
        auto tile = [&](int cur, bool more, bool stage, auto&& side) {
            const unsigned d = sb == 2 * SLOT ? (unsigned)(-2 * (int)SLOT) : SLOT, lb = sb + w * 2048;
            kt = kt == 9 ? 0 : kt + 1;
            sb += d; aw[0] += d; aw[1] += d; bx[0] += d; bx[1] += d;
#pragma unroll
            for (int i = 0; i < 4; ++i) {
                SB();
                if (more) rd(cur ^ 1, i);
                SB();
#pragma unroll
                for (int j = 0; j < 4; ++j) acc1[i & 1][j] = __builtin_amdgcn_mfma_f32_16x16x32_bf16(fa[cur][i >> 1][i & 1], fb[cur][i >> 1][j], acc1[i & 1][j], 0, 0, 0);
                SB();
                if (stage) {
                    if (i & 1) dma16(rw, vw, kt * (64 * 3456) + (i >> 1) * 4 * 3456, lb + (i >> 1) * 1024 + 16384);
                    else dma16(rx, vx[i >> 1], kt * 128, lb + (i >> 1) * 1024);
                }
                side(i);
            }
            SB();
        };
        auto none = [&](int) {};
        if (w >= 6) {
            WAIT_V(4);
            arrive_publish(L_CNT + 8, ws, WS_FW + min(bid, 216) * 128, tag, 1u);
        }
        BAR_L();
        STAMP(15);
#pragma unroll
        for (int i = 0; i < 4; ++i) rd(0, i);
        u4v uld[2], cbv;
        f2v sv[4];
        u64 fpoll = 0;
        const unsigned f1a = WS_FS + 2 * rb * 128, f1b = WS_FC + 2 * cb * 128;
        const unsigned f1_off = min(((lane & 2) ? f1b : f1a) + (lane & 1) * 128, WS_FC + 26 * 128);
        const unsigned f2_off = w == 2 ? WS_FA + (lane & 31) * 128 : WS_FW + (2 * min(54 * w + lane, 107) + (cb2 >> 2)) * 128;
        WAIT_BAR(4); tile(0, true, true, none);
        WAIT_BAR(4); tile(1, true, true, none);
        WAIT_BAR(4); tile(0, true, true, [&](int i) { if (i == 0 && w < 2) fpoll = ld_flag(ws, f2_off); });
        if (w < 2 && !__all(fpoll == tag)) wait_flags(ws, f2_off, tag);
        WAIT_BAR(4);
        STAMP(12);
        tile(1, true, true, [&](int i) { if (i == 0) loadb(0); if (i == 2) loadb(1); });
        WAIT_BAR(6); tile(0, true, true, [&](int i) { if (i == 0) loadb(2); if (i == 2) loadb(3); });
        WAIT_BAR(6); tile(1, true, true, [&](int i) { if (i == 0) loadb(4); if (i == 2) loadb(5); });
        WAIT_BAR(6); tile(0, true, true, [&](int i) { if (i == 0) loadb(6); if (i == 2) loadb(7); });
        WAIT_BAR(6); tile(1, true, false, [&](int i) { if (i == 0 && w == 0) fpoll = ld_flag(ws, f1_off); loadb(8 + i); });
        if (w == 0 && !__all(fpoll == tag)) wait_flags(ws, f1_off, tag);
        WAIT_BAR(4);
        STAMP(3);
        tile(0, true, false, [&](int i) {
            sv[i] = __builtin_bit_cast(f2v, __builtin_amdgcn_raw_buffer_load_b64(wsr, l16 * 8, WS_STATS + (rb * 128 + 64 * tw + 16 * i) * 8, 16));
            if (i == 0 && w == 1) cbv = __builtin_amdgcn_raw_buffer_load_b128(wsr, (lane >> 5) * (WS_B1 - WS_CS) + (lane & 31) * 16, WS_CS + cb * 512, 16);
            if (i == 0 && w == 2) fpoll = ld_flag(ws, f2_off);
            loadb(12 + i);
        });
        tile(1, false, false, [&](int i) { loadb(16 + i); });
        if (w == 1) ldsw<u4v>(L_CB + lane * 16, cbv);
        if (w >= 2 && w < 5) ldsw<u4v>(L_UA + (w - 2) * UA_CH + min(lane, 29) * 16, u4v{0u, 0u, 0u, 0u});
        if (w == 2 && !__all(fpoll == tag)) wait_flags(ws, f2_off, tag);
        BAR_L();
        STAMP(2);
        stage_x();
        {
            f4v cs[2], bb[2];
#pragma unroll
            for (int u = 0; u < 2; ++u) { cs[u] = ldsr<f4v>(L_CB + (32 * fw + 16 * u + 4 * g) * 4); bb[u] = ldsr<f4v>(L_CB + 512 + (32 * fw + 16 * u + 4 * g) * 4); }
            const int s = 4 * cb + fw;
            const unsigned ho = (unsigned)(WS_HA + ((2 * rb + tw) * 54 + s) * 4096 + lane * 16) | oob_if(s >= 54);
            u4v hp[4];
#pragma unroll
            for (int ti = 0; ti < 4; ++ti) {
                const f2v rs2 = {sv[ti][0], sv[ti][0]}, nm2 = {-sv[ti][1], -sv[ti][1]};
#pragma unroll
                for (int i = 0; i < 4; ++i) {
                    const int slot = 4 * ti + i, u = i >> 1, e = 2 * (i & 1);
                    if (slot < 2) {
                        const int q = w + 8 * slot, c = q >> 2;
                        uld[slot] = __builtin_amdgcn_raw_buffer_load_b128(wsr, lane * 16 | oob_if(c >= nch), WS_XT + (ch0 + c) * 4096 + (q & 3) * 1024, 16);
                    }
                    if (slot >= 2 && slot < 12) loadb(18 + slot);
                    SB();
                    const f2v v = __builtin_elementwise_fma(rs2, f2v{acc1[u][ti][e], acc1[u][ti][e + 1]}, __builtin_elementwise_fma(nm2, f2v{cs[u][e], cs[u][e + 1]}, f2v{bb[u][e], bb[u][e + 1]}));
                    const f2v z = v * __builtin_elementwise_fma(v * v, f2v{-0.10294324f, -0.10294324f}, f2v{-2.30220820f, -2.30220820f});
                    const f2v d = f2v{__builtin_amdgcn_exp2f(z[0]), __builtin_amdgcn_exp2f(z[1])} + f2v{1.f, 1.f};
                    const f2v gl = v * f2v{__builtin_amdgcn_rcpf(d[0]), __builtin_amdgcn_rcpf(d[1])};
                    hp[ti][i] = pk2(gl[0], gl[1]);
                    asm volatile("" : "+v"(hp[ti][i]));
                }
            }
            SB();
#pragma unroll
            for (int ti = 0; ti < 4; ++ti)
                __builtin_amdgcn_raw_buffer_store_b128(hp[ti], wsr, ho + 1024 * ti, 0, 16);
        }
#pragma unroll
        for (int i = 0; i < 2; ++i) {
            const int q = w + 8 * i, c = q >> 2;
            if (q < 12) ldsw<u4v>(L_UA + c * UA_CH + UA_PAD + (q & 3) * 1024 + lane * 16, uld[i]);
        }
        STAMP(4);
        BAR_L();
        {
            const int typ = w < 3 ? 0 : w < 6 ? 1 : 2;
            const int c1 = w < 3 ? w : w < 6 ? w - 3 : 2 * (w - 6), c2 = w == 6 ? 1 : c1;
            const unsigned kl = L_KC + (l16 >> 2) * KC_CP + 56 - 16 * g + 8 * (lane & 3);
            const unsigned ul = L_UA + UA_PAD + 32 * l16 - 32 + 16 * g;
            unsigned ka[3], ua[3];
            ka[0] = kl + c1 * KC_CH;
            ka[1] = typ == 2 ? kl + c2 * KC_CH - 4 * 512 : ka[0];
            ka[2] = typ == 1 ? ka[0] - 6 * 512 : ka[1];
            ua[0] = ul + c1 * UA_CH - typ * 1024;
            ua[2] = ul + c2 * UA_CH;
            ua[1] = typ == 2 ? ua[2] : ua[0];
            bf8v uf[8], kf[8];
            auto rdk = [&](int sl) { const unsigned ad = ka[sl < 4 ? 0 : sl < 6 ? 1 : 2] + 512u * sl; kf[sl] = cat(trr(ad + 8u), trr(ad)); };
            auto rdu = [&](int j) { uf[j] = asbf(ldsr<u4v>(ua[j >= 4 ? 0 : j >= 2 ? 1 : 2] + 512u * j)); };
#pragma unroll
            for (int sl = 0; sl < 8; ++sl) { rdk(sl); rdu(7 - sl); }
            f4v A[3] = {}, B[5] = {};
            const unsigned fo = WS_FH + (rb * 14 + cb) * 128;
#pragma nounroll
            for (int rho = 0; rho < 7; ++rho) {
#pragma unroll
                for (int i = 0; i < 3; ++i) { ka[i] += 64u; ua[i] -= 64u; asm volatile("" : "+v"(ka[i]), "+v"(ua[i])); }
#pragma unroll
                for (int sl = 0; sl < 8; ++sl) {
                    SB();
                    const int ia = sl < 4 ? 0 : sl < 6 ? 1 : 2, ib = sl < 3 ? 0 : sl - 2;
                    A[ia] = __builtin_amdgcn_mfma_f32_16x16x32_bf16(kf[sl], uf[7 - sl], A[ia], 0, 0, 0);
                    if (sl < 7) B[ib] = __builtin_amdgcn_mfma_f32_16x16x32_bf16(kf[sl], uf[6 - sl], B[ib], 0, 0, 0);
                    SB();
                    rdk(sl); rdu(7 - sl);
                }
                SB();
                if (rho == PUB_RHO) {
                    WAIT_V(0);
                    arrive_publish(L_CNT, ws, fo, tag);
                }
            }
#pragma unroll
            for (int sl = 0; sl < 8; ++sl) {
                const int ia = sl < 4 ? 0 : sl < 6 ? 1 : 2, ib = sl < 3 ? 0 : sl - 2;
                A[ia] = __builtin_amdgcn_mfma_f32_16x16x32_bf16(kf[sl], uf[7 - sl], A[ia], 0, 0, 0);
                if (sl < 7) B[ib] = __builtin_amdgcn_mfma_f32_16x16x32_bf16(kf[sl], uf[6 - sl], B[ib], 0, 0, 0);
            }
            SB();
            f4v y[4];
            if (typ == 0) { y[0] = A[0] + A[1] + A[2]; y[1] = (B[0] + B[1]) + (B[2] + B[3]) + B[4]; y[2] = y[0]; y[3] = y[1]; }
            else if (typ == 1) { y[0] = A[0] + A[1]; y[1] = B[0] + B[1] + B[2]; y[2] = A[2]; y[3] = B[4]; }
            else { y[0] = A[0]; y[1] = B[0]; y[2] = A[1] + A[2]; y[3] = B[2] + B[3] + B[4]; }
#pragma unroll
            for (int i = 0; i < 4; ++i) {
                const int c = i < 2 ? c1 : c2, tau = (i < 2 ? 7 - 2 * typ : 2 * typ - 1) - (i & 1);
                const bool on = c < nch && (i < 2 || (typ != 0 && w != 7));
                const u2v uv = ldsr<u2v>(L_UA + c * UA_CH + UA_PAD + 512 * max(tau, 0) + 32 * l16 + 8 * g);
                const float dk = c == 0 ? bflo(dsk01) : c == 1 ? bfhi(dsk01) : bflo(dsk2);
                const u2v o = {pk2(y[i][0] + dk * bflo(uv[0]), y[i][1] + dk * bfhi(uv[0])), pk2(y[i][2] + dk * bflo(uv[1]), y[i][3] + dk * bfhi(uv[1]))};
                __builtin_amdgcn_raw_buffer_store_b64(o, wsr, 32 * l16 + 8 * g, (WS_YT + (ch0 + c) * 4096 + 512 * max(tau, 0)) | oob_if(!on), 16);
            }
        }
        STAMP(5);
    } else {
        const int a = (r - 28) * 8 + xcd;
        const u4v rx = mkrsrc(x, 2048 * 1280);
        {
            unsigned vo[5];
#pragma unroll
            for (int i = 0; i < 5; ++i) {
                const int n = i * 64 + lane, rl = n / 80, cp = n - rl * 80;
                vo[i] = (a * 64 + 8 * w + rl) * 1280 + ((cp ^ (2 * w) ^ ((rl >> 1) << 3)) << 4);
                dma16(rx, vo[i], 0, L_AX + (w * 10 + i) * 1024);
            }
#pragma unroll
            for (int i = 5; i < 10; ++i) dma16(rx, vo[i - 5], 4 * 1280, L_AX + (w * 10 + i) * 1024);
        }
        const u64 e0 = ld_flag(ws, WS_E);
        u4v cv[10];
        {
            const auto rw1 = __builtin_amdgcn_make_buffer_rsrc((void*)(u64)w1, 0, 640 * 3456, 0x00020000);
#pragma unroll
            for (int i = 0; i < 10; ++i) cv[i] = __builtin_amdgcn_raw_buffer_load_b128(rw1, ((lane >> 3) * 3456 + (lane & 7) * 16) | oob_if(a >= 27), (80 * w + 8 * i) * 3456 + a * 128, 0);
        }
        if (w == 0) ldsw<unsigned>(L_ACNT + (lane & 7) * 4, 0u);
        STAMP(10);
        WAIT_V(11);
        STAMP(11);
        {
            const int row = 8 * w + (lane >> 3), sw = (2 * w) ^ (((row >> 1) & 1) << 3);
            u4v v[10];
#pragma unroll
            for (int i = 0; i < 10; ++i) v[i] = ldsr<u4v>(L_AX + row * 1280 + (((lane & 7) + 8 * i) ^ sw) * 16);
            SB();
            float s1[2] = {0.f, 0.f}, s2[2] = {0.f, 0.f};
#pragma unroll
            for (int i = 0; i < 10; ++i)
#pragma unroll
                for (int j = 0; j < 4; ++j) { s1[j & 1] = dot2(v[i][j], 0x3f803f80u, s1[j & 1]); s2[j & 1] = dot2(v[i][j], v[i][j], s2[j & 1]); }
            const f2v sv = ln_stats(sum8(s1[0] + s1[1]), sum8(s2[0] + s2[1]));
            ldsw<f2v>(L_AST + row * 8, sv);
            __builtin_amdgcn_raw_buffer_store_b64(__builtin_bit_cast(u2v, sv), wsr, (WS_STATS + (a * 64 + row) * 8) | oob_if(lane & 7), 0, 16);
        }
        BAR_L();
        STAMP(14);
        {
            const int o = lane & 7, cq = lane >> 3;
            f2v rs2[4], nm2[4];
#pragma unroll
            for (int i = 0; i < 4; ++i) { const f4v q = ldsr<f4v>(L_AST + o * 64 + i * 16); rs2[i] = f2v{q[0], q[2]}; nm2[i] = f2v{-q[1], -q[3]}; }
            u2v v[3][8];
#pragma unroll
            for (int k = 0; k < 3; ++k)
#pragma unroll
                for (int t = 0; t < 8; ++t) v[k][t] = ldsr<u2v>(L_AX + (8 * o + t) * 1280 + ((4 * min(w + 8 * k, 19) + (cq >> 1)) ^ ((2 * o) ^ (((t >> 1) & 1) << 3))) * 16 + (cq & 1) * 8);
            SB();
#pragma unroll
            for (int k = 0; k < 3; ++k) {
                if (k < 2 || w < 4) {
#pragma unroll
                    for (int e = 0; e < 4; ++e) {
                        u4v ov;
#pragma unroll
                        for (int p = 0; p < 4; ++p) {
                            const unsigned va = v[k][2 * p][e >> 1], vb = v[k][2 * p + 1][e >> 1];
                            const f2v n2 = __builtin_elementwise_fma((e & 1) ? f2v{bfhi(va), bfhi(vb)} : f2v{bflo(va), bflo(vb)}, rs2[p], nm2[p]);
                            ov[p] = pk2(n2[0], n2[1]);
                        }
                        __builtin_amdgcn_raw_buffer_store_b128(ov, wsr, WS_XT + (32 * (w + 8 * k) + 4 * cq + e) * 4096 + a * 128 + o * 16, 0, 16);
                        SB();
                    }
                }
                if (k == 0) {
                    WAIT_V(4);
                    tag = mk64(__builtin_amdgcn_readfirstlane((unsigned)e0), __builtin_amdgcn_readfirstlane((unsigned)(e0 >> 32))) + 1ull;
                    arrive_publish(L_ACNT + 8, ws, WS_FS + a * 128, tag);
                }
            }
        }
        STAMP(15);
        {
            unsigned selo = 0x00003f80u, sehi = 0x3f800000u;
            asm volatile("" : "+v"(selo), "+v"(sehi));
            float sm[8] = {0.f, 0.f, 0.f, 0.f, 0.f, 0.f, 0.f, 0.f};
#pragma unroll
            for (int i = 0; i < 10; ++i)
#pragma unroll
                for (int j = 0; j < 4; ++j) { sm[2 * j] = dot2(cv[i][j], selo, sm[2 * j]); sm[2 * j + 1] = dot2(cv[i][j], sehi, sm[2 * j + 1]); }
#pragma unroll
            for (int j = 0; j < 8; ++j) sm[j] = dpp_add<0x128>(sm[j]);
            ldsw<f4v>(L_ACS + (4 * w + g) * 256 + (lane & 7) * 32 + (lane & 8) * 2, (lane & 8) ? f4v{sm[4], sm[5], sm[6], sm[7]} : f4v{sm[0], sm[1], sm[2], sm[3]});
        }
        BAR_L();
        if (w != 7 || a >= 27) {
            WAIT_V(0);
            arrive_publish(L_ACNT + 12, ws, WS_FA + a * 128, tag);
        } else {
            const float bf = bf2f(b1[64 * a + lane]);
            float p[32], sum = 0.f;
#pragma unroll
            for (int i = 0; i < 32; ++i) p[i] = ldsr<float>(L_ACS + i * 256 + lane * 4);
#pragma unroll
            for (int i = 0; i < 32; i += 4) sum += (p[i] + p[i + 1]) + (p[i + 2] + p[i + 3]);
            __builtin_amdgcn_raw_buffer_store_b32(__builtin_bit_cast(unsigned, sum), wsr, WS_CS + (64 * a + lane) * 4, 0, 16);
            __builtin_amdgcn_raw_buffer_store_b32(__builtin_bit_cast(unsigned, bf), wsr, WS_B1 + (64 * a + lane) * 4, 0, 16);
            WAIT_V(0);
            raise_flag(ws, WS_FC + a * 128, tag);
            arrive_publish(L_ACNT + 12, ws, WS_FA + a * 128, tag);
        }
        STAMP(2);
        if (w < 2) wait_flags(ws, WS_FW + (2 * min(54 * w + lane, 107) + (cb2 >> 2)) * 128, tag);
        BAR_L();
        STAMP(3);
        stage_x();
#pragma unroll
        for (int n = 0; n < 30; ++n) loadb(n);
        __builtin_amdgcn_s_waitcnt(0x0F70);
    }

    asm volatile("" : "+s"(rot2));
    if (w == 0) wait_flags(ws, WS_FH + ((rb2 >> 1) * 14 + min(lane, 13)) * 128, tag);
    BAR_L();
    STAMP(6);
    u4v ha[7][4];
    auto loada = [&](int i, int t) { ha[i][t] = __builtin_amdgcn_raw_buffer_load_b128(wsr, lane * 16, WS_HA + ((rb2 * 54 + sidx(i)) * 4 + t) * 1024, 16); };
#pragma unroll
    for (int i = 0; i < 8; ++i) loada(i >> 2, i & 3);
    if (is_gemm) {
        WAIT_V(8);
        arrive_publish(L_CNT + 4, ws, WS_FY + bid * 128, tag);
    }
    const int c0 = 80 * cb2;
    int lz = lane;
    asm volatile("" : "+v"(lz));
    const int cA = c0 + 10 * w, cB = cA + 9;
    const int own0 = max((cA * 0xaaab) >> 17, (cA - 192) >> 1), own1 = max((cB * 0xaaab) >> 17, (cB - 192) >> 1);
    u64 ypoll = tag;
    u4v y0, y1;
    f4v acc2[4][5];
#pragma unroll
    for (int i = 0; i < 20; ++i) acc2[i / 5][i % 5] = f4v{};
#pragma unroll
    for (int i = 0; i < 5; ++i) {
#pragma unroll
        for (int t = 0; t < 4; ++t) {
            SB();
#pragma unroll
            for (int c = 0; c < 5; ++c) acc2[t][c] = __builtin_amdgcn_mfma_f32_16x16x32_bf16(asbf(ha[i][t]), asbf(bw[5 * i + c]), acc2[t][c], 0, 0, 0);
            SB();
            if (i > 0) loada(i + 2, t);
            else if (t == 3) { loada(2, 0); loada(2, 1); loada(2, 2); loada(2, 3); }
            if (i == 1 || (i == 2 && t == 0)) loadb(30 + 4 * (i - 1) + t);
            if (i == 3 && t == 0) ypoll = ld_flag(ws, WS_FY + min(own0 + lz, own1) * 128);
            if (i == 4 && t == 3) {
                if (!__all(ypoll == tag)) wait_flags(ws, WS_FY + min(own0 + lz, own1) * 128, tag);
                y0 = __builtin_amdgcn_raw_buffer_load_b128(wsr, (lz >> 3) * 4096 + (lz & 7) * 16, WS_YT + cA * 4096 + rb2 * 128, 16);
                y1 = __builtin_amdgcn_raw_buffer_load_b128(wsr, (unsigned)((8 + (lz >> 3)) * 4096 + (lz & 7) * 16) | oob_if(lz >= 16), WS_YT + cA * 4096 + rb2 * 128, 16);
            }
        }
    }
    STAMP(7);
    auto dump = [&](int t) {
        unsigned p[10];
#pragma unroll
        for (int c = 0; c < 5; ++c) { p[2 * c] = pk2(acc2[t][c][0], acc2[t][c][1]); p[2 * c + 1] = pk2(acc2[t][c][2], acc2[t][c][3]); }
        addtid10(L_RED + w * RED_W + t * 5 * RED_T, p);
    };
#pragma unroll
    for (int t = 0; t < 4; ++t) {
        SB();
#pragma unroll
        for (int c = 0; c < 5; ++c) acc2[t][c] = __builtin_amdgcn_mfma_f32_16x16x32_bf16(asbf(ha[5][t]), asbf(bw[25 + c]), acc2[t][c], 0, 0, 0);
        SB();
        if (t > 0) dump(t - 1);
        SB();
#pragma unroll
        for (int c = 0; c < 5; ++c) acc2[t][c] = __builtin_amdgcn_mfma_f32_16x16x32_bf16(asbf(ha[6][t]), asbf(bw[30 + c]), acc2[t][c], 0, 0, 0);
    }
    SB();
    ldsw<u4v>(L_YS + (10 * w + (lz >> 3)) * YS_ROW + (lz & 7) * 16, y0);
    ldsw<u4v>(L_YS + min(10 * w + 8 + (lz >> 3) + (lz >= 16 ? 80 : 0), 80) * YS_ROW + (lz & 7) * 16, y1);
    dump(3);
    BAR_L();
    STAMP(8);
    {
        unsigned selo = 0x00003f80u, sehi = 0x3f800000u;
        asm volatile("" : "+v"(selo), "+v"(sehi));
#pragma unroll
        for (int k = 0; k < 3; ++k) {
            if (k < 2 || w < 4) {
                const int id = w * 64 + lz + 512 * k, tp = id / 40, c2 = id - tp * 40;
                const unsigned ra = L_RED + ((tp >> 3) * 5 + (c2 >> 3)) * RED_T + (tp & 1) * RED_ROW + ((tp >> 1) & 3) * 64 + (c2 & 7) * 8;
                u2v p[8];
#pragma unroll
                for (int j = 0; j < 8; ++j) p[j] = ldsr<u2v>(ra + j * RED_W);
                const unsigned ya = ldsr<unsigned>(L_YS + 2 * c2 * YS_ROW + tp * 4), yb = ldsr<unsigned>(L_YS + (2 * c2 + 1) * YS_ROW + tp * 4);
                const unsigned xa = ldsr<unsigned>(L_XS + tp * 320 + c2 * 4), xb = ldsr<unsigned>(L_XS + tp * 320 + 160 + c2 * 4), bv = ldsr<unsigned>(L_XS + 10240 + c2 * 4);
                float o00 = bflo(ya) + bflo(xa) + bflo(bv), o01 = bflo(yb) + bfhi(xa) + bfhi(bv);
                float o10 = bfhi(ya) + bflo(xb) + bflo(bv), o11 = bfhi(yb) + bfhi(xb) + bfhi(bv);
#pragma unroll
                for (int j = 0; j < 8; ++j) {
                    o00 = dot2(p[j][0], selo, o00); o10 = dot2(p[j][0], sehi, o10);
                    o01 = dot2(p[j][1], selo, o01); o11 = dot2(p[j][1], sehi, o11);
                }
                const auto ro = __builtin_amdgcn_make_buffer_rsrc((void*)(u64)out, 0, 2048 * 1280, 0x00020000);
                const unsigned oo = ((rb2 * 64 + 2 * tp) * 640 + c0 + 2 * c2) * 2;
                __builtin_amdgcn_raw_buffer_store_b32(pk2(o00, o01), ro, oo, 0, OUT_AUX);
                __builtin_amdgcn_raw_buffer_store_b32(pk2(o10, o11), ro, oo + 1280, 0, OUT_AUX);
            }
        }
    }
    if (bid == 0 && w == 5) {
#pragma unroll
        for (int i = 0; i < 4; ++i) wait_flags(ws, WS_FH + (i * 64 + lz) * 128, tag);
        raise_flag(ws, WS_E, tag);
    }
}

extern "C" void kernel_launch(void* const* d_in, const int* in_sizes, int n_in, void* d_out, int out_size, void* d_ws, size_t ws_size, hipStream_t stream) {
    (void)in_sizes; (void)n_in; (void)out_size; (void)ws_size;
    (void)hipFuncSetAttribute((const void*)s4ffn_fused, hipFuncAttributeMaxDynamicSharedMemorySize, LDS_BYTES);
    s4ffn_fused<<<256, 512, LDS_BYTES, stream>>>((const u16*)d_in[0], (const u16*)d_in[1], (const u16*)d_in[2], (const u16*)d_in[3], (const u16*)d_in[4],
                                                 (const u16*)d_in[5], (const u16*)d_in[6], (const u16*)d_in[7], (const u16*)d_in[8], (const u16*)d_in[10],
                                                 (const u16*)d_in[11], (const u16*)d_in[12], (const u16*)d_in[13], (u16*)d_out, (char*)d_ws);
}
```

```cpp
#include <hip/hip_runtime.h>
#include <stdint.h>
#define PUB_RHO 3
#define CSUM_ON 1
#define CODE_CHUNKS 4
#define CODE_BYTES 28672u
#define PACE 0
#define PT0 7
#define ARR 0

typedef unsigned short u16;
typedef unsigned long long u64;
typedef short s4v __attribute__((ext_vector_type(4)));
typedef __bf16 bf8v __attribute__((ext_vector_type(8)));
typedef __bf16 bf2v __attribute__((ext_vector_type(2)));
typedef float f2v __attribute__((ext_vector_type(2)));
typedef float f4v __attribute__((ext_vector_type(4)));
typedef float f16v __attribute__((ext_vector_type(16)));
typedef unsigned u2v __attribute__((ext_vector_type(2)));
typedef unsigned short h2v __attribute__((ext_vector_type(2)));
typedef unsigned u4v __attribute__((ext_vector_type(4)));
typedef __attribute__((address_space(1))) u64 gu64;
#define LDSP(T) __attribute__((address_space(3))) T*
#define DI __device__ __forceinline__
#define RLX_AGENT __ATOMIC_RELAXED, __HIP_MEMORY_SCOPE_AGENT

constexpr unsigned WS_E = 0;
constexpr unsigned WS_FH = 4096;
constexpr unsigned WS_FA = 32768;
constexpr unsigned WS_FW = 81920;
constexpr unsigned WS_FS = 40960;
constexpr unsigned WS_FC = 45056;
constexpr unsigned WS_FY = 49152;
constexpr unsigned WS_STATS = 131072;
constexpr unsigned WS_CS = 196608, WS_B1 = 204800;
constexpr unsigned WS_STAMP = 262144;
constexpr unsigned WS_XT = 1u << 20;
constexpr unsigned WS_W2T = 4u << 20;
constexpr unsigned WS_YT = 8u << 20;
constexpr unsigned WS_HA = 12u << 20;
constexpr unsigned WS_BYTES = 20u << 20;
constexpr unsigned SLOT = 32768;
constexpr unsigned L_KC = 98304, KC_CH = 16640, KC_CP = 4160;
constexpr unsigned L_UA = 148224, UA_CH = 4576, UA_PAD = 480;
constexpr unsigned L_MISC = 162368, L_CB = L_MISC, L_CNT = L_MISC + 1024;
constexpr unsigned L_TBL = 65536, TBL_ROW = 544, TBL_CH = 53 * TBL_ROW;
constexpr unsigned L_PRM = 152064, L_TT = 158464, TT_CH = 1296, L_W2U = L_UA;
constexpr unsigned L_SCR = L_CB;
constexpr unsigned L_XS = 0, L_RED = 16384, L_YS = L_RED + 81920, YS_ROW = 144, L_OUT = L_YS + 81 * YS_ROW + 112, OUT_ROW = 176;
constexpr unsigned L_AX = 0, L_AST = 81920, L_ACNT = L_AST + 512, L_ACS = L_AST + 1024;
static_assert(L_TT + 3 * TT_CH <= L_MISC && L_CNT + 64 <= 163840 && L_W2U + 10240 <= L_TT, "LDS map");
constexpr unsigned LDS_BYTES = 163840;
constexpr int SPIN_LIMIT = 400000;

#define STAMP(i)

DI float bf2f(unsigned v) { return __uint_as_float(v << 16); }
DI float bflo(unsigned v) { return __uint_as_float(v << 16); }
DI float bfhi(unsigned v) { return __uint_as_float(v & 0xffff0000u); }
DI unsigned pk2(float a, float b) { bf2v v; v[0] = (__bf16)a; v[1] = (__bf16)b; return __builtin_bit_cast(unsigned, v); }
DI float rbf(float a) { return bflo(pk2(a, a)); }
template <typename P> DI u4v mkrsrc(P p, unsigned bytes) { u64 a = (u64)p; u4v r; r[0] = (unsigned)a; r[1] = (unsigned)(a >> 32); r[2] = bytes; r[3] = 0x00020000u; return r; }
DI void dma16(u4v rs, unsigned voff, unsigned soff, unsigned lds) {
    asm volatile("s_mov_b32 m0, %3\n\ts_nop 0\n\tbuffer_load_dwordx4 %0, %1, %2 offen lds" ::"v"(voff), "s"(rs), "s"(soff), "s"(lds) : "memory");
}
DI void dma16g(const void* p, unsigned lds) {
    asm volatile("s_mov_b32 m0, %1\n\ts_nop 0\n\tglobal_load_lds_dwordx4 %0, off" ::"v"(p), "s"(lds) : "memory");
}
DI void dma16g_lanes3(const void* p, unsigned lds) {
    u64 ex;
    asm volatile("s_mov_b64 %0, exec\n\ts_mov_b64 exec, 7\n\ts_mov_b32 m0, %2\n\ts_nop 0\n\tglobal_load_lds_dwordx4 %1, off\n\ts_mov_b64 exec, %0" : "=&s"(ex) : "v"(p), "s"(lds) : "memory");
}
DI void dma16_lanes16(u4v rs, unsigned voff, unsigned soff, unsigned lds) {
    u64 ex;
    asm volatile("s_mov_b64 %0, exec\n\ts_mov_b64 exec, 0xffff\n\ts_mov_b32 m0, %4\n\ts_nop 0\n\tbuffer_load_dwordx4 %1, %2, %3 offen lds\n\ts_mov_b64 exec, %0"
                 : "=&s"(ex) : "v"(voff), "s"(rs), "s"(soff), "s"(lds) : "memory");
}
DI void touch_code(unsigned chunk, int lane, unsigned lds) {
    u64 pc;
    asm volatile("s_getpc_b64 %0" : "=s"(pc));
    const u64 p = (pc & ~127ull) + min(chunk * 8192u + lane * 128u, CODE_BYTES - 128u);
    asm volatile("s_mov_b32 m0, %1\n\ts_nop 0\n\tglobal_load_lds_dword %0, off" ::"v"(p), "s"(lds) : "memory");
}
#define SB() __builtin_amdgcn_sched_barrier(0)
#define WAIT_BAR(n) asm volatile("s_waitcnt vmcnt(" #n ") lgkmcnt(0)\n\ts_barrier" ::: "memory")
#define WAIT_BARN(n) asm volatile("s_waitcnt vmcnt(%0) lgkmcnt(0)\n\ts_barrier" ::"i"(n) : "memory")
#define BAR_L() asm volatile("s_waitcnt lgkmcnt(0)\n\ts_barrier" ::: "memory")
#define WAIT_V(n) asm volatile("s_waitcnt vmcnt(" #n ")" ::: "memory")
template <typename T> DI T ldsr(unsigned a) { return *(LDSP(T))a; }
template <typename T> DI void ldsw(unsigned a, T v) { *(LDSP(T))a = v; }
DI void stw(unsigned a, unsigned v, int off) { asm volatile("ds_write_b32 %0, %1 offset:%2" ::"v"(a), "v"(v), "i"(off) : "memory"); }
DI s4v trr(unsigned a) { return __builtin_amdgcn_ds_read_tr16_b64_v4i16((LDSP(s4v))a); }
DI bf8v cat(s4v a, s4v b) { return __builtin_bit_cast(bf8v, __builtin_shufflevector(a, b, 0, 1, 2, 3, 4, 5, 6, 7)); }
DI bf8v asbf(u4v a) { return __builtin_bit_cast(bf8v, a); }
template <int CTRL> DI float dpp_add(float v) { return v + __builtin_bit_cast(float, __builtin_amdgcn_update_dpp(0, __builtin_bit_cast(int, v), CTRL, 0xf, 0xf, true)); }
DI float sum8(float v) { return dpp_add<0x141>(dpp_add<0x4e>(dpp_add<0xb1>(v))); }
DI float dot2(unsigned a, unsigned b, float c) { return __builtin_amdgcn_fdot2_f32_bf16(__builtin_bit_cast(bf2v, a), __builtin_bit_cast(bf2v, b), c, false); }
DI u64 ld_flag(const char* ws, unsigned off) { return __hip_atomic_load((gu64*)(ws + off), RLX_AGENT); }
DI void wait_flags(const char* ws, unsigned off, u64 tag) {
#pragma nounroll
    for (int it = 0; it < SPIN_LIMIT; ++it) {
        if (__all(ld_flag(ws, off) == tag)) break;
        __builtin_amdgcn_s_sleep(1);
    }
    asm volatile("" ::: "memory");
}
DI unsigned lds_count(unsigned cnt_lds) {
    unsigned old, one = 1u;
    u64 ex;
    asm volatile("s_mov_b64 %1, exec\n\ts_mov_b64 exec, 1\n\ts_waitcnt lgkmcnt(0)\n\tds_add_rtn_u32 %0, %2, %3\n\ts_waitcnt lgkmcnt(0)\n\ts_mov_b64 exec, %1"
                 : "=&v"(old), "=&s"(ex) : "v"(cnt_lds), "v"(one) : "memory");
    return __builtin_amdgcn_readfirstlane(old);
}
DI void raise_flag(char* ws, unsigned flag_off, u64 tag) { __hip_atomic_store((gu64*)(ws + flag_off), tag, RLX_AGENT); }
DI void arrive_publish(unsigned cnt_lds, char* ws, unsigned flag_off, u64 tag, unsigned last = 7u) {
    if (lds_count(cnt_lds) == last) raise_flag(ws, flag_off, tag);
}
DI void lds_wait(unsigned cnt_lds, unsigned n) {
#pragma nounroll
    for (int it = 0; it < SPIN_LIMIT; ++it) {
        if (__builtin_amdgcn_readfirstlane(*(volatile LDSP(unsigned))cnt_lds) >= n) break;
        __builtin_amdgcn_s_sleep(1);
    }
    asm volatile("" ::: "memory");
}
DI unsigned oob_if(bool c) { return c ? 0x80000000u : 0u; }
DI f2v ln_stats(float s1, float s2) {
    const float mean = s1 * (1.f / 640.f), var = fmaxf(__builtin_fmaf(-mean, mean, s2 * (1.f / 640.f)), 0.f);
    const float rs = __builtin_amdgcn_rcpf(rbf(__builtin_amdgcn_sqrtf(rbf(rbf(var) + 1e-5f))));
    return f2v{rs, rbf(mean) * rs};
}
DI f2v cmul_a(f2v a, f2v b) { f2v t; asm("v_pk_mul_f32 %0, %1, %2 op_sel_hi:[0,1]" : "=v"(t) : "v"(a), "v"(b)); return t; }
DI f2v cmul_b(f2v a, f2v b, f2v t) {
    f2v o;
    asm("v_pk_fma_f32 %0, %1, %2, %3 op_sel:[1,1,0] op_sel_hi:[1,0,1] neg_lo:[0,1,0]" : "=v"(o) : "v"(a), "v"(b), "v"(t));
    return o;
}
DI f2v cmul(f2v a, f2v b) { return cmul_b(a, b, cmul_a(a, b)); }
DI f2v cexp_p(float are, float aim, float p) {
    const float m = __builtin_amdgcn_exp2f(are * p), an = __builtin_amdgcn_fractf(aim * p);
    return f2v{m * __builtin_amdgcn_cosf(an), m * __builtin_amdgcn_sinf(an)};
}

#define GP(T) __attribute__((address_space(1))) T*
typedef unsigned u8v __attribute__((ext_vector_type(8)));
DI u64 mk64(unsigned lo, unsigned hi) { return ((u64)hi << 32) | lo; }
__global__ __launch_bounds__(512, 2) void s4ffn_fused(const u16*, const u16*, const u16*, const u16*, const u16*, const u16*, const u16*, const u16*, const u16*,
                                                      const u16*, const u16*, const u16*, const u16*, u16*, char*) {
    const int tid = threadIdx.x, lane = tid & 63, w = __builtin_amdgcn_readfirstlane(tid >> 6);
    const int xcd = blockIdx.x & 7, r = ((blockIdx.x >> 3) + 28) & 31, bid = r * 8 + xcd;
    const bool is_gemm = r < 28;
    STAMP(0);
    if (w < CODE_CHUNKS && ((r + w) & 3) == 0) touch_code(w, lane, 163840 - 256);
    u64 a_x, a_prm;
    u8v a_m;
    u4v a_t;
    asm volatile("s_load_dwordx2 %0, %4, 0x0\n\ts_load_dwordx2 %1, %4, %5 offset:0x8\n\ts_load_dwordx8 %2, %4, 0x48\n\ts_load_dwordx4 %3, %4, 0x68\n\ts_waitcnt lgkmcnt(0)"
                 : "=&s"(a_x), "=&s"(a_prm), "=&s"(a_m), "=&s"(a_t) : "s"(__builtin_amdgcn_kernarg_segment_ptr()), "s"(8 * w));
    const GP(u16) x = (const GP(u16))a_x;
    const char* prm = (const char*)a_prm;
    const GP(u16) w1 = (const GP(u16))mk64(a_m[0], a_m[1]);
    const GP(u16) b1 = (const GP(u16))mk64(a_m[2], a_m[3]);
    const GP(u16) w2 = (const GP(u16))mk64(a_m[4], a_m[5]);
    const GP(u16) b2 = (const GP(u16))mk64(a_m[6], a_m[7]);
    GP(u16) out = (GP(u16))mk64(a_t[0], a_t[1]);
    char* ws = (char*)mk64(a_t[2], a_t[3]);
    u64 tag;
    const auto wsr = __builtin_amdgcn_make_buffer_rsrc((void*)ws, 0, WS_BYTES, 0x00020000);
    const int nch = bid < 192 ? 3 : 2, ch0 = 3 * bid - max(bid - 192, 0);
    const int g = lane >> 4, l16 = lane & 15;
    const int rb2 = xcd * 4 + (r & 3), cb2 = r >> 2;
    const bool has7 = w < 6;
    u4v bw[35];
    int rot2 = 7 * cb2 + 14 * (r & 3) + w;
    rot2 = rot2 >= 54 ? rot2 - 54 : rot2;
    auto sidx = [&](int i) { const int s = rot2 + 8 * i; return s >= 54 ? s - 54 : s; };
    auto loadb = [&](int n) {
        bw[n] = __builtin_amdgcn_raw_buffer_load_b128(wsr, lane * 16 | oob_if(n >= 30 && !has7), WS_W2T + ((cb2 * 54 + sidx(n / 5)) * 5 + n % 5) * 1024, 16);
    };

    auto stage_x = [&]() {
        const u4v rx = mkrsrc(x, 2048 * 1280);
        const int n1 = 64 + (lane & 15);
        dma16(rx, (lane / 10) * 1280 + (lane % 10) * 16, (rb2 * 64 + 8 * w) * 1280 + cb2 * 160, L_XS + w * 1280);
        dma16_lanes16(rx, (n1 / 10) * 1280 + (n1 % 10) * 16, (rb2 * 64 + 8 * w) * 1280 + cb2 * 160, L_XS + w * 1280 + 1024);
        if (w == 0) dma16_lanes16(mkrsrc(b2, 1280), min(lane, 9) * 16, cb2 * 160, L_XS + 10240);
    };
    if (__builtin_expect(is_gemm, 1)) {
        if (w < 6) dma16g(prm + (ch0 + min(g, nch - 1)) * 256 + l16 * 16, L_PRM + w * 1024);
        else dma16g_lanes3(lane == 2 ? (const char*)ws + WS_E : prm + min(((ch0 * 2) & ~15) + lane * 16, 1264), L_SCR + (w - 6) * 64);
        SB();
        const int gi = r & 3, gj = r >> 2, rb = (xcd >> 1) * 4 + gi, cb = (xcd & 1) * 7 + gj;
        const int fw = w & 3, tw = w >> 2;
        const u4v rx = mkrsrc(x, 2048 * 1280), rw = mkrsrc(w1, 640 * 3456);
        unsigned vx[2];
#pragma unroll
        for (int u = 0; u < 2; ++u) vx[u] = (rb * 128 + 16 * w + 8 * u + (lane >> 3)) * 1280 + (((lane & 7) ^ (4 * u + g)) << 4);
        const int wc = l16 ^ (2 * g) ^ ((w & 1) << 3);
        unsigned vw = (unsigned)((8 * w + g) * 3456 + cb * 256 + wc * 16) | (cb == 13 ? (unsigned)(wc & 8) << 28 : 0u);
        int rot = gj + 3 * gi + xcd;
        rot = rot >= 20 ? rot - 20 : rot >= 10 ? rot - 10 : rot;
        auto stage1w = [&](int t, int slot, int p, int dw) {
            int kt = t + rot; kt = kt >= 10 ? kt - 10 : kt;
            const unsigned lb = slot * SLOT + (w + dw) * 2048 + (p >> 1) * 1024;
            if (p & 1) dma16(rw, vw + dw * 8 * 3456, kt * (64 * 3456) + (p >> 1) * 4 * 3456, lb + 16384);
            else dma16(rx, vx[p >> 1] + dw * 16 * 1280, kt * 128, lb);
        };
        auto stage1 = [&](int t, int slot, int p) { stage1w(t, slot, p, 0); };
        unsigned ix[12];
        {
            const int l0 = 8 * (tid & 255) - 3;
            const float lf = (float)l0;
            const unsigned kb = L_TT + tw * TT_CH, kc = kb - 2u * (0x4300 - 128);
#pragma unroll
            for (int i = 0; i < 6; ++i) {
                const unsigned c2 = pk2(lf + (float)(2 * i), lf + (float)(2 * i + 1));
                asm("v_mad_u32_u16 %0, %1, 2, %2" : "=v"(ix[2 * i]) : "v"(c2), "s"(kc));
                asm("v_mad_u32_u16 %0, %1, 2, %2 op_sel:[1,0,0,0]" : "=v"(ix[2 * i + 1]) : "v"(c2), "s"(kc));
            }
            if (fw == 0) {
#pragma unroll
                for (int i = 0; i < 11; ++i) {
                    const int l = l0 + i;
                    const unsigned lo = kb + 2u * min((unsigned)l, 641u), m = (unsigned)((l - 128) >> 31);
                    ix[i] = (lo & m) | (ix[i] & ~m);
                }
            }
#pragma unroll
            for (int i = 0; i < 11; ++i) asm volatile("" : "+v"(ix[i]));
        }
        const int col = 16 * (w & 1) + l16;
        unsigned ta = L_TBL + (w >> 1) * TBL_CH + l16 * TBL_ROW + g * 16, te = ta + (21 + 16 * (w & 1)) * TBL_ROW;
        unsigned ta0, ta1, tb0, tb1, st1;
        {
            const unsigned tt = L_TT + (w >> 1) * TT_CH;
            ta0 = tt + ((col << (3 - max(g - 1, 0))) + ((0x30200100u >> (8 * g)) & 0xffu) * (g == 1 ? 4u : 8u)) * 2;
            ta1 = ta0 + (g < 3 ? 4 : 128);
            tb0 = tt + (g == 0 ? 512 + col : 644) * 2; st1 = g == 0 ? 64 : 0;
            tb1 = g == 1 && col < 2 ? tt + (640 + col) * 2 : tb0;
        }
        asm volatile("" : "+v"(ta), "+v"(te), "+v"(ta0), "+v"(ta1), "+v"(tb0), "+v"(tb1), "+v"(st1), "+v"(vx[0]), "+v"(vx[1]), "+v"(vw));
        if (w == 0) ldsw<unsigned>(L_CNT + (lane & 7) * 4, 0u);
        STAMP(10);
        WAIT_BAR(0);
        STAMP(11);
        const unsigned so = (ch0 * 2) & 15;
        { const u2v tv = ldsr<u2v>(L_SCR + 32); tag = mk64(__builtin_amdgcn_readfirstlane(tv[0]), __builtin_amdgcn_readfirstlane(tv[1])) + 1ull; }
        const unsigned dsk01 = __builtin_amdgcn_readfirstlane(ldsr<u16>(L_SCR + 64 + so) | ((unsigned)ldsr<u16>(L_SCR + 64 + so + 2) << 16));
        const unsigned dsk2 = __builtin_amdgcn_readfirstlane(ldsr<u16>(L_SCR + 64 + so + 4));
        {
            const int hc = w < 6 ? w : w - 2, c = hc >> 1, mode = (hc & 1) * 64 + lane;
            const float ldt = bf2f(ldsr<u16>(L_SCR + so + 2 * c));
            const unsigned pb = L_PRM + c * 256 + mode * 2;
            const float pa = bf2f(ldsr<u16>(pb)), pi = bf2f(ldsr<u16>(pb + 1024));
            const f2v Bc = {bf2f(ldsr<u16>(pb + 2048)), bf2f(ldsr<u16>(pb + 3072))}, Cc = {bf2f(ldsr<u16>(pb + 4096)), bf2f(ldsr<u16>(pb + 5120))};
            auto pc = [&](int i) { if (w >= 4) { stage1w(i >> 2, i >> 2, i & 3, 0); stage1w(i >> 2, i >> 2, i & 3, -4); } };
            if (c >= nch) {
#pragma unroll
                for (int i = 0; i < 8; ++i) pc(i);
            }
            if (c < nch) {
                const float dt = rbf(__builtin_amdgcn_exp2f(ldt * 1.4426950408889634f));
                const float are = dt * pa * 1.4426950408889634f, aim = dt * pi * 0.15915494309189535f;
                const unsigned tb = L_TBL + c * TBL_CH + mode * 4;
                if (w < 6) {
                    pc(0);
                    const f2v z8 = cexp_p(are, aim, 8.f), z16 = cexp_p(are, aim, 16.f), z64 = cexp_p(are, aim, 64.f);
                    pc(1);
                    f2v e[8];
                    e[0] = f2v{1.f, 0.f}; e[1] = z8; e[2] = z16; e[3] = cmul(z16, z8); e[4] = cmul(z16, z16);
                    e[5] = cmul(e[3], z16); e[6] = cmul(e[4], z16); e[7] = cmul(e[4], e[3]);
#pragma unroll
                    for (int i = 0; i < 4; ++i) {
                        f2v t[8];
                        if (i < 3) pc(2 + 2 * i);
#pragma unroll
                        for (int j = 0; j < 8; ++j) {
                            if (i < 3) t[j] = cmul_a(e[j], z64);
                            stw(tb, pk2(e[j][0], e[j][1]), (21 + 8 * i + j) * TBL_ROW);
                        }
                        if (i < 3) {
                            pc(3 + 2 * i);
#pragma unroll
                            for (int j = 0; j < 8; ++j) e[j] = cmul_b(e[j], z64, t[j]);
                        }
                    }
                }
                if (w < 4 || w >= 6) {
                    pc(0);
                    const f2v z1 = cexp_p(are, aim, 1.f), z2 = cexp_p(are, aim, 2.f), z4 = cexp_p(are, aim, 4.f);
                    pc(1);
                    const f2v z256 = cexp_p(are, aim, 256.f), z1024 = cexp_p(are, aim, 1024.f);
                    pc(2);
                    const f2v W = cmul(Cc, Bc * dt);
                    auto putx = [&](int row, f2v v) { stw(tb, pk2(v[0], -v[1]), row * TBL_ROW); };
                    const f2v x1 = cmul(W, z1), x2 = cmul(W, z2), x8 = cmul(W, z256), x16 = cmul(W, z1024);
                    pc(3);
                    const f2v x3 = cmul(x1, z2), x9 = cmul(x8, z2), x12 = cmul(x8, z256), x17 = cmul(x16, z256), x20 = cmul(x16, z1024);
                    const f2v x14 = cmul(x12, z256), x18 = cmul(x17, z256);
                    pc(4);
                    putx(0, W); putx(1, x1); putx(2, x2); putx(3, x3); putx(8, x8); putx(9, x9); putx(12, x12); putx(14, x14);
                    pc(5);
                    putx(16, x16); putx(17, x17); putx(18, x18); putx(20, x20);
                    pc(6);
                    putx(4, cmul(W, z4)); putx(5, cmul(x1, z4)); putx(6, cmul(x2, z4)); putx(7, cmul(x3, z4));
                    pc(7);
                    putx(10, cmul(x8, z4)); putx(11, cmul(x9, z4)); putx(13, cmul(x12, z4)); putx(15, cmul(x14, z4)); putx(19, cmul(x18, z256));
                }
            }
        }
        u4v w2a, w2b;
        {
            const auto rw2 = __builtin_amdgcn_make_buffer_rsrc((void*)(u64)w2, 0, 1728 * 1280, 0x00020000);
            const unsigned so2 = ((bid >> 2) * 32 + ((bid >> 1) & 1) * 8 + (w >> 2) * 16 + (w & 3) * 2) * 1280 + (bid & 1) * 640;
            w2a = __builtin_amdgcn_raw_buffer_load_b128(rw2, lane * 16 + (lane >= 40 ? 640 : 0), so2, 0);
            w2b = __builtin_amdgcn_raw_buffer_load_b128(rw2, (1280 + 384 + lane * 16) | oob_if(lane >= 16), so2, 0);
        }
        STAMP(13);
        BAR_L();
        if (w < 2 * nch) {
            u4v fa[8], fb[8], fe[8];
#pragma unroll
            for (int s = 0; s < 8; ++s) { fe[s] = ldsr<u4v>(te + s * 64); fa[s] = ldsr<u4v>(ta + s * 64); fb[s] = ldsr<u4v>(ta + 16 * TBL_ROW + s * 64); }
            SB();
            f4v k0 = {}, k1 = {};
#pragma unroll
            for (int s = 0; s < 8; ++s) {
                k0 = __builtin_amdgcn_mfma_f32_16x16x32_bf16(asbf(fa[s]), asbf(fe[s]), k0, 0, 0, 0);
                k1 = __builtin_amdgcn_mfma_f32_16x16x32_bf16(asbf(fb[s]), asbf(fe[s]), k1, 0, 0, 0);
            }
            ldsw<unsigned>(ta0, pk2(k0[0], k0[1]));
            ldsw<unsigned>(ta1, pk2(k0[2], k0[3]));
            const unsigned q01 = pk2(k1[0], k1[1]), q23 = pk2(k1[2], k1[3]);
            ldsw<u16>(tb1, g == 1 && col == 1 ? (u16)0 : (u16)q01);
            ldsw<u16>(tb0 + st1, (u16)(q01 >> 16));
            ldsw<u16>(tb0 + 2 * st1, (u16)q23);
            ldsw<u16>(tb0 + 3 * st1, (u16)(q23 >> 16));
        }
        BAR_L();
        STAMP(14);
        {
            const bool two = w < 4 && nch == 3;
            const int t = tid & 255;
#pragma unroll
            for (int k = 0; k < 2; ++k) {
                if (k == 0 || two) {
                    unsigned pe[6], po[5];
#pragma unroll
                    for (int i = 0; i < 5; ++i) pe[i] = __builtin_bit_cast(unsigned, h2v{ldsr<u16>(ix[2 * i] + k * 2 * TT_CH), ldsr<u16>(ix[2 * i + 1] + k * 2 * TT_CH)});
                    pe[5] = ldsr<u16>(ix[10] + k * 2 * TT_CH);
#pragma unroll
                    for (int i = 0; i < 5; ++i) po[i] = __builtin_amdgcn_alignbit(pe[i + 1], pe[i], 16);
                    const unsigned cbase = L_KC + (tw + 2 * k) * KC_CH, d = cbase + 32 + t * 16;
                    ldsw<u4v>(d, u4v{po[1], po[2], po[3], po[4]});
                    ldsw<u4v>(d + KC_CP, u4v{pe[1], pe[2], pe[3], pe[4]});
                    ldsw<u4v>(d + 2 * KC_CP, u4v{po[0], po[1], po[2], po[3]});
                    ldsw<u4v>(d + 3 * KC_CP, u4v{pe[0], pe[1], pe[2], pe[3]});
                }
            }
            if (w == 7) ldsw<u4v>(L_KC + min(lane >> 1, 11) * KC_CP + (lane & 1) * 16, u4v{0u, 0u, 0u, 0u});
        }
        ldsw<u4v>(L_W2U + w * 1280 + lane * 16, w2a);
        ldsw<u4v>(lane < 16 ? L_W2U + w * 1280 + 1024 + lane * 16 : L_CNT + 48, w2b);
        stage1(2, 2, 0); stage1(2, 2, 1); stage1(2, 2, 2); stage1(2, 2, 3);
        STAMP(1);
        f4v acc1[2][4];
#pragma unroll
        for (int i = 0; i < 8; ++i) acc1[i >> 2][i & 3] = f4v{};
        unsigned bx[2], aw[2];
        {
            const int q4 = l16 >> 2, p4 = lane & 3;
#pragma unroll
            for (int i = 0; i < 2; ++i) {
                bx[i] = (64 * tw + l16) * 128 + (((4 * i + g) ^ (l16 >> 1)) << 4);
                aw[i] = 16384 + (8 * g + q4) * 256 + (((2 * fw + i) ^ q4 ^ ((g & 1) << 2)) << 5) + p4 * 8;
            }
        }
        bf8v fa[2][2][2], fb[2][2][4];
        auto rd = [&](int buf, int i) {
            const int ks = i >> 1, h = i & 1;
            fa[buf][ks][h] = cat(trr(aw[h] + ks * 8192), trr(aw[h] + ks * 8192 + 1024));
            fb[buf][ks][2 * h] = asbf(ldsr<u4v>(bx[ks] + h * 4096));
            fb[buf][ks][2 * h + 1] = asbf(ldsr<u4v>(bx[ks] + h * 4096 + 2048));
        };
        unsigned sb = 0;
        int kt = rot + 2; kt = kt >= 10 ? kt - 10 : kt;
        auto tile = [&](int cur, bool more, bool stage, auto&& side) {
            const unsigned d = sb == 2 * SLOT ? (unsigned)(-2 * (int)SLOT) : SLOT, lb = sb + w * 2048;
            kt = kt == 9 ? 0 : kt + 1;
            sb += d; aw[0] += d; aw[1] += d; bx[0] += d; bx[1] += d;
#pragma unroll
            for (int i = 0; i < 4; ++i) {
                SB();
                if (more) rd(cur ^ 1, i);
                SB();
#pragma unroll
                for (int j = 0; j < 4; ++j) acc1[i & 1][j] = __builtin_amdgcn_mfma_f32_16x16x32_bf16(fa[cur][i >> 1][i & 1], fb[cur][i >> 1][j], acc1[i & 1][j], 0, 0, 0);
                SB();
                if (stage) {
                    if (i & 1) dma16(rw, vw, kt * (64 * 3456) + (i >> 1) * 4 * 3456, lb + (i >> 1) * 1024 + 16384);
                    else dma16(rx, vx[i >> 1], kt * 128, lb + (i >> 1) * 1024);
                }
                side(i);
            }
            SB();
        };
        auto none = [&](int) {};
        BAR_L();
        STAMP(15);
#pragma unroll
        for (int i = 0; i < 4; ++i) rd(0, i);
        u4v uld[2], cbv;
        f2v sv[4];
        u64 fpoll = 0;
        const unsigned f1a = WS_FS + 2 * rb * 128, f1b = WS_FC + 2 * cb * 128;
        const unsigned f1_off = min(((lane & 2) ? f1b : f1a) + (lane & 1) * 128, WS_FC + 26 * 128);
        const unsigned f2_off = w == 2 ? WS_FA + (lane & 31) * 128 : WS_FW + (2 * min(54 * w + lane, 107) + (cb2 >> 2)) * 128;
        {
            const unsigned tl = L_W2U + (4 * (g & 1) + (l16 >> 2)) * 640 + (lane >> 5) * 32 + (lane & 3) * 8;
#pragma unroll
            for (int k = 0; k < 2; ++k) {
                const int i = min(w + 8 * k, 9), P = 20 * (bid & 1) + 2 * i + (lane >> 5), cbb = (P * 52) >> 8;
                const bf8v f = cat(trr(tl + i * 64), trr(tl + i * 64 + 8 * 640));
                __builtin_amdgcn_raw_buffer_store_b128(__builtin_bit_cast(u4v, f), wsr,
                    (unsigned)(WS_W2T + (bid >> 2) * 5120 + ((bid >> 1) & 1) * 512 + (cbb * 265 + P) * 1024 + (lane & 31) * 16) | oob_if(bid >= 216 || w + 8 * k > 9), 0, 16);
            }
        }
        BAR_L(); tile(0, true, true, none);
        WAIT_BAR(6); tile(1, true, true, none);
        WAIT_BAR(4);
        arrive_publish(L_CNT + 8, ws, WS_FW + min(bid, 216) * 128, tag);
        tile(0, true, true, none);
        STAMP(12);
        auto ride = [&](int t, int i) { if (t >= PT0 && (i == 0 || (i == 2 && PACE > 1)) && PACE > 0) loadb((t - PT0) * PACE + (i >> 1)); };
        auto check_w = [&](int t) { if (t == PT0 && w < 2 && !__all(fpoll == tag)) wait_flags(ws, f2_off, tag); };
        auto poll_w = [&](int t, int i) { if (t + 1 == PT0 && i == 0 && w < 2) fpoll = ld_flag(ws, f2_off); };
        constexpr int NP = (7 - PT0) * PACE;
        WAIT_BAR(4); tile(1, true, true, [&](int i) { poll_w(3, i); });
        check_w(4); WAIT_BAR(4); tile(0, true, true, [&](int i) { poll_w(4, i); ride(4, i); });
        check_w(5); if (PT0 <= 4) WAIT_BARN(4 + PACE); else WAIT_BAR(4);
        tile(1, true, true, [&](int i) { poll_w(5, i); ride(5, i); });
        check_w(6); if (PT0 <= 5) WAIT_BARN(4 + PACE); else WAIT_BAR(4);
        tile(0, true, true, [&](int i) { poll_w(6, i); ride(6, i); });
        check_w(7); if (PT0 <= 6) WAIT_BARN(4 + PACE); else WAIT_BAR(4);
        tile(1, true, false, [&](int i) { if (i == 0 && w == 0) fpoll = ld_flag(ws, f1_off); loadb(NP + i); });
        if (w == 0 && !__all(fpoll == tag)) wait_flags(ws, f1_off, tag);
        WAIT_BAR(4);
        STAMP(3);
        tile(0, true, false, [&](int i) {
            sv[i] = __builtin_bit_cast(f2v, __builtin_amdgcn_raw_buffer_load_b64(wsr, l16 * 8, WS_STATS + (rb * 128 + 64 * tw + 16 * i) * 8, 16));
            if (i == 0 && w == 1) cbv = __builtin_amdgcn_raw_buffer_load_b128(wsr, (lane >> 5) * (WS_B1 - WS_CS) + (lane & 31) * 16, WS_CS + cb * 512, 16);
            if (i == 0 && w == 2) fpoll = ld_flag(ws, f2_off);
            loadb(NP + 4 + i);
        });
        tile(1, false, false, [&](int i) { loadb(NP + 8 + i); });
        if (w == 1) ldsw<u4v>(L_CB + lane * 16, cbv);
        if (w >= 2 && w < 5) ldsw<u4v>(L_UA + (w - 2) * UA_CH + min(lane, 29) * 16, u4v{0u, 0u, 0u, 0u});
        if (w == 2 && !__all(fpoll == tag)) wait_flags(ws, f2_off, tag);
        BAR_L();
        STAMP(2);
        stage_x();
        {
            f4v cs[2], bb[2];
#pragma unroll
            for (int u = 0; u < 2; ++u) { cs[u] = ldsr<f4v>(L_CB + (32 * fw + 16 * u + 4 * g) * 4); bb[u] = ldsr<f4v>(L_CB + 512 + (32 * fw + 16 * u + 4 * g) * 4); }
            const int s = 4 * cb + fw;
            const unsigned ho = (unsigned)(WS_HA + ((2 * rb + tw) * 54 + s) * 4096 + lane * 16) | oob_if(s >= 54);
            u4v hp[4];
#pragma unroll
            for (int ti = 0; ti < 4; ++ti) {
                const f2v rs2 = {sv[ti][0], sv[ti][0]}, nm2 = {-sv[ti][1], -sv[ti][1]};
#pragma unroll
                for (int i = 0; i < 4; ++i) {
                    const int slot = 4 * ti + i, u = i >> 1, e = 2 * (i & 1);
                    if (slot < 2) {
                        const int q = w + 8 * slot, c = q >> 2;
                        uld[slot] = __builtin_amdgcn_raw_buffer_load_b128(wsr, lane * 16 | oob_if(c >= nch), WS_XT + (ch0 + c) * 4096 + (q & 3) * 1024, 16);
                    }
                    SB();
                    const f2v v = __builtin_elementwise_fma(rs2, f2v{acc1[u][ti][e], acc1[u][ti][e + 1]}, __builtin_elementwise_fma(nm2, f2v{cs[u][e], cs[u][e + 1]}, f2v{bb[u][e], bb[u][e + 1]}));
                    const f2v z = v * __builtin_elementwise_fma(v * v, f2v{-0.10294324f, -0.10294324f}, f2v{-2.30220820f, -2.30220820f});
                    const f2v d = f2v{__builtin_amdgcn_exp2f(z[0]), __builtin_amdgcn_exp2f(z[1])} + f2v{1.f, 1.f};
                    const f2v gl = v * f2v{__builtin_amdgcn_rcpf(d[0]), __builtin_amdgcn_rcpf(d[1])};
                    hp[ti][i] = pk2(gl[0], gl[1]);
                }
                __builtin_amdgcn_raw_buffer_store_b128(hp[ti], wsr, ho + 1024 * ti, 0, 16);
                SB();
            }
        }
#pragma unroll
        for (int i = 0; i < 2; ++i) {
            const int q = w + 8 * i, c = q >> 2;
            if (q < 12) ldsw<u4v>(L_UA + c * UA_CH + UA_PAD + (q & 3) * 1024 + lane * 16, uld[i]);
        }
        STAMP(4);
        BAR_L();
        {
            const int typ = w < 3 ? 0 : w < 6 ? 1 : 2;
            const int c1 = w < 3 ? w : w < 6 ? w - 3 : 2 * (w - 6), c2 = w == 6 ? 1 : c1;
            const unsigned kl = L_KC + (l16 >> 2) * KC_CP + 56 - 16 * g + 8 * (lane & 3);
            const unsigned ul = L_UA + UA_PAD + 32 * l16 - 32 + 16 * g;
            unsigned ka[3], ua[3];
            ka[0] = kl + c1 * KC_CH;
            ka[1] = typ == 2 ? kl + c2 * KC_CH - 4 * 512 : ka[0];
            ka[2] = typ == 1 ? ka[0] - 6 * 512 : ka[1];
            ua[0] = ul + c1 * UA_CH - typ * 1024;
            ua[2] = ul + c2 * UA_CH;
            ua[1] = typ == 2 ? ua[2] : ua[0];
            bf8v uf[8], kf[8];
            auto rdk = [&](int sl) { const unsigned ad = ka[sl < 4 ? 0 : sl < 6 ? 1 : 2] + 512u * sl; kf[sl] = cat(trr(ad + 8u), trr(ad)); };
            auto rdu = [&](int j) { uf[j] = asbf(ldsr<u4v>(ua[j >= 4 ? 0 : j >= 2 ? 1 : 2] + 512u * j)); };
#pragma unroll
            for (int sl = 0; sl < 8; ++sl) { rdk(sl); rdu(7 - sl); }
            f4v A[3] = {}, B[5] = {};
            const unsigned fo = WS_FH + (rb * 14 + cb) * 128;
#pragma nounroll
            for (int rho = 0; rho < 7; ++rho) {
#pragma unroll
                for (int i = 0; i < 3; ++i) { ka[i] += 64u; ua[i] -= 64u; asm volatile("" : "+v"(ka[i]), "+v"(ua[i])); }
#pragma unroll
                for (int sl = 0; sl < 8; ++sl) {
                    SB();
                    const int ia = sl < 4 ? 0 : sl < 6 ? 1 : 2, ib = sl < 3 ? 0 : sl - 2;
                    A[ia] = __builtin_amdgcn_mfma_f32_16x16x32_bf16(kf[sl], uf[7 - sl], A[ia], 0, 0, 0);
                    if (sl < 7) B[ib] = __builtin_amdgcn_mfma_f32_16x16x32_bf16(kf[sl], uf[6 - sl], B[ib], 0, 0, 0);
                    SB();
                    rdk(sl); rdu(7 - sl);
                }
                SB();
                if (rho == PUB_RHO) {
                    WAIT_V(0);
                    arrive_publish(L_CNT, ws, fo, tag);
                }
#pragma unroll
                for (int k = 0; k < 7 - PUB_RHO; ++k) {
                    constexpr int NL = (30 - NP - 12 + 6 - PUB_RHO) / (7 - PUB_RHO);
                    if (rho == PUB_RHO + k) {
#pragma unroll
                        for (int n = NP + 12 + NL * k; n < NP + 12 + NL * (k + 1) && n < 30; ++n) loadb(n);
                    }
                }
            }
#pragma unroll
            for (int sl = 0; sl < 8; ++sl) {
                const int ia = sl < 4 ? 0 : sl < 6 ? 1 : 2, ib = sl < 3 ? 0 : sl - 2;
                A[ia] = __builtin_amdgcn_mfma_f32_16x16x32_bf16(kf[sl], uf[7 - sl], A[ia], 0, 0, 0);
                if (sl < 7) B[ib] = __builtin_amdgcn_mfma_f32_16x16x32_bf16(kf[sl], uf[6 - sl], B[ib], 0, 0, 0);
            }
            SB();
            f4v y[4];
            {
                const f4v z = {};
                const f4v a1 = typ < 2 ? A[1] : z, a1b = typ < 2 ? z : A[1], a2 = typ == 0 ? A[2] : z;
                const f4v b12 = B[1] + B[2], b34 = B[3] + B[4];
                y[0] = A[0] + a1 + a2;
                y[2] = A[2] + a1b;
                y[1] = B[0] + (typ < 2 ? b12 : z) + (typ == 0 ? b34 : z);
                y[3] = typ == 1 ? B[4] : B[2] + b34;
            }
            const unsigned yl = 32 * l16 + 8 * g;
#pragma unroll
            for (int i = 0; i < 4; ++i) {
                const int c = i < 2 ? c1 : c2, tau = (i < 2 ? 7 - 2 * typ : max(2 * typ - 1, 1)) - (i & 1);
                const unsigned off = oob_if(c >= nch || (i >= 2 && (typ == 0 || w == 7)));
                const u2v uv = ldsr<u2v>(yl + (L_UA + UA_PAD + c * UA_CH + 512 * tau));
                const float dk = __uint_as_float(((c == 2 ? dsk2 : dsk01) >> (c == 1 ? 16 : 0)) << 16);
                const u2v o = {pk2(y[i][0] + dk * bflo(uv[0]), y[i][1] + dk * bfhi(uv[0])), pk2(y[i][2] + dk * bflo(uv[1]), y[i][3] + dk * bfhi(uv[1]))};
                __builtin_amdgcn_raw_buffer_store_b64(o, wsr, yl, (WS_YT + (ch0 + c) * 4096 + 512 * tau) | off, 16);
            }
        }
        STAMP(5);
    } else {
        const int a = (r - 28) * 8 + xcd;
        const u4v rx = mkrsrc(x, 2048 * 1280);
        {
            unsigned vo[5];
#pragma unroll
            for (int i = 0; i < 5; ++i) {
                const int n = i * 64 + lane, rl = n / 80, cp = n - rl * 80;
                vo[i] = (a * 64 + 8 * w + rl) * 1280 + ((cp ^ (2 * w) ^ ((rl >> 1) << 3)) << 4);
                dma16(rx, vo[i], 0, L_AX + (w * 10 + i) * 1024);
            }
#pragma unroll
            for (int i = 5; i < 10; ++i) dma16(rx, vo[i - 5], 4 * 1280, L_AX + (w * 10 + i) * 1024);
        }
        const u64 e0 = ld_flag(ws, WS_E);
        u4v cv[10];
        {
            const auto rw1 = __builtin_amdgcn_make_buffer_rsrc((void*)(u64)w1, 0, 640 * 3456, 0x00020000);
#pragma unroll
            for (int i = 0; i < 10; ++i) cv[i] = __builtin_amdgcn_raw_buffer_load_b128(rw1, ((lane >> 3) * 3456 + (lane & 7) * 16) | oob_if(a >= 27), (80 * w + 8 * i) * 3456 + a * 128, 0);
        }
        if (w == 0) ldsw<unsigned>(L_ACNT + (lane & 7) * 4, 0u);
        STAMP(10);
        WAIT_V(11);
        STAMP(11);
        {
            const int row = 8 * w + (lane >> 3), sw = (2 * w) ^ (((row >> 1) & 1) << 3);
            u4v v[10];
#pragma unroll
            for (int i = 0; i < 10; ++i) v[i] = ldsr<u4v>(L_AX + row * 1280 + (((lane & 7) + 8 * i) ^ sw) * 16);
            SB();
            float s1[2] = {0.f, 0.f}, s2[2] = {0.f, 0.f};
#pragma unroll
            for (int i = 0; i < 10; ++i)
#pragma unroll
                for (int j = 0; j < 4; ++j) { s1[j & 1] = dot2(v[i][j], 0x3f803f80u, s1[j & 1]); s2[j & 1] = dot2(v[i][j], v[i][j], s2[j & 1]); }
            const f2v sv = ln_stats(sum8(s1[0] + s1[1]), sum8(s2[0] + s2[1]));
            ldsw<f2v>(L_AST + row * 8, sv);
            __builtin_amdgcn_raw_buffer_store_b64(__builtin_bit_cast(u2v, sv), wsr, (WS_STATS + (a * 64 + row) * 8) | oob_if(lane & 7), 0, 16);
        }
        BAR_L();
        STAMP(14);
        {
            const int o = lane & 7, cq = lane >> 3;
            f2v rs2[4], nm2[4];
#pragma unroll
            for (int i = 0; i < 4; ++i) { const f4v q = ldsr<f4v>(L_AST + o * 64 + i * 16); rs2[i] = f2v{q[0], q[2]}; nm2[i] = f2v{-q[1], -q[3]}; }
            u2v v[3][8];
#pragma unroll
            for (int k = 0; k < 3; ++k)
#pragma unroll
                for (int t = 0; t < 8; ++t) v[k][t] = ldsr<u2v>(L_AX + (8 * o + t) * 1280 + ((4 * min(w + 8 * k, 19) + (cq >> 1)) ^ ((2 * o) ^ (((t >> 1) & 1) << 3))) * 16 + (cq & 1) * 8);
            SB();
#pragma unroll
            for (int k = 0; k < 3; ++k) {
                if (k < 2 || w < 4) {
#pragma unroll
                    for (int e = 0; e < 4; ++e) {
                        u4v ov;
#pragma unroll
                        for (int p = 0; p < 4; ++p) {
                            const unsigned va = v[k][2 * p][e >> 1], vb = v[k][2 * p + 1][e >> 1];
                            const f2v n2 = __builtin_elementwise_fma((e & 1) ? f2v{bfhi(va), bfhi(vb)} : f2v{bflo(va), bflo(vb)}, rs2[p], nm2[p]);
                            ov[p] = pk2(n2[0], n2[1]);
                        }
                        __builtin_amdgcn_raw_buffer_store_b128(ov, wsr, WS_XT + (32 * (w + 8 * k) + 4 * cq + e) * 4096 + a * 128 + o * 16, 0, 16);
                        SB();
                    }
                }
                if (k == 0) {
                    WAIT_V(4);
                    tag = mk64(__builtin_amdgcn_readfirstlane((unsigned)e0), __builtin_amdgcn_readfirstlane((unsigned)(e0 >> 32))) + 1ull;
                    arrive_publish(L_ACNT + 8, ws, WS_FS + a * 128, tag);
                }
            }
        }
        STAMP(15);
        {
            unsigned selo = 0x00003f80u, sehi = 0x3f800000u;
            asm volatile("" : "+v"(selo), "+v"(sehi));
            float sm[8] = {0.f, 0.f, 0.f, 0.f, 0.f, 0.f, 0.f, 0.f};
#pragma unroll
            for (int i = 0; i < 10; ++i)
#pragma unroll
                for (int j = 0; j < 4; ++j) { sm[2 * j] = dot2(cv[i][j], selo, sm[2 * j]); sm[2 * j + 1] = dot2(cv[i][j], sehi, sm[2 * j + 1]); }
#pragma unroll
            for (int j = 0; j < 8; ++j) sm[j] = dpp_add<0x128>(sm[j]);
            ldsw<f4v>(L_ACS + (4 * w + g) * 256 + (lane & 7) * 32 + (lane & 8) * 2, (lane & 8) ? f4v{sm[4], sm[5], sm[6], sm[7]} : f4v{sm[0], sm[1], sm[2], sm[3]});
        }
        BAR_L();
        if (w != 7 || a >= 27) {
            WAIT_V(0);
            arrive_publish(L_ACNT + 12, ws, WS_FA + a * 128, tag);
        } else {
            const float bf = bf2f(b1[64 * a + lane]);
            float p[32], sum = 0.f;
#pragma unroll
            for (int i = 0; i < 32; ++i) p[i] = ldsr<float>(L_ACS + i * 256 + lane * 4);
#pragma unroll
            for (int i = 0; i < 32; i += 4) sum += (p[i] + p[i + 1]) + (p[i + 2] + p[i + 3]);
            __builtin_amdgcn_raw_buffer_store_b32(__builtin_bit_cast(unsigned, sum), wsr, WS_CS + (64 * a + lane) * 4, 0, 16);
            __builtin_amdgcn_raw_buffer_store_b32(__builtin_bit_cast(unsigned, bf), wsr, WS_B1 + (64 * a + lane) * 4, 0, 16);
            WAIT_V(0);
            raise_flag(ws, WS_FC + a * 128, tag);
            arrive_publish(L_ACNT + 12, ws, WS_FA + a * 128, tag);
        }
        STAMP(2);
        if (w < 2) wait_flags(ws, WS_FW + (2 * min(54 * w + lane, 107) + (cb2 >> 2)) * 128, tag);
        BAR_L();
        STAMP(3);
        stage_x();
#pragma unroll
        for (int n = 0; n < 30; ++n) loadb(n);
        __builtin_amdgcn_s_waitcnt(0x0F70);
    }

    asm volatile("" : "+s"(rot2));
    if (w == 0) wait_flags(ws, WS_FH + ((rb2 >> 1) * 14 + min(lane, 13)) * 128, tag);
    BAR_L();
    STAMP(6);
    u4v ha[7][4];
    auto loada = [&](int i, int t) { ha[i][t] = __builtin_amdgcn_raw_buffer_load_b128(wsr, lane * 16, WS_HA + ((rb2 * 54 + sidx(i)) * 4 + t) * 1024, 16); };
#pragma unroll
    for (int i = 0; i < 8; ++i) loada(i >> 2, i & 3);
    if (is_gemm) {
        WAIT_V(8);
        arrive_publish(L_CNT + 4, ws, WS_FY + bid * 128, tag);
    }
    const int c0 = 80 * cb2;
    int lz = lane;
    asm volatile("" : "+v"(lz));
    const int cA = c0 + 10 * w, cB = cA + 9;
    const int own0 = max((cA * 0xaaab) >> 17, (cA - 192) >> 1), own1 = max((cB * 0xaaab) >> 17, (cB - 192) >> 1);
    u64 ypoll = tag;
    u4v y0, y1;
    f4v acc2[4][5];
#pragma unroll
    for (int i = 0; i < 20; ++i) acc2[i / 5][i % 5] = f4v{};
#pragma unroll
    for (int i = 0; i < 5; ++i) {
#pragma unroll
        for (int t = 0; t < 4; ++t) {
            SB();
#pragma unroll
            for (int c = 0; c < 5; ++c) acc2[t][c] = __builtin_amdgcn_mfma_f32_16x16x32_bf16(asbf(ha[i][t]), asbf(bw[5 * i + c]), acc2[t][c], 0, 0, 0);
            SB();
            if (i > 0) loada(i + 2, t);
            else if (t == 3) { loada(2, 0); loada(2, 1); loada(2, 2); loada(2, 3); }
            if (i == 1 || (i == 2 && t == 0)) loadb(30 + 4 * (i - 1) + t);
            if (i == 3 && t == 0) ypoll = ld_flag(ws, WS_FY + min(own0 + lz, own1) * 128);
            if (i == 4 && t == 3) {
                if (!__all(ypoll == tag)) wait_flags(ws, WS_FY + min(own0 + lz, own1) * 128, tag);
                y0 = __builtin_amdgcn_raw_buffer_load_b128(wsr, (lz >> 3) * 4096 + (lz & 7) * 16, WS_YT + cA * 4096 + rb2 * 128, 16);
                y1 = __builtin_amdgcn_raw_buffer_load_b128(wsr, (unsigned)((8 + (lz >> 3)) * 4096 + (lz & 7) * 16) | oob_if(lz >= 16), WS_YT + cA * 4096 + rb2 * 128, 16);
            }
        }
    }
    STAMP(7);
    const unsigned da = L_RED + (lz >> 4) * 1024 + (w >> 1) * 256 + (lz & 15) * 16 + (w & 1) * 8;
    auto dump = [&](int t) {
#pragma unroll
        for (int c = 0; c < 5; ++c) ldsw<u2v>(da + (5 * t + c) * 4096, u2v{pk2(acc2[t][c][0], acc2[t][c][1]), pk2(acc2[t][c][2], acc2[t][c][3])});
    };
#pragma unroll
    for (int t = 0; t < 4; ++t) {
        SB();
#pragma unroll
        for (int c = 0; c < 5; ++c) acc2[t][c] = __builtin_amdgcn_mfma_f32_16x16x32_bf16(asbf(ha[5][t]), asbf(bw[25 + c]), acc2[t][c], 0, 0, 0);
        SB();
        if (t > 0) dump(t - 1);
        SB();
#pragma unroll
        for (int c = 0; c < 5; ++c) acc2[t][c] = __builtin_amdgcn_mfma_f32_16x16x32_bf16(asbf(ha[6][t]), asbf(bw[30 + c]), acc2[t][c], 0, 0, 0);
    }
    SB();
    ldsw<u4v>(L_YS + (10 * w + (lz >> 3)) * YS_ROW + (lz & 7) * 16, y0);
    ldsw<u4v>(L_YS + min(10 * w + 8 + (lz >> 3) + (lz >= 16 ? 80 : 0), 80) * YS_ROW + (lz & 7) * 16, y1);
    u4v sel[4], one[2];
    {
        const int n = lz & 15, gz = lz >> 4;
        const unsigned lo = (n & 2) ? 0u : 0x3f80u << (16 * (n & 1)), hi = (n & 2) ? 0x3f80u << (16 * (n & 1)) : 0u;
#pragma unroll
        for (int q = 0; q < 4; ++q) { const unsigned m = (n >> 2) == q ? ~0u : 0u; sel[q] = u4v{lo & m, hi & m, lo & m, hi & m}; }
#pragma unroll
        for (int q = 0; q < 2; ++q) {
            const unsigned m0 = gz == 2 * q + (n >> 3) && !(n & 4) ? ~0u : 0u, m1 = gz == 2 * q + (n >> 3) && (n & 4) ? ~0u : 0u;
            one[q] = u4v{lo & m0, hi & m0, lo & m1, hi & m1};
        }
    }
    dump(3);
    BAR_L();
    STAMP(8);
    {
        const int n = lz & 15, gz = lz >> 4;
#pragma unroll
        for (int k = 0; k < 3; ++k) {
            if (k < 2 || w < 4) {
                const int tl = w + 8 * k, t = (tl * 52) >> 8, c = tl - 5 * t;
                u4v p[4];
#pragma unroll
                for (int q = 0; q < 4; ++q) p[q] = ldsr<u4v>(L_RED + lz * 16 + (tl * 4096 + q * 1024));
                const u4v yf = ldsr<u4v>(L_YS + n * YS_ROW + gz * 16 + (16 * c * YS_ROW + (t >> 1) * 64));
                const u4v xf = ldsr<u4v>(L_XS + n * 160 + gz * 16 + (16 * t * 160 + (c >> 1) * 64));
                const u2v bv = ldsr<u2v>(L_XS + 10240 + gz * 8 + c * 32);
                f4v d = {bflo(bv[0]), bfhi(bv[0]), bflo(bv[1]), bfhi(bv[1])};
#pragma unroll
                for (int q = 0; q < 4; ++q) d = __builtin_amdgcn_mfma_f32_16x16x32_bf16(asbf(p[q]), asbf(sel[q]), d, 0, 0, 0);
                d = __builtin_amdgcn_mfma_f32_16x16x32_bf16(asbf(yf), asbf((t & 1) ? one[1] : one[0]), d, 0, 0, 0);
                d = __builtin_amdgcn_mfma_f32_16x16x32_bf16(asbf((c & 1) ? one[1] : one[0]), asbf(xf), d, 0, 0, 0);
                ldsw<u2v>(L_OUT + n * OUT_ROW + gz * 8 + (16 * t * OUT_ROW + c * 32), u2v{pk2(d[0], d[1]), pk2(d[2], d[3])});
            }
        }
        BAR_L();
        const auto ro = __builtin_amdgcn_make_buffer_rsrc((void*)(u64)out, 0, 2048 * 1280, 0x00020000);
#pragma unroll
        for (int k = 0; k < 2; ++k) {
            const int id = 64 * k + lz, row = (id * 205) >> 11, ch = id - 10 * row;
            const u4v v = ldsr<u4v>(L_OUT + (8 * w + min(row, 7)) * OUT_ROW + ch * 16);
            __builtin_amdgcn_raw_buffer_store_b128(v, ro, (unsigned)((rb2 * 64 + 8 * w + row) * 1280 + cb2 * 160 + ch * 16) | oob_if(id >= 80), 0, 0);
        }
    }
    if (bid == 0 && w == 5) {
#pragma unroll
        for (int i = 0; i < 4; ++i) wait_flags(ws, WS_FH + (i * 64 + lz) * 128, tag);
        raise_flag(ws, WS_E, tag);
    }
}

extern "C" void kernel_launch(void* const* d_in, const int* in_sizes, int n_in, void* d_out, int out_size, void* d_ws, size_t ws_size, hipStream_t stream) {
    (void)in_sizes; (void)n_in; (void)out_size; (void)ws_size;
    (void)hipFuncSetAttribute((const void*)s4ffn_fused, hipFuncAttributeMaxDynamicSharedMemorySize, LDS_BYTES);
    s4ffn_fused<<<256, 512, LDS_BYTES, stream>>>((const u16*)d_in[0], (const u16*)d_in[1], (const u16*)d_in[2], (const u16*)d_in[3], (const u16*)d_in[4],
                                                 (const u16*)d_in[5], (const u16*)d_in[6], (const u16*)d_in[7], (const u16*)d_in[8], (const u16*)d_in[10],
                                                 (const u16*)d_in[11], (const u16*)d_in[12], (const u16*)d_in[13], (u16*)d_out, (char*)d_ws);
}
```

```cpp
#define STAMPS
#include <hip/hip_runtime.h>
#include <stdint.h>
#define PUB_RHO 4
#define CSUM_ON 1
#define CODE_CHUNKS 4
#define CODE_BYTES 28672u
#define PACE 0
#define PT0 7
#define HFIRST 1
#define GL 4
#define ARR 0

typedef unsigned short u16;
typedef unsigned long long u64;
typedef short s4v __attribute__((ext_vector_type(4)));
typedef __bf16 bf8v __attribute__((ext_vector_type(8)));
typedef __bf16 bf2v __attribute__((ext_vector_type(2)));
typedef float f2v __attribute__((ext_vector_type(2)));
typedef float f4v __attribute__((ext_vector_type(4)));
typedef float f16v __attribute__((ext_vector_type(16)));
typedef unsigned u2v __attribute__((ext_vector_type(2)));
typedef unsigned short h2v __attribute__((ext_vector_type(2)));
typedef unsigned u4v __attribute__((ext_vector_type(4)));
typedef __attribute__((address_space(1))) u64 gu64;
#define LDSP(T) __attribute__((address_space(3))) T*
#define DI __device__ __forceinline__
#define RLX_AGENT __ATOMIC_RELAXED, __HIP_MEMORY_SCOPE_AGENT

constexpr unsigned WS_E = 0;
constexpr unsigned WS_FH = 4096;
constexpr unsigned WS_FA = 32768;
constexpr unsigned WS_FW = 81920;
constexpr unsigned WS_FS = 40960;
constexpr unsigned WS_FC = 45056;
constexpr unsigned WS_FY = 49152;
constexpr unsigned WS_STATS = 131072;
constexpr unsigned WS_CS = 196608, WS_B1 = 204800;
constexpr unsigned WS_STAMP = 262144;
constexpr unsigned WS_XT = 1u << 20;
constexpr unsigned WS_W2T = 4u << 20;
constexpr unsigned WS_YT = 8u << 20;
constexpr unsigned WS_HA = 12u << 20;
constexpr unsigned WS_BYTES = 20u << 20;
constexpr unsigned SLOT = 32768;
constexpr unsigned L_KC = 98304, KC_CH = 16640, KC_CP = 4160;
constexpr unsigned L_UA = 148224, UA_CH = 4576, UA_PAD = 480;
constexpr unsigned L_MISC = 162368, L_CB = L_MISC, L_CNT = L_MISC + 1024;
constexpr unsigned L_TBL = 65536, TBL_ROW = 544, TBL_CH = 53 * TBL_ROW;
constexpr unsigned L_PRM = 152064, L_TT = 158464, TT_CH = 1296, L_W2U = L_UA;
constexpr unsigned L_SCR = L_CB;
constexpr unsigned L_XS = 32768, L_ST = L_XS + 10752, L_RED = 49152, L_YS = L_RED + 81920, YS_ROW = 144, L_OUT = L_YS + 81 * YS_ROW + 112, OUT_ROW = 176;
constexpr unsigned L_AX = 0, L_AST = 81920, L_ACNT = L_AST + 512, L_ACS = L_AST + 1024;
static_assert(L_TT + 3 * TT_CH <= L_MISC && L_CNT + 64 <= 163840 && L_W2U + 10240 <= L_TT, "LDS map");
constexpr unsigned LDS_BYTES = 163840;
constexpr int SPIN_LIMIT = 400000;

constexpr int NSTAMP = 16;
#define STAMP(i) do { st[i] = __builtin_amdgcn_s_memrealtime(); } while (0)

DI float bf2f(unsigned v) { return __uint_as_float(v << 16); }
DI float bflo(unsigned v) { return __uint_as_float(v << 16); }
DI float bfhi(unsigned v) { return __uint_as_float(v & 0xffff0000u); }
DI unsigned pk2(float a, float b) { bf2v v; v[0] = (__bf16)a; v[1] = (__bf16)b; return __builtin_bit_cast(unsigned, v); }
DI float rbf(float a) { return bflo(pk2(a, a)); }
template <typename P> DI u4v mkrsrc(P p, unsigned bytes) { u64 a = (u64)p; u4v r; r[0] = (unsigned)a; r[1] = (unsigned)(a >> 32); r[2] = bytes; r[3] = 0x00020000u; return r; }
DI void dma16(u4v rs, unsigned voff, unsigned soff, unsigned lds) {
    asm volatile("s_mov_b32 m0, %3\n\ts_nop 0\n\tbuffer_load_dwordx4 %0, %1, %2 offen lds" ::"v"(voff), "s"(rs), "s"(soff), "s"(lds) : "memory");
}
DI void dma16g(const void* p, unsigned lds) {
    asm volatile("s_mov_b32 m0, %1\n\ts_nop 0\n\tglobal_load_lds_dwordx4 %0, off" ::"v"(p), "s"(lds) : "memory");
}
DI void dma16g_lanes3(const void* p, unsigned lds) {
    u64 ex;
    asm volatile("s_mov_b64 %0, exec\n\ts_mov_b64 exec, 7\n\ts_mov_b32 m0, %2\n\ts_nop 0\n\tglobal_load_lds_dwordx4 %1, off\n\ts_mov_b64 exec, %0" : "=&s"(ex) : "v"(p), "s"(lds) : "memory");
}
DI void dma16_lanes16(u4v rs, unsigned voff, unsigned soff, unsigned lds) {
    u64 ex;
    asm volatile("s_mov_b64 %0, exec\n\ts_mov_b64 exec, 0xffff\n\ts_mov_b32 m0, %4\n\ts_nop 0\n\tbuffer_load_dwordx4 %1, %2, %3 offen lds\n\ts_mov_b64 exec, %0"
                 : "=&s"(ex) : "v"(voff), "s"(rs), "s"(soff), "s"(lds) : "memory");
}
DI void touch_code(unsigned chunk, int lane, unsigned lds) {
    u64 pc;
    asm volatile("s_getpc_b64 %0" : "=s"(pc));
    const u64 p = (pc & ~127ull) + min(chunk * 8192u + lane * 128u, CODE_BYTES - 128u);
    asm volatile("s_mov_b32 m0, %1\n\ts_nop 0\n\tglobal_load_lds_dword %0, off" ::"v"(p), "s"(lds) : "memory");
}
DI void dma16_sc1(u4v rs, unsigned voff, unsigned soff, unsigned lds) {
    asm volatile("s_mov_b32 m0, %3\n\ts_nop 0\n\tbuffer_load_dwordx4 %0, %1, %2 offen sc1 lds" ::"v"(voff), "s"(rs), "s"(soff), "s"(lds) : "memory");
}
#define SB() __builtin_amdgcn_sched_barrier(0)
#define WAIT_BAR(n) asm volatile("s_waitcnt vmcnt(" #n ") lgkmcnt(0)\n\ts_barrier" ::: "memory")
#define WAIT_BARN(n) asm volatile("s_waitcnt vmcnt(%0) lgkmcnt(0)\n\ts_barrier" ::"i"(n) : "memory")
#define BAR_L() asm volatile("s_waitcnt lgkmcnt(0)\n\ts_barrier" ::: "memory")
#define WAIT_V(n) asm volatile("s_waitcnt vmcnt(" #n ")" ::: "memory")
template <typename T> DI T ldsr(unsigned a) { return *(LDSP(T))a; }
template <typename T> DI void ldsw(unsigned a, T v) { *(LDSP(T))a = v; }
DI void stw(unsigned a, unsigned v, int off) { asm volatile("ds_write_b32 %0, %1 offset:%2" ::"v"(a), "v"(v), "i"(off) : "memory"); }
DI s4v trr(unsigned a) { return __builtin_amdgcn_ds_read_tr16_b64_v4i16((LDSP(s4v))a); }
DI bf8v cat(s4v a, s4v b) { return __builtin_bit_cast(bf8v, __builtin_shufflevector(a, b, 0, 1, 2, 3, 4, 5, 6, 7)); }
DI bf8v asbf(u4v a) { return __builtin_bit_cast(bf8v, a); }
template <int CTRL> DI float dpp_add(float v) { return v + __builtin_bit_cast(float, __builtin_amdgcn_update_dpp(0, __builtin_bit_cast(int, v), CTRL, 0xf, 0xf, true)); }
DI float sum8(float v) { return dpp_add<0x141>(dpp_add<0x4e>(dpp_add<0xb1>(v))); }
DI float dot2(unsigned a, unsigned b, float c) { return __builtin_amdgcn_fdot2_f32_bf16(__builtin_bit_cast(bf2v, a), __builtin_bit_cast(bf2v, b), c, false); }
DI u64 ld_flag(const char* ws, unsigned off) { return __hip_atomic_load((gu64*)(ws + off), RLX_AGENT); }
DI void wait_flags(const char* ws, unsigned off, u64 tag) {
#pragma nounroll
    for (int it = 0; it < SPIN_LIMIT; ++it) {
        if (__all(ld_flag(ws, off) == tag)) break;
        __builtin_amdgcn_s_sleep(1);
    }
    asm volatile("" ::: "memory");
}
DI unsigned lds_count(unsigned cnt_lds) {
    unsigned old, one = 1u;
    u64 ex;
    asm volatile("s_mov_b64 %1, exec\n\ts_mov_b64 exec, 1\n\ts_waitcnt lgkmcnt(0)\n\tds_add_rtn_u32 %0, %2, %3\n\ts_waitcnt lgkmcnt(0)\n\ts_mov_b64 exec, %1"
                 : "=&v"(old), "=&s"(ex) : "v"(cnt_lds), "v"(one) : "memory");
    return __builtin_amdgcn_readfirstlane(old);
}
DI void raise_flag(char* ws, unsigned flag_off, u64 tag) { __hip_atomic_store((gu64*)(ws + flag_off), tag, RLX_AGENT); }
DI void arrive_publish(unsigned cnt_lds, char* ws, unsigned flag_off, u64 tag, unsigned last = 7u) {
    if (lds_count(cnt_lds) == last) raise_flag(ws, flag_off, tag);
}
DI void lds_wait(unsigned cnt_lds, unsigned n) {
#pragma nounroll
    for (int it = 0; it < SPIN_LIMIT; ++it) {
        if (__builtin_amdgcn_readfirstlane(*(volatile LDSP(unsigned))cnt_lds) >= n) break;
        __builtin_amdgcn_s_sleep(1);
    }
    asm volatile("" ::: "memory");
}
DI unsigned oob_if(bool c) { return c ? 0x80000000u : 0u; }
DI f2v ln_stats(float s1, float s2) {
    const float mean = s1 * (1.f / 640.f), var = fmaxf(__builtin_fmaf(-mean, mean, s2 * (1.f / 640.f)), 0.f);
    const float rs = __builtin_amdgcn_rcpf(rbf(__builtin_amdgcn_sqrtf(rbf(rbf(var) + 1e-5f))));
    return f2v{rs, rbf(mean) * rs};
}
DI f2v cmul_a(f2v a, f2v b) { f2v t; asm("v_pk_mul_f32 %0, %1, %2 op_sel_hi:[0,1]" : "=v"(t) : "v"(a), "v"(b)); return t; }
DI f2v cmul_b(f2v a, f2v b, f2v t) {
    f2v o;
    asm("v_pk_fma_f32 %0, %1, %2, %3 op_sel:[1,1,0] op_sel_hi:[1,0,1] neg_lo:[0,1,0]" : "=v"(o) : "v"(a), "v"(b), "v"(t));
    return o;
}
DI f2v cmul(f2v a, f2v b) { return cmul_b(a, b, cmul_a(a, b)); }
DI f2v cexp_p(float are, float aim, float p) {
    const float m = __builtin_amdgcn_exp2f(are * p), an = __builtin_amdgcn_fractf(aim * p);
    return f2v{m * __builtin_amdgcn_cosf(an), m * __builtin_amdgcn_sinf(an)};
}

#define GP(T) __attribute__((address_space(1))) T*
typedef unsigned u8v __attribute__((ext_vector_type(8)));
DI u64 mk64(unsigned lo, unsigned hi) { return ((u64)hi << 32) | lo; }
__global__ __launch_bounds__(512, 2) void s4ffn_fused(const u16*, const u16*, const u16*, const u16*, const u16*, const u16*, const u16*, const u16*, const u16*,
                                                      const u16*, const u16*, const u16*, const u16*, u16*, char*) {
    const int tid = threadIdx.x, lane = tid & 63, w = __builtin_amdgcn_readfirstlane(tid >> 6);
    const int xcd = blockIdx.x & 7, r = ((blockIdx.x >> 3) + 28) & 31, bid = r * 8 + xcd;
    const bool is_gemm = r < 28;
    u64 st[NSTAMP];
    for (int i = 0; i < NSTAMP; ++i) st[i] = 0;
    u64 cyc0 = __builtin_amdgcn_s_memtime();
    STAMP(0);
    if (w < CODE_CHUNKS && ((r + w) & 3) == 0) touch_code(w, lane, 163840 - 256);
    u64 a_x, a_prm;
    u8v a_m;
    u4v a_t;
    asm volatile("s_load_dwordx2 %0, %4, 0x0\n\ts_load_dwordx2 %1, %4, %5 offset:0x8\n\ts_load_dwordx8 %2, %4, 0x48\n\ts_load_dwordx4 %3, %4, 0x68\n\ts_waitcnt lgkmcnt(0)"
                 : "=&s"(a_x), "=&s"(a_prm), "=&s"(a_m), "=&s"(a_t) : "s"(__builtin_amdgcn_kernarg_segment_ptr()), "s"(8 * w));
    const GP(u16) x = (const GP(u16))a_x;
    const char* prm = (const char*)a_prm;
    const GP(u16) w1 = (const GP(u16))mk64(a_m[0], a_m[1]);
    const GP(u16) b1 = (const GP(u16))mk64(a_m[2], a_m[3]);
    const GP(u16) w2 = (const GP(u16))mk64(a_m[4], a_m[5]);
    const GP(u16) b2 = (const GP(u16))mk64(a_m[6], a_m[7]);
    GP(u16) out = (GP(u16))mk64(a_t[0], a_t[1]);
    char* ws = (char*)mk64(a_t[2], a_t[3]);
    u64 tag;
    const auto wsr = __builtin_amdgcn_make_buffer_rsrc((void*)ws, 0, WS_BYTES, 0x00020000);
    const int nch = bid < 192 ? 3 : 2, ch0 = 3 * bid - max(bid - 192, 0);
    const int g = lane >> 4, l16 = lane & 15;
    const int rb2 = xcd * 4 + (r & 3), cb2 = r >> 2;
    const bool has7 = w < 6;
    u4v bw[35];
    int rot2 = 7 * cb2 + 14 * (r & 3) + w;
    rot2 = rot2 >= 54 ? rot2 - 54 : rot2;
    auto sidx = [&](int i) { const int s = rot2 + 8 * i; return s >= 54 ? s - 54 : s; };
    auto loadb = [&](int n) {
        bw[n] = __builtin_amdgcn_raw_buffer_load_b128(wsr, lane * 16 | oob_if(n >= 30 && !has7), WS_W2T + ((cb2 * 54 + sidx(n / 5)) * 5 + n % 5) * 1024, 16);
        asm volatile("" ::: "memory");
    };

    auto stage_x = [&]() {
        const u4v rx = mkrsrc(x, 2048 * 1280);
        const int n1 = 64 + (lane & 15);
        dma16(rx, (lane / 10) * 1280 + (lane % 10) * 16, (rb2 * 64 + 8 * w) * 1280 + cb2 * 160, L_XS + w * 1280);
        dma16_lanes16(rx, (n1 / 10) * 1280 + (n1 % 10) * 16, (rb2 * 64 + 8 * w) * 1280 + cb2 * 160, L_XS + w * 1280 + 1024);
        if (w == 0) dma16_lanes16(mkrsrc(b2, 1280), min(lane, 9) * 16, cb2 * 160, L_XS + 10240);
    };
    if (__builtin_expect(is_gemm, 1)) {
        if (w < 6) dma16g(prm + (ch0 + min(g, nch - 1)) * 256 + l16 * 16, L_PRM + w * 1024);
        else dma16g_lanes3(lane == 2 ? (const char*)ws + WS_E : prm + min(((ch0 * 2) & ~15) + lane * 16, 1264), L_SCR + (w - 6) * 64);
        SB();
        const int gi = r & 3, gj = r >> 2, rb = (xcd >> 1) * 4 + gi, cb = (xcd & 1) * 7 + gj;
        const int fw = w & 3, tw = w >> 2;
        const u4v rx = mkrsrc(x, 2048 * 1280), rw = mkrsrc(w1, 640 * 3456);
        unsigned vx[2];
#pragma unroll
        for (int u = 0; u < 2; ++u) vx[u] = (rb * 128 + 16 * w + 8 * u + (lane >> 3)) * 1280 + (((lane & 7) ^ (4 * u + g)) << 4);
        const int wc = l16 ^ (2 * g) ^ ((w & 1) << 3);
        unsigned vw = (unsigned)((8 * w + g) * 3456 + cb * 256 + wc * 16) | (cb == 13 ? (unsigned)(wc & 8) << 28 : 0u);
        int rot = gj + 3 * gi + xcd;
        rot = rot >= 20 ? rot - 20 : rot >= 10 ? rot - 10 : rot;
        auto stage1w = [&](int t, int slot, int p, int dw) {
            int kt = t + rot; kt = kt >= 10 ? kt - 10 : kt;
            const unsigned lb = slot * SLOT + (w + dw) * 2048 + (p >> 1) * 1024;
            if (p & 1) dma16(rw, vw + dw * 8 * 3456, kt * (64 * 3456) + (p >> 1) * 4 * 3456, lb + 16384);
            else dma16(rx, vx[p >> 1] + dw * 16 * 1280, kt * 128, lb);
        };
        auto stage1 = [&](int t, int slot, int p) { stage1w(t, slot, p, 0); };
        unsigned ix[12];
        {
            const int l0 = 8 * (tid & 255) - 3;
            const float lf = (float)l0;
            const unsigned kb = L_TT + tw * TT_CH, kc = kb - 2u * (0x4300 - 128);
#pragma unroll
            for (int i = 0; i < 6; ++i) {
                const unsigned c2 = pk2(lf + (float)(2 * i), lf + (float)(2 * i + 1));
                asm("v_mad_u32_u16 %0, %1, 2, %2" : "=v"(ix[2 * i]) : "v"(c2), "s"(kc));
                asm("v_mad_u32_u16 %0, %1, 2, %2 op_sel:[1,0,0,0]" : "=v"(ix[2 * i + 1]) : "v"(c2), "s"(kc));
            }
            if (fw == 0) {
#pragma unroll
                for (int i = 0; i < 11; ++i) {
                    const int l = l0 + i;
                    const unsigned lo = kb + 2u * min((unsigned)l, 641u), m = (unsigned)((l - 128) >> 31);
                    ix[i] = (lo & m) | (ix[i] & ~m);
                }
            }
#pragma unroll
            for (int i = 0; i < 11; ++i) asm volatile("" : "+v"(ix[i]));
        }
        const int col = 16 * (w & 1) + l16;
        unsigned ta = L_TBL + (w >> 1) * TBL_CH + l16 * TBL_ROW + g * 16, te = ta + (21 + 16 * (w & 1)) * TBL_ROW;
        unsigned ta0, ta1, tb0, tb1, st1;
        {
            const unsigned tt = L_TT + (w >> 1) * TT_CH;
            ta0 = tt + ((col << (3 - max(g - 1, 0))) + ((0x30200100u >> (8 * g)) & 0xffu) * (g == 1 ? 4u : 8u)) * 2;
            ta1 = ta0 + (g < 3 ? 4 : 128);
            tb0 = tt + (g == 0 ? 512 + col : 644) * 2; st1 = g == 0 ? 64 : 0;
            tb1 = g == 1 && col < 2 ? tt + (640 + col) * 2 : tb0;
        }
        unsigned wa[2];
#pragma unroll
        for (int k = 0; k < 2; ++k) {
            const int P = 20 * (bid & 1) + 2 * min(w + 8 * k, 9) + (lane >> 5), cbb = (P * 52) >> 8;
            wa[k] = (unsigned)(WS_W2T + (bid >> 2) * 5120 + ((bid >> 1) & 1) * 512 + (cbb * 265 + P) * 1024 + (lane & 31) * 16) | oob_if(bid >= 216 || w + 8 * k > 9);
        }
        unsigned wtl = L_W2U + (4 * (g & 1) + (l16 >> 2)) * 640 + (lane >> 5) * 32 + (lane & 3) * 8 + w * 64;
        unsigned bx[2], aw[2];
#pragma unroll
        for (int i = 0; i < 2; ++i) {
            bx[i] = (64 * tw + l16) * 128 + (((4 * i + g) ^ (l16 >> 1)) << 4);
            aw[i] = 16384 + (8 * g + (l16 >> 2)) * 256 + (((2 * fw + i) ^ (l16 >> 2) ^ ((g & 1) << 2)) << 5) + (lane & 3) * 8;
        }
        unsigned f_off;
        {
            const unsigned f1a = WS_FS + 2 * rb * 128, f1b = WS_FC + 2 * cb * 128;
            const unsigned f_w = WS_FW + (2 * min(54 * w + lane, 107) + (cb2 >> 2)) * 128, f_a = WS_FA + (lane & 31) * 128;
            const unsigned f_s = min(((lane & 2) ? f1b : f1a) + (lane & 1) * 128, WS_FC + 26 * 128);
            f_off = w < 2 ? f_w : w == 2 ? f_a : f_s;
        }
        asm volatile("" : "+v"(wa[0]), "+v"(wa[1]), "+v"(wtl), "+v"(bx[0]), "+v"(bx[1]), "+v"(aw[0]), "+v"(aw[1]), "+v"(f_off));
        asm volatile("" : "+v"(ta), "+v"(te), "+v"(ta0), "+v"(ta1), "+v"(tb0), "+v"(tb1), "+v"(st1), "+v"(vx[0]), "+v"(vx[1]), "+v"(vw));
        if (w == 0) ldsw<unsigned>(L_CNT + (lane & 7) * 4, 0u);
        STAMP(10);
        WAIT_BAR(0);
        STAMP(11);
        const unsigned so = (ch0 * 2) & 15;
        { const u2v tv = ldsr<u2v>(L_SCR + 32); tag = mk64(__builtin_amdgcn_readfirstlane(tv[0]), __builtin_amdgcn_readfirstlane(tv[1])) + 1ull; }
        const unsigned dsk01 = __builtin_amdgcn_readfirstlane(ldsr<u16>(L_SCR + 64 + so) | ((unsigned)ldsr<u16>(L_SCR + 64 + so + 2) << 16));
        const unsigned dsk2 = __builtin_amdgcn_readfirstlane(ldsr<u16>(L_SCR + 64 + so + 4));
        {
            const int hc = w < 6 ? w : w - 2, c = hc >> 1, mode = (hc & 1) * 64 + lane;
            const float ldt = bf2f(ldsr<u16>(L_SCR + so + 2 * c));
            const unsigned pb = L_PRM + c * 256 + mode * 2;
            const float pa = bf2f(ldsr<u16>(pb)), pi = bf2f(ldsr<u16>(pb + 1024));
            const f2v Bc = {bf2f(ldsr<u16>(pb + 2048)), bf2f(ldsr<u16>(pb + 3072))}, Cc = {bf2f(ldsr<u16>(pb + 4096)), bf2f(ldsr<u16>(pb + 5120))};
            auto pc = [&](int i) { if (w >= 4) { stage1w(i >> 2, i >> 2, i & 3, 0); stage1w(i >> 2, i >> 2, i & 3, -4); } };
            if (c >= nch) {
#pragma unroll
                for (int i = 0; i < 8; ++i) pc(i);
            }
            if (c < nch) {
                const float dt = rbf(__builtin_amdgcn_exp2f(ldt * 1.4426950408889634f));
                const float are = dt * pa * 1.4426950408889634f, aim = dt * pi * 0.15915494309189535f;
                const unsigned tb = L_TBL + c * TBL_CH + mode * 4;
                if (w < 6) {
                    pc(0);
                    const f2v z8 = cexp_p(are, aim, 8.f), z16 = cexp_p(are, aim, 16.f), z64 = cexp_p(are, aim, 64.f);
                    pc(1);
                    f2v e[8];
                    e[0] = f2v{1.f, 0.f}; e[1] = z8; e[2] = z16; e[3] = cmul(z16, z8); e[4] = cmul(z16, z16);
                    e[5] = cmul(e[3], z16); e[6] = cmul(e[4], z16); e[7] = cmul(e[4], e[3]);
#pragma unroll
                    for (int i = 0; i < 4; ++i) {
                        f2v t[8];
                        if (i < 3) pc(2 + 2 * i);
#pragma unroll
                        for (int j = 0; j < 8; ++j) {
                            if (i < 3) t[j] = cmul_a(e[j], z64);
                            stw(tb, pk2(e[j][0], e[j][1]), (21 + 8 * i + j) * TBL_ROW);
                        }
                        if (i < 3) {
                            pc(3 + 2 * i);
#pragma unroll
                            for (int j = 0; j < 8; ++j) e[j] = cmul_b(e[j], z64, t[j]);
                        }
                    }
                }
                if (w < 4 || w >= 6) {
                    pc(0);
                    const f2v z1 = cexp_p(are, aim, 1.f), z2 = cexp_p(are, aim, 2.f), z4 = cexp_p(are, aim, 4.f);
                    pc(1);
                    const f2v z256 = cexp_p(are, aim, 256.f), z1024 = cexp_p(are, aim, 1024.f);
                    pc(2);
                    const f2v W = cmul(Cc, Bc * dt);
                    auto putx = [&](int row, f2v v) { stw(tb, pk2(v[0], -v[1]), row * TBL_ROW); };
                    const f2v x1 = cmul(W, z1), x2 = cmul(W, z2), x8 = cmul(W, z256), x16 = cmul(W, z1024);
                    pc(3);
                    const f2v x3 = cmul(x1, z2), x9 = cmul(x8, z2), x12 = cmul(x8, z256), x17 = cmul(x16, z256), x20 = cmul(x16, z1024);
                    const f2v x14 = cmul(x12, z256), x18 = cmul(x17, z256);
                    pc(4);
                    putx(0, W); putx(1, x1); putx(2, x2); putx(3, x3); putx(8, x8); putx(9, x9); putx(12, x12); putx(14, x14);
                    pc(5);
                    putx(16, x16); putx(17, x17); putx(18, x18); putx(20, x20);
                    pc(6);
                    putx(4, cmul(W, z4)); putx(5, cmul(x1, z4)); putx(6, cmul(x2, z4)); putx(7, cmul(x3, z4));
                    pc(7);
                    putx(10, cmul(x8, z4)); putx(11, cmul(x9, z4)); putx(13, cmul(x12, z4)); putx(15, cmul(x14, z4)); putx(19, cmul(x18, z256));
                }
            }
        }
        u4v w2a, w2b;
        {
            const unsigned so2 = ((bid >> 2) * 32 + ((bid >> 1) & 1) * 8 + (w >> 2) * 16 + (w & 3) * 2) * 1280 + (bid & 1) * 640;
            asm volatile("buffer_load_dwordx4 %0, %2, %4, %5 offen\n\tbuffer_load_dwordx4 %1, %3, %4, %5 offen" : "=&v"(w2a), "=&v"(w2b)
                         : "v"(lane * 16 + (lane >= 40 ? 640 : 0)), "v"((1280 + 384 + lane * 16) | oob_if(lane >= 16)), "s"(mkrsrc(w2, 1728 * 1280)), "s"(so2) : "memory");
        }
        STAMP(13);
        BAR_L();
        if (w < 2 * nch) {
            u4v fa[8], fb[8], fe[8];
#pragma unroll
            for (int s = 0; s < 8; ++s) { fe[s] = ldsr<u4v>(te + s * 64); fa[s] = ldsr<u4v>(ta + s * 64); fb[s] = ldsr<u4v>(ta + 16 * TBL_ROW + s * 64); }
            SB();
            f4v k0 = {}, k1 = {};
#pragma unroll
            for (int s = 0; s < 8; ++s) {
                k0 = __builtin_amdgcn_mfma_f32_16x16x32_bf16(asbf(fa[s]), asbf(fe[s]), k0, 0, 0, 0);
                k1 = __builtin_amdgcn_mfma_f32_16x16x32_bf16(asbf(fb[s]), asbf(fe[s]), k1, 0, 0, 0);
            }
            ldsw<unsigned>(ta0, pk2(k0[0], k0[1]));
            ldsw<unsigned>(ta1, pk2(k0[2], k0[3]));
            const unsigned q01 = pk2(k1[0], k1[1]), q23 = pk2(k1[2], k1[3]);
            ldsw<u16>(tb1, g == 1 && col == 1 ? (u16)0 : (u16)q01);
            ldsw<u16>(tb0 + st1, (u16)(q01 >> 16));
            ldsw<u16>(tb0 + 2 * st1, (u16)q23);
            ldsw<u16>(tb0 + 3 * st1, (u16)(q23 >> 16));
        }
        BAR_L();
        STAMP(14);
        stage1(2, 2, 0); stage1(2, 2, 1); stage1(2, 2, 2); stage1(2, 2, 3);
        {
            const bool two = w < 4 && nch == 3;
            const int t = tid & 255;
#pragma unroll
            for (int k = 0; k < 2; ++k) {
                if (k == 0 || two) {
                    unsigned pe[6], po[5];
#pragma unroll
                    for (int i = 0; i < 5; ++i) pe[i] = __builtin_bit_cast(unsigned, h2v{ldsr<u16>(ix[2 * i] + k * 2 * TT_CH), ldsr<u16>(ix[2 * i + 1] + k * 2 * TT_CH)});
                    pe[5] = ldsr<u16>(ix[10] + k * 2 * TT_CH);
#pragma unroll
                    for (int i = 0; i < 5; ++i) po[i] = __builtin_amdgcn_alignbit(pe[i + 1], pe[i], 16);
                    const unsigned cbase = L_KC + (tw + 2 * k) * KC_CH, d = cbase + 32 + t * 16;
                    ldsw<u4v>(d, u4v{po[1], po[2], po[3], po[4]});
                    ldsw<u4v>(d + KC_CP, u4v{pe[1], pe[2], pe[3], pe[4]});
                    ldsw<u4v>(d + 2 * KC_CP, u4v{po[0], po[1], po[2], po[3]});
                    ldsw<u4v>(d + 3 * KC_CP, u4v{pe[0], pe[1], pe[2], pe[3]});
                }
            }
            if (w == 7) ldsw<u4v>(L_KC + min(lane >> 1, 11) * KC_CP + (lane & 1) * 16, u4v{0u, 0u, 0u, 0u});
        }
        asm volatile("s_waitcnt vmcnt(4)" : "+v"(w2a), "+v"(w2b)::"memory");
        ldsw<u4v>(L_W2U + w * 1280 + lane * 16, w2a);
        ldsw<u4v>(lane < 16 ? L_W2U + w * 1280 + 1024 + lane * 16 : L_CNT + 48, w2b);
        STAMP(1);
        f4v acc1[2][4];
#pragma unroll
        for (int i = 0; i < 8; ++i) acc1[i >> 2][i & 3] = f4v{};
        bf8v fa[2][2][2], fb[2][2][4];
        auto rd = [&](int buf, int i) {
            const int ks = i >> 1, h = i & 1;
            fa[buf][ks][h] = cat(trr(aw[h] + ks * 8192), trr(aw[h] + ks * 8192 + 1024));
            fb[buf][ks][2 * h] = asbf(ldsr<u4v>(bx[ks] + h * 4096));
            fb[buf][ks][2 * h + 1] = asbf(ldsr<u4v>(bx[ks] + h * 4096 + 2048));
        };
        unsigned sb = 0;
        int kt = rot + 2; kt = kt >= 10 ? kt - 10 : kt;
        auto tile = [&](int cur, bool more, bool stage, auto&& side) {
            const unsigned d = sb == 2 * SLOT ? (unsigned)(-2 * (int)SLOT) : SLOT, lb = sb + w * 2048;
            kt = kt == 9 ? 0 : kt + 1;
            sb += d; aw[0] += d; aw[1] += d; bx[0] += d; bx[1] += d;
#pragma unroll
            for (int i = 0; i < 4; ++i) {
                SB();
                if (more) rd(cur ^ 1, i);
                SB();
#pragma unroll
                for (int j = 0; j < 4; ++j) acc1[i & 1][j] = __builtin_amdgcn_mfma_f32_16x16x32_bf16(fa[cur][i >> 1][i & 1], fb[cur][i >> 1][j], acc1[i & 1][j], 0, 0, 0);
                SB();
                if (stage) {
                    if (i & 1) dma16(rw, vw, kt * (64 * 3456) + (i >> 1) * 4 * 3456, lb + (i >> 1) * 1024 + 16384);
                    else dma16(rx, vx[i >> 1], kt * 128, lb + (i >> 1) * 1024);
                }
                side(i);
            }
            SB();
        };
        auto none = [&](int) {};
        BAR_L();
        STAMP(15);
#pragma unroll
        for (int i = 0; i < 4; ++i) rd(0, i);
        u64 fpa, fpb;
        const u4v wsq = mkrsrc(ws, WS_BYTES);
        auto poll = [&](u64& f, bool mine) { asm volatile("global_load_dwordx2 %0, %1, %2 sc1" : "=v"(f) : "v"(mine ? f_off : WS_FW), "s"(ws) : "memory"); };
        bf8v w2f[2];
        BAR_L();
        tile(0, true, true, [&](int i) {
            if (i < 2) w2f[i] = cat(trr(wtl + i * 512), trr(wtl + i * 512 + 8 * 640));
            if (i == 3) {
                __builtin_amdgcn_raw_buffer_store_b128(__builtin_bit_cast(u4v, w2f[0]), wsr, wa[0], 0, 16);
                __builtin_amdgcn_raw_buffer_store_b128(__builtin_bit_cast(u4v, w2f[1]), wsr, wa[1], 0, 16);
            }
        });
        WAIT_BAR(6); tile(1, true, true, none);
        WAIT_BAR(6); tile(0, true, true, none);
        WAIT_BAR(4);
        STAMP(12);
        tile(1, true, true, [&](int i) { if (i == 0) arrive_publish(L_CNT + 8, ws, WS_FW + min(bid, 216) * 128, tag); });
        WAIT_BAR(4);
        tile(0, true, true, [&](int i) { if (i == 3) poll(fpa, w == 2 || w == 3); });
        WAIT_BAR(5);
        tile(1, true, true, [&](int i) { if (i == 3) poll(fpb, w < 2); });
        asm volatile("s_waitcnt vmcnt(5)" : "+v"(fpa)::"memory");
        if ((w == 2 || w == 3) && !__all(fpa == tag)) wait_flags(ws, f_off, tag);
        BAR_L();
        tile(0, true, true, [&](int i) {
            if (i == 3) {
                dma16_sc1(wsq, lane * 16, WS_XT + (ch0 + tw) * 4096 + fw * 1024, L_UA + tw * UA_CH + UA_PAD + fw * 1024);
                if (w < 4) dma16_sc1(wsq, lane * 16, WS_XT + (ch0 + nch - 1) * 4096 + w * 1024, L_UA + 2 * UA_CH + UA_PAD + w * 1024);
                if (w == 4) dma16_sc1(wsq, (lane >> 5) * (WS_B1 - WS_CS) + (lane & 31) * 16, WS_CS + cb * 512, L_CB);
            }
        });
        asm volatile("s_waitcnt vmcnt(6)" : "+v"(fpb)::"memory");
        if (w >= 5) WAIT_V(5);
        if (w < 2 && !__all(fpb == tag)) wait_flags(ws, f_off, tag);
        BAR_L();
        STAMP(3);
        tile(1, true, false, [&](int i) {
            if (i == 0) {
                if (w == 5) dma16_sc1(wsq, lane * 16, WS_STATS + rb * 1024, L_ST);
                stage_x();
            } else loadb(i - 1);
        });
        if (w < 4) WAIT_V(7); else WAIT_V(6);
        BAR_L();
        tile(0, true, false, [&](int i) { loadb(3 + i); });
        tile(1, false, false, [&](int i) { if (i == 0) loadb(7); });
        if (w >= 5) ldsw<u4v>(L_UA + (w - 5) * UA_CH + min(lane, 29) * 16, u4v{0u, 0u, 0u, 0u});
        WAIT_BAR(8);
        STAMP(2);
        u4v hp[4];
        unsigned ho;
        {
            f4v cs[2], bb[2];
#pragma unroll
            for (int u = 0; u < 2; ++u) { cs[u] = ldsr<f4v>(L_CB + (32 * fw + 16 * u + 4 * g) * 4); bb[u] = ldsr<f4v>(L_CB + 512 + (32 * fw + 16 * u + 4 * g) * 4); }
            const int s = 4 * cb + fw;
            ho = (unsigned)(WS_HA + ((2 * rb + tw) * 54 + s) * 4096 + lane * 16) | oob_if(s >= 54);
#pragma unroll
            for (int ti = 0; ti < 4; ++ti) {
                const f2v svt = ldsr<f2v>(L_ST + (64 * tw + 16 * ti + l16) * 8);
                const f2v rs2 = {svt[0], svt[0]}, nm2 = {-svt[1], -svt[1]};
#pragma unroll
                for (int i = 0; i < 4; ++i) {
                    const int u = i >> 1, e = 2 * (i & 1);
                    const f2v v = __builtin_elementwise_fma(rs2, f2v{acc1[u][ti][e], acc1[u][ti][e + 1]}, __builtin_elementwise_fma(nm2, f2v{cs[u][e], cs[u][e + 1]}, f2v{bb[u][e], bb[u][e + 1]}));
                    const f2v z = v * __builtin_elementwise_fma(v * v, f2v{-0.10294324f, -0.10294324f}, f2v{-2.30220820f, -2.30220820f});
                    const f2v d = f2v{__builtin_amdgcn_exp2f(z[0]), __builtin_amdgcn_exp2f(z[1])} + f2v{1.f, 1.f};
                    const f2v gl = v * f2v{__builtin_amdgcn_rcpf(d[0]), __builtin_amdgcn_rcpf(d[1])};
                    hp[ti][i] = pk2(gl[0], gl[1]);
                    if (4 * ti + i >= 16 - GL) { SB(); loadb(8 + 4 * ti + i - (16 - GL)); }
                }
                asm volatile("" : "+v"(hp[ti]));
                SB();
            }
        }
        STAMP(4);
        {
            const int typ = w < 3 ? 0 : w < 6 ? 1 : 2;
            const int c1 = w < 3 ? w : w < 6 ? w - 3 : 2 * (w - 6), c2 = w == 6 ? 1 : c1;
            const unsigned kl = L_KC + (l16 >> 2) * KC_CP + 56 - 16 * g + 8 * (lane & 3);
            const unsigned ul = L_UA + UA_PAD + 32 * l16 - 32 + 16 * g;
            unsigned ka[3], ua[3];
            ka[0] = kl + c1 * KC_CH;
            ka[1] = typ == 2 ? kl + c2 * KC_CH - 4 * 512 : ka[0];
            ka[2] = typ == 1 ? ka[0] - 6 * 512 : ka[1];
            ua[0] = ul + c1 * UA_CH - typ * 1024;
            ua[2] = ul + c2 * UA_CH;
            ua[1] = typ == 2 ? ua[2] : ua[0];
            bf8v uf[8], kf[8];
            auto rdk = [&](int sl) { const unsigned ad = ka[sl < 4 ? 0 : sl < 6 ? 1 : 2] + 512u * sl; kf[sl] = cat(trr(ad + 8u), trr(ad)); };
            auto rdu = [&](int j) { uf[j] = asbf(ldsr<u4v>(ua[j >= 4 ? 0 : j >= 2 ? 1 : 2] + 512u * j)); };
#pragma unroll
            for (int sl = 0; sl < 8; ++sl) { rdk(sl); rdu(7 - sl); }
            f4v A[3] = {}, B[5] = {};
            __builtin_amdgcn_raw_buffer_store_b128(hp[0], wsr, ho, 0, 16); __builtin_amdgcn_raw_buffer_store_b128(hp[1], wsr, ho + 1024, 0, 16);
            const unsigned fo = WS_FH + (rb * 14 + cb) * 128;
#pragma nounroll
            for (int rho = 0; rho < 7; ++rho) {
#pragma unroll
                for (int i = 0; i < 3; ++i) { ka[i] += 64u; ua[i] -= 64u; asm volatile("" : "+v"(ka[i]), "+v"(ua[i])); }
#pragma unroll
                for (int sl = 0; sl < 8; ++sl) {
                    SB();
                    const int ia = sl < 4 ? 0 : sl < 6 ? 1 : 2, ib = sl < 3 ? 0 : sl - 2;
                    A[ia] = __builtin_amdgcn_mfma_f32_16x16x32_bf16(kf[sl], uf[7 - sl], A[ia], 0, 0, 0);
                    if (sl < 7) B[ib] = __builtin_amdgcn_mfma_f32_16x16x32_bf16(kf[sl], uf[6 - sl], B[ib], 0, 0, 0);
                    SB();
                    rdk(sl); rdu(7 - sl);
                }
                SB();
                constexpr int NR = 30 - 8 - GL;
                if (rho == 0) { __builtin_amdgcn_raw_buffer_store_b128(hp[2], wsr, ho + 2048, 0, 16); __builtin_amdgcn_raw_buffer_store_b128(hp[3], wsr, ho + 3072, 0, 16); }
                if (rho == PUB_RHO) { WAIT_V(0); arrive_publish(L_CNT, ws, fo, tag); }
#pragma unroll
                for (int k = 0; k < 7 - PUB_RHO; ++k) {
                    constexpr int NL = (NR + 6 - PUB_RHO) / (7 - PUB_RHO);
                    if (rho == PUB_RHO + k) {
#pragma unroll
                        for (int n = 8 + GL + NL * k; n < 8 + GL + NL * (k + 1) && n < 30; ++n) loadb(n);
                    }
                }
            }
#pragma unroll
            for (int sl = 0; sl < 8; ++sl) {
                const int ia = sl < 4 ? 0 : sl < 6 ? 1 : 2, ib = sl < 3 ? 0 : sl - 2;
                A[ia] = __builtin_amdgcn_mfma_f32_16x16x32_bf16(kf[sl], uf[7 - sl], A[ia], 0, 0, 0);
                if (sl < 7) B[ib] = __builtin_amdgcn_mfma_f32_16x16x32_bf16(kf[sl], uf[6 - sl], B[ib], 0, 0, 0);
            }
            SB();
            f4v y[4];
            {
                const f4v z = {};
                const f4v a1 = typ < 2 ? A[1] : z, a1b = typ < 2 ? z : A[1], a2 = typ == 0 ? A[2] : z;
                const f4v b12 = B[1] + B[2], b34 = B[3] + B[4];
                y[0] = A[0] + a1 + a2;
                y[2] = A[2] + a1b;
                y[1] = B[0] + (typ < 2 ? b12 : z) + (typ == 0 ? b34 : z);
                y[3] = typ == 1 ? B[4] : B[2] + b34;
            }
            const unsigned yl = 32 * l16 + 8 * g;
#pragma unroll
            for (int i = 0; i < 4; ++i) {
                const int c = i < 2 ? c1 : c2, tau = (i < 2 ? 7 - 2 * typ : max(2 * typ - 1, 1)) - (i & 1);
                const unsigned off = oob_if(c >= nch || (i >= 2 && (typ == 0 || w == 7)));
                const u2v uv = ldsr<u2v>(yl + (L_UA + UA_PAD + c * UA_CH + 512 * tau));
                const float dk = __uint_as_float(((c == 2 ? dsk2 : dsk01) >> (c == 1 ? 16 : 0)) << 16);
                const u2v o = {pk2(y[i][0] + dk * bflo(uv[0]), y[i][1] + dk * bfhi(uv[0])), pk2(y[i][2] + dk * bflo(uv[1]), y[i][3] + dk * bfhi(uv[1]))};
                __builtin_amdgcn_raw_buffer_store_b64(o, wsr, yl, (WS_YT + (ch0 + c) * 4096 + 512 * tau) | off, 16);
            }
        }
        STAMP(5);
    } else {
        const int a = (r - 28) * 8 + xcd;
        const u4v rx = mkrsrc(x, 2048 * 1280);
        {
            unsigned vo[5];
#pragma unroll
            for (int i = 0; i < 5; ++i) {
                const int n = i * 64 + lane, rl = n / 80, cp = n - rl * 80;
                vo[i] = (a * 64 + 8 * w + rl) * 1280 + ((cp ^ (2 * w) ^ ((rl >> 1) << 3)) << 4);
                dma16(rx, vo[i], 0, L_AX + (w * 10 + i) * 1024);
            }
#pragma unroll
            for (int i = 5; i < 10; ++i) dma16(rx, vo[i - 5], 4 * 1280, L_AX + (w * 10 + i) * 1024);
        }
        const u64 e0 = ld_flag(ws, WS_E);
        if (w == 0) ldsw<unsigned>(L_ACNT + (lane & 7) * 4, 0u);
        STAMP(10);
        WAIT_V(1);
        STAMP(11);
        {
            const int row = 8 * w + (lane >> 3), sw = (2 * w) ^ (((row >> 1) & 1) << 3);
            u4v v[10];
#pragma unroll
            for (int i = 0; i < 10; ++i) v[i] = ldsr<u4v>(L_AX + row * 1280 + (((lane & 7) + 8 * i) ^ sw) * 16);
            SB();
            float s1[2] = {0.f, 0.f}, s2[2] = {0.f, 0.f};
#pragma unroll
            for (int i = 0; i < 10; ++i)
#pragma unroll
                for (int j = 0; j < 4; ++j) { s1[j & 1] = dot2(v[i][j], 0x3f803f80u, s1[j & 1]); s2[j & 1] = dot2(v[i][j], v[i][j], s2[j & 1]); }
            const f2v sv = ln_stats(sum8(s1[0] + s1[1]), sum8(s2[0] + s2[1]));
            ldsw<f2v>(L_AST + row * 8, sv);
            __builtin_amdgcn_raw_buffer_store_b64(__builtin_bit_cast(u2v, sv), wsr, (WS_STATS + (a * 64 + row) * 8) | oob_if(lane & 7), 0, 16);
        }
        u4v cv[10];
        {
            const auto rw1 = __builtin_amdgcn_make_buffer_rsrc((void*)(u64)w1, 0, 640 * 3456, 0x00020000);
#pragma unroll
            for (int i = 0; i < 10; ++i) cv[i] = __builtin_amdgcn_raw_buffer_load_b128(rw1, ((lane >> 3) * 3456 + (lane & 7) * 16) | oob_if(a >= 27), (80 * w + 8 * i) * 3456 + a * 128, 0);
        }
        BAR_L();
        STAMP(14);
        {
            const int o = lane & 7, cq = lane >> 3;
            f2v rs2[4], nm2[4];
#pragma unroll
            for (int i = 0; i < 4; ++i) { const f4v q = ldsr<f4v>(L_AST + o * 64 + i * 16); rs2[i] = f2v{q[0], q[2]}; nm2[i] = f2v{-q[1], -q[3]}; }
            u2v v[3][8];
#pragma unroll
            for (int k = 0; k < 3; ++k)
#pragma unroll
                for (int t = 0; t < 8; ++t) v[k][t] = ldsr<u2v>(L_AX + (8 * o + t) * 1280 + ((4 * min(w + 8 * k, 19) + (cq >> 1)) ^ ((2 * o) ^ (((t >> 1) & 1) << 3))) * 16 + (cq & 1) * 8);
            SB();
#pragma unroll
            for (int k = 0; k < 3; ++k) {
                if (k < 2 || w < 4) {
#pragma unroll
                    for (int e = 0; e < 4; ++e) {
                        u4v ov;
#pragma unroll
                        for (int p = 0; p < 4; ++p) {
                            const unsigned va = v[k][2 * p][e >> 1], vb = v[k][2 * p + 1][e >> 1];
                            const f2v n2 = __builtin_elementwise_fma((e & 1) ? f2v{bfhi(va), bfhi(vb)} : f2v{bflo(va), bflo(vb)}, rs2[p], nm2[p]);
                            ov[p] = pk2(n2[0], n2[1]);
                        }
                        __builtin_amdgcn_raw_buffer_store_b128(ov, wsr, WS_XT + (32 * (w + 8 * k) + 4 * cq + e) * 4096 + a * 128 + o * 16, 0, 16);
                        SB();
                    }
                }
                if (k == 0) {
                    WAIT_V(4);
                    tag = mk64(__builtin_amdgcn_readfirstlane((unsigned)e0), __builtin_amdgcn_readfirstlane((unsigned)(e0 >> 32))) + 1ull;
                    arrive_publish(L_ACNT + 8, ws, WS_FS + a * 128, tag);
                }
            }
        }
        STAMP(15);
        {
            unsigned selo = 0x00003f80u, sehi = 0x3f800000u;
            asm volatile("" : "+v"(selo), "+v"(sehi));
            float sm[8] = {0.f, 0.f, 0.f, 0.f, 0.f, 0.f, 0.f, 0.f};
#pragma unroll
            for (int i = 0; i < 10; ++i)
#pragma unroll
                for (int j = 0; j < 4; ++j) { sm[2 * j] = dot2(cv[i][j], selo, sm[2 * j]); sm[2 * j + 1] = dot2(cv[i][j], sehi, sm[2 * j + 1]); }
#pragma unroll
            for (int j = 0; j < 8; ++j) sm[j] = dpp_add<0x128>(sm[j]);
            ldsw<f4v>(L_ACS + (4 * w + g) * 256 + (lane & 7) * 32 + (lane & 8) * 2, (lane & 8) ? f4v{sm[4], sm[5], sm[6], sm[7]} : f4v{sm[0], sm[1], sm[2], sm[3]});
        }
        BAR_L();
        if (w != 7 || a >= 27) {
            WAIT_V(0);
            arrive_publish(L_ACNT + 12, ws, WS_FA + a * 128, tag);
        } else {
            const float bf = bf2f(b1[64 * a + lane]);
            float p[32], sum = 0.f;
#pragma unroll
            for (int i = 0; i < 32; ++i) p[i] = ldsr<float>(L_ACS + i * 256 + lane * 4);
#pragma unroll
            for (int i = 0; i < 32; i += 4) sum += (p[i] + p[i + 1]) + (p[i + 2] + p[i + 3]);
            __builtin_amdgcn_raw_buffer_store_b32(__builtin_bit_cast(unsigned, sum), wsr, WS_CS + (64 * a + lane) * 4, 0, 16);
            __builtin_amdgcn_raw_buffer_store_b32(__builtin_bit_cast(unsigned, bf), wsr, WS_B1 + (64 * a + lane) * 4, 0, 16);
            WAIT_V(0);
            raise_flag(ws, WS_FC + a * 128, tag);
            arrive_publish(L_ACNT + 12, ws, WS_FA + a * 128, tag);
        }
        STAMP(2);
        if (w < 2) wait_flags(ws, WS_FW + (2 * min(54 * w + lane, 107) + (cb2 >> 2)) * 128, tag);
        BAR_L();
        STAMP(3);
        stage_x();
#pragma unroll
        for (int n = 0; n < 30; ++n) loadb(n);
        __builtin_amdgcn_s_waitcnt(0x0F70);
    }

    asm volatile("" : "+s"(rot2));
    if (w == 0) wait_flags(ws, WS_FH + ((rb2 >> 1) * 14 + min(lane, 13)) * 128, tag);
    BAR_L();
    STAMP(6);
    u4v ha[7][4];
    auto loada = [&](int i, int t) { ha[i][t] = __builtin_amdgcn_raw_buffer_load_b128(wsr, lane * 16, WS_HA + ((rb2 * 54 + sidx(i)) * 4 + t) * 1024, 16); asm volatile("" ::: "memory"); };
#pragma unroll
    for (int i = 0; i < 8; ++i) loada(i >> 2, i & 3);
    if (is_gemm) {
        WAIT_V(8);
        arrive_publish(L_CNT + 4, ws, WS_FY + bid * 128, tag);
    }
    const int c0 = 80 * cb2;
    int lz = lane;
    asm volatile("" : "+v"(lz));
    const int cA = c0 + 10 * w, cB = cA + 9;
    const int own0 = max((cA * 0xaaab) >> 17, (cA - 192) >> 1), own1 = max((cB * 0xaaab) >> 17, (cB - 192) >> 1);
    u64 ypoll = tag;
    u4v y0, y1;
    f4v acc2[4][5];
#pragma unroll
    for (int i = 0; i < 20; ++i) acc2[i / 5][i % 5] = f4v{};
#pragma unroll
    for (int i = 0; i < 5; ++i) {
#pragma unroll
        for (int t = 0; t < 4; ++t) {
            SB();
#pragma unroll
            for (int c = 0; c < 5; ++c) acc2[t][c] = __builtin_amdgcn_mfma_f32_16x16x32_bf16(asbf(ha[i][t]), asbf(bw[5 * i + c]), acc2[t][c], 0, 0, 0);
            SB();
            if (i > 0) loada(i + 2, t);
            else if (t == 3) { loada(2, 0); loada(2, 1); loada(2, 2); loada(2, 3); }
            if (i == 1 || (i == 2 && t == 0)) loadb(30 + 4 * (i - 1) + t);
            if (i == 3 && t == 0) ypoll = ld_flag(ws, WS_FY + min(own0 + lz, own1) * 128);
            if (i == 4 && t == 3) {
                if (!__all(ypoll == tag)) wait_flags(ws, WS_FY + min(own0 + lz, own1) * 128, tag);
                y0 = __builtin_amdgcn_raw_buffer_load_b128(wsr, (lz >> 3) * 4096 + (lz & 7) * 16, WS_YT + cA * 4096 + rb2 * 128, 16);
                y1 = __builtin_amdgcn_raw_buffer_load_b128(wsr, (unsigned)((8 + (lz >> 3)) * 4096 + (lz & 7) * 16) | oob_if(lz >= 16), WS_YT + cA * 4096 + rb2 * 128, 16);
            }
        }
    }
    STAMP(7);
    const unsigned da = L_RED + (lz >> 4) * 1024 + (w >> 1) * 256 + (lz & 15) * 16 + (w & 1) * 8;
    auto dump = [&](int t) {
#pragma unroll
        for (int c = 0; c < 5; ++c) ldsw<u2v>(da + (5 * t + c) * 4096, u2v{pk2(acc2[t][c][0], acc2[t][c][1]), pk2(acc2[t][c][2], acc2[t][c][3])});
    };
#pragma unroll
    for (int t = 0; t < 4; ++t) {
        SB();
#pragma unroll
        for (int c = 0; c < 5; ++c) acc2[t][c] = __builtin_amdgcn_mfma_f32_16x16x32_bf16(asbf(ha[5][t]), asbf(bw[25 + c]), acc2[t][c], 0, 0, 0);
        SB();
        if (t > 0) dump(t - 1);
        SB();
#pragma unroll
        for (int c = 0; c < 5; ++c) acc2[t][c] = __builtin_amdgcn_mfma_f32_16x16x32_bf16(asbf(ha[6][t]), asbf(bw[30 + c]), acc2[t][c], 0, 0, 0);
    }
    SB();
    ldsw<u4v>(L_YS + (10 * w + (lz >> 3)) * YS_ROW + (lz & 7) * 16, y0);
    ldsw<u4v>(L_YS + min(10 * w + 8 + (lz >> 3) + (lz >= 16 ? 80 : 0), 80) * YS_ROW + (lz & 7) * 16, y1);
    u4v sel[4], one[2];
    {
        const int n = lz & 15, gz = lz >> 4;
        const unsigned lo = (n & 2) ? 0u : 0x3f80u << (16 * (n & 1)), hi = (n & 2) ? 0x3f80u << (16 * (n & 1)) : 0u;
#pragma unroll
        for (int q = 0; q < 4; ++q) { const unsigned m = (n >> 2) == q ? ~0u : 0u; sel[q] = u4v{lo & m, hi & m, lo & m, hi & m}; }
#pragma unroll
        for (int q = 0; q < 2; ++q) {
            const unsigned m0 = gz == 2 * q + (n >> 3) && !(n & 4) ? ~0u : 0u, m1 = gz == 2 * q + (n >> 3) && (n & 4) ? ~0u : 0u;
            one[q] = u4v{lo & m0, hi & m0, lo & m1, hi & m1};
        }
    }
    dump(3);
    BAR_L();
    STAMP(8);
    {
        const int n = lz & 15, gz = lz >> 4;
#pragma unroll
        for (int k = 0; k < 3; ++k) {
            if (k < 2 || w < 4) {
                const int tl = w + 8 * k, t = (tl * 52) >> 8, c = tl - 5 * t;
                u4v p[4];
#pragma unroll
                for (int q = 0; q < 4; ++q) p[q] = ldsr<u4v>(L_RED + lz * 16 + (tl * 4096 + q * 1024));
                const u4v yf = ldsr<u4v>(L_YS + n * YS_ROW + gz * 16 + (16 * c * YS_ROW + (t >> 1) * 64));
                const u4v xf = ldsr<u4v>(L_XS + n * 160 + gz * 16 + (16 * t * 160 + (c >> 1) * 64));
                const u2v bv = ldsr<u2v>(L_XS + 10240 + gz * 8 + c * 32);
                f4v d = {bflo(bv[0]), bfhi(bv[0]), bflo(bv[1]), bfhi(bv[1])};
#pragma unroll
                for (int q = 0; q < 4; ++q) d = __builtin_amdgcn_mfma_f32_16x16x32_bf16(asbf(p[q]), asbf(sel[q]), d, 0, 0, 0);
                d = __builtin_amdgcn_mfma_f32_16x16x32_bf16(asbf(yf), asbf((t & 1) ? one[1] : one[0]), d, 0, 0, 0);
                d = __builtin_amdgcn_mfma_f32_16x16x32_bf16(asbf((c & 1) ? one[1] : one[0]), asbf(xf), d, 0, 0, 0);
                ldsw<u2v>(L_OUT + n * OUT_ROW + gz * 8 + (16 * t * OUT_ROW + c * 32), u2v{pk2(d[0], d[1]), pk2(d[2], d[3])});
            }
        }
        BAR_L();
        const auto ro = __builtin_amdgcn_make_buffer_rsrc((void*)(u64)out, 0, 2048 * 1280, 0x00020000);
#pragma unroll
        for (int k = 0; k < 2; ++k) {
            const int id = 64 * k + lz, row = (id * 205) >> 11, ch = id - 10 * row;
            const u4v v = ldsr<u4v>(L_OUT + (8 * w + min(row, 7)) * OUT_ROW + ch * 16);
            __builtin_amdgcn_raw_buffer_store_b128(v, ro, (unsigned)((rb2 * 64 + 8 * w + row) * 1280 + cb2 * 160 + ch * 16) | oob_if(id >= 80), 0, 0);
        }
    }
    if (bid == 0 && w == 5) {
#pragma unroll
        for (int i = 0; i < 4; ++i) wait_flags(ws, WS_FH + (i * 64 + lz) * 128, tag);
        raise_flag(ws, WS_E, tag);
    }
    STAMP(9);
    if (w == 0 && bid == 5) { u64 c1 = __builtin_amdgcn_s_memtime(); *(u64*)(ws + WS_STAMP + 400000) = c1 - cyc0; *(u64*)(ws + WS_STAMP + 400008) = st[9] - st[0]; }
    for (int i = 0; i < NSTAMP; ++i) *(u64*)(ws + WS_STAMP + ((bid * 8 + w) * NSTAMP + i) * 8) = st[i];
}

__global__ void k_spin(const u64* st, int which, int mode) {
  u64 mn=~0ull; for(int i=0;i<2048;i++){ u64 a=st[i*NSTAMP]; mn=a<mn?a:mn; }
  u64 mx=0,sum=0; int n=0;
  for(int b=0;b<256;b++){ bool aux=(b>>3)>=28; u64 h=0; for(int w=0;w<8;w++){ u64 v=st[(b*8+w)*NSTAMP+which]; h=v>h?v:h; } if(h){ h-=mn; mx=h>mx?h:mx; if(aux==(mode==2)){ sum+=h; n++; } } }
  u64 d = (mode==0? mx : sum/(n?n:1))*100ull;
  if(d>1000000ull) d=1000000ull;
  u64 t0=__builtin_amdgcn_s_memrealtime();
  while(__builtin_amdgcn_s_memrealtime()-t0<d){ __builtin_amdgcn_s_sleep(8); }
}

extern "C" void kernel_launch(void* const* d_in, const int* in_sizes, int n_in, void* d_out, int out_size, void* d_ws, size_t ws_size, hipStream_t stream) {
    (void)in_sizes; (void)n_in; (void)out_size; (void)ws_size;
    (void)hipFuncSetAttribute((const void*)s4ffn_fused, hipFuncAttributeMaxDynamicSharedMemorySize, LDS_BYTES);
    s4ffn_fused<<<256, 512, LDS_BYTES, stream>>>((const u16*)d_in[0], (const u16*)d_in[1], (const u16*)d_in[2], (const u16*)d_in[3], (const u16*)d_in[4],
                                                 (const u16*)d_in[5], (const u16*)d_in[6], (const u16*)d_in[7], (const u16*)d_in[8], (const u16*)d_in[10],
                                                 (const u16*)d_in[11], (const u16*)d_in[12], (const u16*)d_in[13], (u16*)d_out, (char*)d_ws);
    k_spin<<<1,1,0,stream>>>((const u64*)((char*)d_ws+WS_STAMP),0,0);
}
```

```cpp
#include <hip/hip_runtime.h>
#include <stdint.h>
#define PUB_RHO 3
#define CSUM_ON 1
#define CODE_CHUNKS 4
#define CODE_BYTES 28672u
#define PACE 0
#define PT0 7
#define HFIRST 1
#define GL 0
#define ARR 0

typedef unsigned short u16;
typedef unsigned long long u64;
typedef short s4v __attribute__((ext_vector_type(4)));
typedef __bf16 bf8v __attribute__((ext_vector_type(8)));
typedef __bf16 bf2v __attribute__((ext_vector_type(2)));
typedef float f2v __attribute__((ext_vector_type(2)));
typedef float f4v __attribute__((ext_vector_type(4)));
typedef float f16v __attribute__((ext_vector_type(16)));
typedef unsigned u2v __attribute__((ext_vector_type(2)));
typedef unsigned short h2v __attribute__((ext_vector_type(2)));
typedef unsigned u4v __attribute__((ext_vector_type(4)));
typedef __attribute__((address_space(1))) u64 gu64;
#define LDSP(T) __attribute__((address_space(3))) T*
#define DI __device__ __forceinline__
#define RLX_AGENT __ATOMIC_RELAXED, __HIP_MEMORY_SCOPE_AGENT

constexpr unsigned WS_E = 0;
constexpr unsigned WS_FH = 4096;
constexpr unsigned WS_FA = 32768;
constexpr unsigned WS_FW = 81920;
constexpr unsigned WS_FS = 40960;
constexpr unsigned WS_FC = 45056;
constexpr unsigned WS_FY = 49152;
constexpr unsigned WS_STATS = 131072;
constexpr unsigned WS_CS = 196608, WS_B1 = 204800;
constexpr unsigned WS_STAMP = 262144;
constexpr unsigned WS_XT = 1u << 20;
constexpr unsigned WS_W2T = 4u << 20;
constexpr unsigned WS_YT = 8u << 20;
constexpr unsigned WS_HA = 12u << 20;
constexpr unsigned WS_BYTES = 20u << 20;
constexpr unsigned SLOT = 32768;
constexpr unsigned L_KC = 98304, KC_CH = 16640, KC_CP = 4160;
constexpr unsigned L_UA = 148224, UA_CH = 4576, UA_PAD = 480;
constexpr unsigned L_MISC = 162368, L_CB = L_MISC, L_CNT = L_MISC + 1024;
constexpr unsigned L_TBL = 65536, TBL_ROW = 544, TBL_CH = 53 * TBL_ROW;
constexpr unsigned L_PRM = 152064, L_TT = 158464, TT_CH = 1296, L_W2U = L_UA;
constexpr unsigned L_SCR = L_CB;
constexpr unsigned L_XS = 32768, L_ST = L_XS + 10752, L_RED = 49152, L_YS = L_RED + 81920, YS_ROW = 144, L_OUT = L_YS + 81 * YS_ROW + 112, OUT_ROW = 176;
constexpr unsigned L_AX = 0, L_AST = 81920, L_ACNT = L_AST + 512, L_ACS = L_AST + 1024;
static_assert(L_TT + 3 * TT_CH <= L_MISC && L_CNT + 64 <= 163840 && L_W2U + 10240 <= L_TT, "LDS map");
constexpr unsigned LDS_BYTES = 163840;
constexpr int SPIN_LIMIT = 400000;

#define STAMP(i)

DI float bf2f(unsigned v) { return __uint_as_float(v << 16); }
DI float bflo(unsigned v) { return __uint_as_float(v << 16); }
DI float bfhi(unsigned v) { return __uint_as_float(v & 0xffff0000u); }
DI unsigned pk2(float a, float b) { bf2v v; v[0] = (__bf16)a; v[1] = (__bf16)b; return __builtin_bit_cast(unsigned, v); }
DI float rbf(float a) { return bflo(pk2(a, a)); }
template <typename P> DI u4v mkrsrc(P p, unsigned bytes) { u64 a = (u64)p; u4v r; r[0] = (unsigned)a; r[1] = (unsigned)(a >> 32); r[2] = bytes; r[3] = 0x00020000u; return r; }
DI void dma16(u4v rs, unsigned voff, unsigned soff, unsigned lds) {
    asm volatile("s_mov_b32 m0, %3\n\ts_nop 0\n\tbuffer_load_dwordx4 %0, %1, %2 offen lds" ::"v"(voff), "s"(rs), "s"(soff), "s"(lds) : "memory");
}
DI void dma16g(const void* p, unsigned lds) {
    asm volatile("s_mov_b32 m0, %1\n\ts_nop 0\n\tglobal_load_lds_dwordx4 %0, off" ::"v"(p), "s"(lds) : "memory");
}
DI void dma16g_lanes3(const void* p, unsigned lds) {
    u64 ex;
    asm volatile("s_mov_b64 %0, exec\n\ts_mov_b64 exec, 7\n\ts_mov_b32 m0, %2\n\ts_nop 0\n\tglobal_load_lds_dwordx4 %1, off\n\ts_mov_b64 exec, %0" : "=&s"(ex) : "v"(p), "s"(lds) : "memory");
}
DI void dma16_lanes16(u4v rs, unsigned voff, unsigned soff, unsigned lds) {
    u64 ex;
    asm volatile("s_mov_b64 %0, exec\n\ts_mov_b64 exec, 0xffff\n\ts_mov_b32 m0, %4\n\ts_nop 0\n\tbuffer_load_dwordx4 %1, %2, %3 offen lds\n\ts_mov_b64 exec, %0"
                 : "=&s"(ex) : "v"(voff), "s"(rs), "s"(soff), "s"(lds) : "memory");
}
DI void touch_code(unsigned chunk, int lane, unsigned lds) {
    u64 pc;
    asm volatile("s_getpc_b64 %0" : "=s"(pc));
    const u64 p = (pc & ~127ull) + min(chunk * 8192u + lane * 128u, CODE_BYTES - 128u);
    asm volatile("s_mov_b32 m0, %1\n\ts_nop 0\n\tglobal_load_lds_dword %0, off" ::"v"(p), "s"(lds) : "memory");
}
DI void dma16_sc1(u4v rs, unsigned voff, unsigned soff, unsigned lds) {
    asm volatile("s_mov_b32 m0, %3\n\ts_nop 0\n\tbuffer_load_dwordx4 %0, %1, %2 offen sc1 lds" ::"v"(voff), "s"(rs), "s"(soff), "s"(lds) : "memory");
}
#define SB() __builtin_amdgcn_sched_barrier(0)
#define WAIT_BAR(n) asm volatile("s_waitcnt vmcnt(" #n ") lgkmcnt(0)\n\ts_barrier" ::: "memory")
#define WAIT_BARN(n) asm volatile("s_waitcnt vmcnt(%0) lgkmcnt(0)\n\ts_barrier" ::"i"(n) : "memory")
#define BAR_L() asm volatile("s_waitcnt lgkmcnt(0)\n\ts_barrier" ::: "memory")
#define WAIT_V(n) asm volatile("s_waitcnt vmcnt(" #n ")" ::: "memory")
template <typename T> DI T ldsr(unsigned a) { return *(LDSP(T))a; }
template <typename T> DI void ldsw(unsigned a, T v) { *(LDSP(T))a = v; }
DI void stw(unsigned a, unsigned v, int off) { asm volatile("ds_write_b32 %0, %1 offset:%2" ::"v"(a), "v"(v), "i"(off) : "memory"); }
DI s4v trr(unsigned a) { return __builtin_amdgcn_ds_read_tr16_b64_v4i16((LDSP(s4v))a); }
DI bf8v cat(s4v a, s4v b) { return __builtin_bit_cast(bf8v, __builtin_shufflevector(a, b, 0, 1, 2, 3, 4, 5, 6, 7)); }
DI bf8v asbf(u4v a) { return __builtin_bit_cast(bf8v, a); }
template <int CTRL> DI float dpp_add(float v) { return v + __builtin_bit_cast(float, __builtin_amdgcn_update_dpp(0, __builtin_bit_cast(int, v), CTRL, 0xf, 0xf, true)); }
DI float sum8(float v) { return dpp_add<0x141>(dpp_add<0x4e>(dpp_add<0xb1>(v))); }
DI float dot2(unsigned a, unsigned b, float c) { return __builtin_amdgcn_fdot2_f32_bf16(__builtin_bit_cast(bf2v, a), __builtin_bit_cast(bf2v, b), c, false); }
DI u64 ld_flag(const char* ws, unsigned off) { return __hip_atomic_load((gu64*)(ws + off), RLX_AGENT); }
DI void wait_flags(const char* ws, unsigned off, u64 tag) {
#pragma nounroll
    for (int it = 0; it < SPIN_LIMIT; ++it) {
        if (__all(ld_flag(ws, off) == tag)) break;
        __builtin_amdgcn_s_sleep(1);
    }
    asm volatile("" ::: "memory");
}
DI unsigned lds_count(unsigned cnt_lds) {
    unsigned old, one = 1u;
    u64 ex;
    asm volatile("s_mov_b64 %1, exec\n\ts_mov_b64 exec, 1\n\ts_waitcnt lgkmcnt(0)\n\tds_add_rtn_u32 %0, %2, %3\n\ts_waitcnt lgkmcnt(0)\n\ts_mov_b64 exec, %1"
                 : "=&v"(old), "=&s"(ex) : "v"(cnt_lds), "v"(one) : "memory");
    return __builtin_amdgcn_readfirstlane(old);
}
DI void raise_flag(char* ws, unsigned flag_off, u64 tag) { __hip_atomic_store((gu64*)(ws + flag_off), tag, RLX_AGENT); }
DI void arrive_publish(unsigned cnt_lds, char* ws, unsigned flag_off, u64 tag, unsigned last = 7u) {
    if (lds_count(cnt_lds) == last) raise_flag(ws, flag_off, tag);
}
DI void lds_wait(unsigned cnt_lds, unsigned n) {
#pragma nounroll
    for (int it = 0; it < SPIN_LIMIT; ++it) {
        if (__builtin_amdgcn_readfirstlane(*(volatile LDSP(unsigned))cnt_lds) >= n) break;
        __builtin_amdgcn_s_sleep(1);
    }
    asm volatile("" ::: "memory");
}
DI unsigned oob_if(bool c) { return c ? 0x80000000u : 0u; }
DI f2v ln_stats(float s1, float s2) {
    const float mean = s1 * (1.f / 640.f), var = fmaxf(__builtin_fmaf(-mean, mean, s2 * (1.f / 640.f)), 0.f);
    const float rs = __builtin_amdgcn_rcpf(rbf(__builtin_amdgcn_sqrtf(rbf(rbf(var) + 1e-5f))));
    return f2v{rs, rbf(mean) * rs};
}
DI f2v cmul_a(f2v a, f2v b) { f2v t; asm("v_pk_mul_f32 %0, %1, %2 op_sel_hi:[0,1]" : "=v"(t) : "v"(a), "v"(b)); return t; }
DI f2v cmul_b(f2v a, f2v b, f2v t) {
    f2v o;
    asm("v_pk_fma_f32 %0, %1, %2, %3 op_sel:[1,1,0] op_sel_hi:[1,0,1] neg_lo:[0,1,0]" : "=v"(o) : "v"(a), "v"(b), "v"(t));
    return o;
}
DI f2v cmul(f2v a, f2v b) { return cmul_b(a, b, cmul_a(a, b)); }
DI f2v cexp_p(float are, float aim, float p) {
    const float m = __builtin_amdgcn_exp2f(are * p), an = __builtin_amdgcn_fractf(aim * p);
    return f2v{m * __builtin_amdgcn_cosf(an), m * __builtin_amdgcn_sinf(an)};
}

#define GP(T) __attribute__((address_space(1))) T*
typedef unsigned u8v __attribute__((ext_vector_type(8)));
DI u64 mk64(unsigned lo, unsigned hi) { return ((u64)hi << 32) | lo; }
__global__ __launch_bounds__(512, 2) void s4ffn_fused(const u16*, const u16*, const u16*, const u16*, const u16*, const u16*, const u16*, const u16*, const u16*,
                                                      const u16*, const u16*, const u16*, const u16*, u16*, char*) {
    const int tid = threadIdx.x, lane = tid & 63, w = __builtin_amdgcn_readfirstlane(tid >> 6);
    const int xcd = blockIdx.x & 7, r = ((blockIdx.x >> 3) + 28) & 31, bid = r * 8 + xcd;
    const bool is_gemm = r < 28;
    STAMP(0);
    if (w < CODE_CHUNKS && ((r + w) & 3) == 0) touch_code(w, lane, 163840 - 256);
    u64 a_x, a_prm;
    u8v a_m;
    u4v a_t;
    asm volatile("s_load_dwordx2 %0, %4, 0x0\n\ts_load_dwordx2 %1, %4, %5 offset:0x8\n\ts_load_dwordx8 %2, %4, 0x48\n\ts_load_dwordx4 %3, %4, 0x68\n\ts_waitcnt lgkmcnt(0)"
                 : "=&s"(a_x), "=&s"(a_prm), "=&s"(a_m), "=&s"(a_t) : "s"(__builtin_amdgcn_kernarg_segment_ptr()), "s"(8 * w));
    const GP(u16) x = (const GP(u16))a_x;
    const char* prm = (const char*)a_prm;
    const GP(u16) w1 = (const GP(u16))mk64(a_m[0], a_m[1]);
    const GP(u16) b1 = (const GP(u16))mk64(a_m[2], a_m[3]);
    const GP(u16) w2 = (const GP(u16))mk64(a_m[4], a_m[5]);
    const GP(u16) b2 = (const GP(u16))mk64(a_m[6], a_m[7]);
    GP(u16) out = (GP(u16))mk64(a_t[0], a_t[1]);
    char* ws = (char*)mk64(a_t[2], a_t[3]);
    u64 tag;
    const auto wsr = __builtin_amdgcn_make_buffer_rsrc((void*)ws, 0, WS_BYTES, 0x00020000);
    const int nch = bid < 192 ? 3 : 2, ch0 = 3 * bid - max(bid - 192, 0);
    const int g = lane >> 4, l16 = lane & 15;
    const int rb2 = xcd * 4 + (r & 3), cb2 = r >> 2;
    const bool has7 = w < 6;
    u4v bw[35];
    int rot2 = 7 * cb2 + 14 * (r & 3) + w;
    rot2 = rot2 >= 54 ? rot2 - 54 : rot2;
    auto sidx = [&](int i) { const int s = rot2 + 8 * i; return s >= 54 ? s - 54 : s; };
    auto loadb = [&](int n) {
        bw[n] = __builtin_amdgcn_raw_buffer_load_b128(wsr, lane * 16 | oob_if(n >= 30 && !has7), WS_W2T + ((cb2 * 54 + sidx(n / 5)) * 5 + n % 5) * 1024, 16);
        asm volatile("" ::: "memory");
    };

    auto stage_x = [&]() {
        const u4v rx = mkrsrc(x, 2048 * 1280);
        const int n1 = 64 + (lane & 15);
        dma16(rx, (lane / 10) * 1280 + (lane % 10) * 16, (rb2 * 64 + 8 * w) * 1280 + cb2 * 160, L_XS + w * 1280);
        dma16_lanes16(rx, (n1 / 10) * 1280 + (n1 % 10) * 16, (rb2 * 64 + 8 * w) * 1280 + cb2 * 160, L_XS + w * 1280 + 1024);
        if (w == 0) dma16_lanes16(mkrsrc(b2, 1280), min(lane, 9) * 16, cb2 * 160, L_XS + 10240);
    };
    if (__builtin_expect(is_gemm, 1)) {
        if (w < 6) dma16g(prm + (ch0 + min(g, nch - 1)) * 256 + l16 * 16, L_PRM + w * 1024);
        else dma16g_lanes3(lane == 2 ? (const char*)ws + WS_E : prm + min(((ch0 * 2) & ~15) + lane * 16, 1264), L_SCR + (w - 6) * 64);
        SB();
        const int gi = r & 3, gj = r >> 2, rb = (xcd >> 1) * 4 + gi, cb = (xcd & 1) * 7 + gj;
        const int fw = w & 3, tw = w >> 2;
        const u4v rx = mkrsrc(x, 2048 * 1280), rw = mkrsrc(w1, 640 * 3456);
        unsigned vx[2];
#pragma unroll
        for (int u = 0; u < 2; ++u) vx[u] = (rb * 128 + 16 * w + 8 * u + (lane >> 3)) * 1280 + (((lane & 7) ^ (4 * u + g)) << 4);
        const int wc = l16 ^ (2 * g) ^ ((w & 1) << 3);
        unsigned vw = (unsigned)((8 * w + g) * 3456 + cb * 256 + wc * 16) | (cb == 13 ? (unsigned)(wc & 8) << 28 : 0u);
        int rot = gj + 3 * gi + xcd;
        rot = rot >= 20 ? rot - 20 : rot >= 10 ? rot - 10 : rot;
        auto stage1w = [&](int t, int slot, int p, int dw) {
            int kt = t + rot; kt = kt >= 10 ? kt - 10 : kt;
            const unsigned lb = slot * SLOT + (w + dw) * 2048 + (p >> 1) * 1024;
            if (p & 1) dma16(rw, vw + dw * 8 * 3456, kt * (64 * 3456) + (p >> 1) * 4 * 3456, lb + 16384);
            else dma16(rx, vx[p >> 1] + dw * 16 * 1280, kt * 128, lb);
        };
        auto stage1 = [&](int t, int slot, int p) { stage1w(t, slot, p, 0); };
        unsigned ix[12];
        {
            const int l0 = 8 * (tid & 255) - 3;
            const float lf = (float)l0;
            const unsigned kb = L_TT + tw * TT_CH, kc = kb - 2u * (0x4300 - 128);
#pragma unroll
            for (int i = 0; i < 6; ++i) {
                const unsigned c2 = pk2(lf + (float)(2 * i), lf + (float)(2 * i + 1));
                asm("v_mad_u32_u16 %0, %1, 2, %2" : "=v"(ix[2 * i]) : "v"(c2), "s"(kc));
                asm("v_mad_u32_u16 %0, %1, 2, %2 op_sel:[1,0,0,0]" : "=v"(ix[2 * i + 1]) : "v"(c2), "s"(kc));
            }
            if (fw == 0) {
#pragma unroll
                for (int i = 0; i < 11; ++i) {
                    const int l = l0 + i;
                    const unsigned lo = kb + 2u * min((unsigned)l, 641u), m = (unsigned)((l - 128) >> 31);
                    ix[i] = (lo & m) | (ix[i] & ~m);
                }
            }
#pragma unroll
            for (int i = 0; i < 11; ++i) asm volatile("" : "+v"(ix[i]));
        }
        const int col = 16 * (w & 1) + l16;
        unsigned ta = L_TBL + (w >> 1) * TBL_CH + l16 * TBL_ROW + g * 16, te = ta + (21 + 16 * (w & 1)) * TBL_ROW;
        unsigned ta0, ta1, tb0, tb1, st1;
        {
            const unsigned tt = L_TT + (w >> 1) * TT_CH;
            ta0 = tt + ((col << (3 - max(g - 1, 0))) + ((0x30200100u >> (8 * g)) & 0xffu) * (g == 1 ? 4u : 8u)) * 2;
            ta1 = ta0 + (g < 3 ? 4 : 128);
            tb0 = tt + (g == 0 ? 512 + col : 644) * 2; st1 = g == 0 ? 64 : 0;
            tb1 = g == 1 && col < 2 ? tt + (640 + col) * 2 : tb0;
        }
        unsigned wa[2];
#pragma unroll
        for (int k = 0; k < 2; ++k) {
            const int P = 20 * (bid & 1) + 2 * min(w + 8 * k, 9) + (lane >> 5), cbb = (P * 52) >> 8;
            wa[k] = (unsigned)(WS_W2T + (bid >> 2) * 5120 + ((bid >> 1) & 1) * 512 + (cbb * 265 + P) * 1024 + (lane & 31) * 16) | oob_if(bid >= 216 || w + 8 * k > 9);
        }
        unsigned wtl = L_W2U + (4 * (g & 1) + (l16 >> 2)) * 640 + (lane >> 5) * 32 + (lane & 3) * 8 + w * 64;
        unsigned bx[2], aw[2];
#pragma unroll
        for (int i = 0; i < 2; ++i) {
            bx[i] = (64 * tw + l16) * 128 + (((4 * i + g) ^ (l16 >> 1)) << 4);
            aw[i] = 16384 + (8 * g + (l16 >> 2)) * 256 + (((2 * fw + i) ^ (l16 >> 2) ^ ((g & 1) << 2)) << 5) + (lane & 3) * 8;
        }
        unsigned f_off;
        {
            const unsigned f1a = WS_FS + 2 * rb * 128, f1b = WS_FC + 2 * cb * 128;
            const unsigned f_w = WS_FW + (2 * min(54 * w + lane, 107) + (cb2 >> 2)) * 128, f_a = WS_FA + (lane & 31) * 128;
            const unsigned f_s = min(((lane & 2) ? f1b : f1a) + (lane & 1) * 128, WS_FC + 26 * 128);
            f_off = w < 2 ? f_w : w == 2 ? f_a : f_s;
        }
        asm volatile("" : "+v"(wa[0]), "+v"(wa[1]), "+v"(wtl), "+v"(bx[0]), "+v"(bx[1]), "+v"(aw[0]), "+v"(aw[1]), "+v"(f_off));
        asm volatile("" : "+v"(ta), "+v"(te), "+v"(ta0), "+v"(ta1), "+v"(tb0), "+v"(tb1), "+v"(st1), "+v"(vx[0]), "+v"(vx[1]), "+v"(vw));
        if (w == 0) ldsw<unsigned>(L_CNT + (lane & 7) * 4, 0u);
        STAMP(10);
        WAIT_BAR(0);
        STAMP(11);
        const unsigned so = (ch0 * 2) & 15;
        { const u2v tv = ldsr<u2v>(L_SCR + 32); tag = mk64(__builtin_amdgcn_readfirstlane(tv[0]), __builtin_amdgcn_readfirstlane(tv[1])) + 1ull; }
        const unsigned dsk01 = __builtin_amdgcn_readfirstlane(ldsr<u16>(L_SCR + 64 + so) | ((unsigned)ldsr<u16>(L_SCR + 64 + so + 2) << 16));
        const unsigned dsk2 = __builtin_amdgcn_readfirstlane(ldsr<u16>(L_SCR + 64 + so + 4));
        {
            const int hc = w < 6 ? w : w - 2, c = hc >> 1, mode = (hc & 1) * 64 + lane;
            const float ldt = bf2f(ldsr<u16>(L_SCR + so + 2 * c));
            const unsigned pb = L_PRM + c * 256 + mode * 2;
            const float pa = bf2f(ldsr<u16>(pb)), pi = bf2f(ldsr<u16>(pb + 1024));
            const f2v Bc = {bf2f(ldsr<u16>(pb + 2048)), bf2f(ldsr<u16>(pb + 3072))}, Cc = {bf2f(ldsr<u16>(pb + 4096)), bf2f(ldsr<u16>(pb + 5120))};
            auto pc = [&](int i) { if (w >= 4) { stage1w(i >> 2, i >> 2, i & 3, 0); stage1w(i >> 2, i >> 2, i & 3, -4); } };
            if (c >= nch) {
#pragma unroll
                for (int i = 0; i < 8; ++i) pc(i);
            }
            if (c < nch) {
                const float dt = rbf(__builtin_amdgcn_exp2f(ldt * 1.4426950408889634f));
                const float are = dt * pa * 1.4426950408889634f, aim = dt * pi * 0.15915494309189535f;
                const unsigned tb = L_TBL + c * TBL_CH + mode * 4;
                if (w < 6) {
                    pc(0);
                    const f2v z8 = cexp_p(are, aim, 8.f), z16 = cexp_p(are, aim, 16.f), z64 = cexp_p(are, aim, 64.f);
                    pc(1);
                    f2v e[8];
                    e[0] = f2v{1.f, 0.f}; e[1] = z8; e[2] = z16; e[3] = cmul(z16, z8); e[4] = cmul(z16, z16);
                    e[5] = cmul(e[3], z16); e[6] = cmul(e[4], z16); e[7] = cmul(e[4], e[3]);
#pragma unroll
                    for (int i = 0; i < 4; ++i) {
                        f2v t[8];
                        if (i < 3) pc(2 + 2 * i);
#pragma unroll
                        for (int j = 0; j < 8; ++j) {
                            if (i < 3) t[j] = cmul_a(e[j], z64);
                            stw(tb, pk2(e[j][0], e[j][1]), (21 + 8 * i + j) * TBL_ROW);
                        }
                        if (i < 3) {
                            pc(3 + 2 * i);
#pragma unroll
                            for (int j = 0; j < 8; ++j) e[j] = cmul_b(e[j], z64, t[j]);
                        }
                    }
                }
                if (w < 4 || w >= 6) {
                    pc(0);
                    const f2v z1 = cexp_p(are, aim, 1.f), z2 = cexp_p(are, aim, 2.f), z4 = cexp_p(are, aim, 4.f);
                    pc(1);
                    const f2v z256 = cexp_p(are, aim, 256.f), z1024 = cexp_p(are, aim, 1024.f);
                    pc(2);
                    const f2v W = cmul(Cc, Bc * dt);
                    auto putx = [&](int row, f2v v) { stw(tb, pk2(v[0], -v[1]), row * TBL_ROW); };
                    const f2v x1 = cmul(W, z1), x2 = cmul(W, z2), x8 = cmul(W, z256), x16 = cmul(W, z1024);
                    pc(3);
                    const f2v x3 = cmul(x1, z2), x9 = cmul(x8, z2), x12 = cmul(x8, z256), x17 = cmul(x16, z256), x20 = cmul(x16, z1024);
                    const f2v x14 = cmul(x12, z256), x18 = cmul(x17, z256);
                    pc(4);
                    putx(0, W); putx(1, x1); putx(2, x2); putx(3, x3); putx(8, x8); putx(9, x9); putx(12, x12); putx(14, x14);
                    pc(5);
                    putx(16, x16); putx(17, x17); putx(18, x18); putx(20, x20);
                    pc(6);
                    putx(4, cmul(W, z4)); putx(5, cmul(x1, z4)); putx(6, cmul(x2, z4)); putx(7, cmul(x3, z4));
                    pc(7);
                    putx(10, cmul(x8, z4)); putx(11, cmul(x9, z4)); putx(13, cmul(x12, z4)); putx(15, cmul(x14, z4)); putx(19, cmul(x18, z256));
                }
            }
        }
        u4v w2a, w2b;
        {
            const unsigned so2 = ((bid >> 2) * 32 + ((bid >> 1) & 1) * 8 + (w >> 2) * 16 + (w & 3) * 2) * 1280 + (bid & 1) * 640;
            asm volatile("buffer_load_dwordx4 %0, %2, %4, %5 offen\n\tbuffer_load_dwordx4 %1, %3, %4, %5 offen" : "=&v"(w2a), "=&v"(w2b)
                         : "v"(lane * 16 + (lane >= 40 ? 640 : 0)), "v"((1280 + 384 + lane * 16) | oob_if(lane >= 16)), "s"(mkrsrc(w2, 1728 * 1280)), "s"(so2) : "memory");
        }
        STAMP(13);
        BAR_L();
        if (w < 2 * nch) {
            u4v fa[8], fb[8], fe[8];
#pragma unroll
            for (int s = 0; s < 8; ++s) { fe[s] = ldsr<u4v>(te + s * 64); fa[s] = ldsr<u4v>(ta + s * 64); fb[s] = ldsr<u4v>(ta + 16 * TBL_ROW + s * 64); }
            SB();
            f4v k0 = {}, k1 = {};
#pragma unroll
            for (int s = 0; s < 8; ++s) {
                k0 = __builtin_amdgcn_mfma_f32_16x16x32_bf16(asbf(fa[s]), asbf(fe[s]), k0, 0, 0, 0);
                k1 = __builtin_amdgcn_mfma_f32_16x16x32_bf16(asbf(fb[s]), asbf(fe[s]), k1, 0, 0, 0);
            }
            ldsw<unsigned>(ta0, pk2(k0[0], k0[1]));
            ldsw<unsigned>(ta1, pk2(k0[2], k0[3]));
            const unsigned q01 = pk2(k1[0], k1[1]), q23 = pk2(k1[2], k1[3]);
            ldsw<u16>(tb1, g == 1 && col == 1 ? (u16)0 : (u16)q01);
            ldsw<u16>(tb0 + st1, (u16)(q01 >> 16));
            ldsw<u16>(tb0 + 2 * st1, (u16)q23);
            ldsw<u16>(tb0 + 3 * st1, (u16)(q23 >> 16));
        }
        BAR_L();
        STAMP(14);
        stage1(2, 2, 0); stage1(2, 2, 1); stage1(2, 2, 2); stage1(2, 2, 3);
        {
            const bool two = w < 4 && nch == 3;
            const int t = tid & 255;
#pragma unroll
            for (int k = 0; k < 2; ++k) {
                if (k == 0 || two) {
                    unsigned pe[6], po[5];
#pragma unroll
                    for (int i = 0; i < 5; ++i) pe[i] = __builtin_bit_cast(unsigned, h2v{ldsr<u16>(ix[2 * i] + k * 2 * TT_CH), ldsr<u16>(ix[2 * i + 1] + k * 2 * TT_CH)});
                    pe[5] = ldsr<u16>(ix[10] + k * 2 * TT_CH);
#pragma unroll
                    for (int i = 0; i < 5; ++i) po[i] = __builtin_amdgcn_alignbit(pe[i + 1], pe[i], 16);
                    const unsigned cbase = L_KC + (tw + 2 * k) * KC_CH, d = cbase + 32 + t * 16;
                    ldsw<u4v>(d, u4v{po[1], po[2], po[3], po[4]});
                    ldsw<u4v>(d + KC_CP, u4v{pe[1], pe[2], pe[3], pe[4]});
                    ldsw<u4v>(d + 2 * KC_CP, u4v{po[0], po[1], po[2], po[3]});
                    ldsw<u4v>(d + 3 * KC_CP, u4v{pe[0], pe[1], pe[2], pe[3]});
                }
            }
            if (w == 7) ldsw<u4v>(L_KC + min(lane >> 1, 11) * KC_CP + (lane & 1) * 16, u4v{0u, 0u, 0u, 0u});
        }
        asm volatile("s_waitcnt vmcnt(4)" : "+v"(w2a), "+v"(w2b)::"memory");
        ldsw<u4v>(L_W2U + w * 1280 + lane * 16, w2a);
        ldsw<u4v>(lane < 16 ? L_W2U + w * 1280 + 1024 + lane * 16 : L_CNT + 48, w2b);
        STAMP(1);
        f4v acc1[2][4];
#pragma unroll
        for (int i = 0; i < 8; ++i) acc1[i >> 2][i & 3] = f4v{};
        bf8v fa[2][2][2], fb[2][2][4];
        auto rd = [&](int buf, int i) {
            const int ks = i >> 1, h = i & 1;
            fa[buf][ks][h] = cat(trr(aw[h] + ks * 8192), trr(aw[h] + ks * 8192 + 1024));
            fb[buf][ks][2 * h] = asbf(ldsr<u4v>(bx[ks] + h * 4096));
            fb[buf][ks][2 * h + 1] = asbf(ldsr<u4v>(bx[ks] + h * 4096 + 2048));
        };
        unsigned sb = 0;
        int kt = rot + 2; kt = kt >= 10 ? kt - 10 : kt;
        auto tile = [&](int cur, bool more, bool stage, auto&& side) {
            const unsigned d = sb == 2 * SLOT ? (unsigned)(-2 * (int)SLOT) : SLOT, lb = sb + w * 2048;
            kt = kt == 9 ? 0 : kt + 1;
            sb += d; aw[0] += d; aw[1] += d; bx[0] += d; bx[1] += d;
#pragma unroll
            for (int i = 0; i < 4; ++i) {
                SB();
                if (more) rd(cur ^ 1, i);
                SB();
#pragma unroll
                for (int j = 0; j < 4; ++j) acc1[i & 1][j] = __builtin_amdgcn_mfma_f32_16x16x32_bf16(fa[cur][i >> 1][i & 1], fb[cur][i >> 1][j], acc1[i & 1][j], 0, 0, 0);
                SB();
                if (stage) {
                    if (i & 1) dma16(rw, vw, kt * (64 * 3456) + (i >> 1) * 4 * 3456, lb + (i >> 1) * 1024 + 16384);
                    else dma16(rx, vx[i >> 1], kt * 128, lb + (i >> 1) * 1024);
                }
                side(i);
            }
            SB();
        };
        auto none = [&](int) {};
        BAR_L();
        STAMP(15);
#pragma unroll
        for (int i = 0; i < 4; ++i) rd(0, i);
        u64 fpa, fpb;
        const u4v wsq = mkrsrc(ws, WS_BYTES);
        auto poll = [&](u64& f, bool mine) { asm volatile("global_load_dwordx2 %0, %1, %2 sc1" : "=v"(f) : "v"(mine ? f_off : WS_FW), "s"(ws) : "memory"); };
        bf8v w2f[2];
        BAR_L();
        tile(0, true, true, [&](int i) {
            if (i < 2) w2f[i] = cat(trr(wtl + i * 512), trr(wtl + i * 512 + 8 * 640));
            if (i == 3) {
                __builtin_amdgcn_raw_buffer_store_b128(__builtin_bit_cast(u4v, w2f[0]), wsr, wa[0], 0, 16);
                __builtin_amdgcn_raw_buffer_store_b128(__builtin_bit_cast(u4v, w2f[1]), wsr, wa[1], 0, 16);
            }
        });
        WAIT_BAR(6); tile(1, true, true, none);
        WAIT_BAR(6); tile(0, true, true, none);
        WAIT_BAR(4);
        STAMP(12);
        tile(1, true, true, [&](int i) { if (i == 0) arrive_publish(L_CNT + 8, ws, WS_FW + bid * 128, tag); });
        WAIT_BAR(4);
        tile(0, true, true, [&](int i) { if (i == 3) poll(fpa, w == 2 || w == 3); });
        WAIT_BAR(5);
        tile(1, true, true, [&](int i) { if (i == 3) poll(fpb, w < 2); });
        asm volatile("s_waitcnt vmcnt(5)" : "+v"(fpa)::"memory");
        if ((w == 2 || w == 3) && !__all(fpa == tag)) wait_flags(ws, f_off, tag);
        BAR_L();
        tile(0, true, true, [&](int i) {
            if (i == 3) {
                dma16_sc1(wsq, lane * 16, WS_XT + (ch0 + tw) * 4096 + fw * 1024, L_UA + tw * UA_CH + UA_PAD + fw * 1024);
                if (w < 4) dma16_sc1(wsq, lane * 16, WS_XT + (ch0 + nch - 1) * 4096 + w * 1024, L_UA + 2 * UA_CH + UA_PAD + w * 1024);
                if (w == 4) dma16_sc1(wsq, (lane >> 5) * (WS_B1 - WS_CS) + (lane & 31) * 16, WS_CS + cb * 512, L_CB);
            }
        });
        asm volatile("s_waitcnt vmcnt(6)" : "+v"(fpb)::"memory");
        if (w >= 5) WAIT_V(5);
        if (w < 2 && !__all(fpb == tag)) wait_flags(ws, f_off, tag);
        BAR_L();
        STAMP(3);
        tile(1, true, false, [&](int i) {
            if (i == 0) {
                if (w == 5) dma16_sc1(wsq, lane * 16, WS_STATS + rb * 1024, L_ST);
                stage_x();
            } else loadb(i - 1);
        });
        if (w < 4) WAIT_V(7); else WAIT_V(6);
        BAR_L();
        tile(0, true, false, [&](int i) { loadb(3 + i); });
        tile(1, false, false, [&](int i) { if (i == 0) loadb(7); });
        if (w >= 5) ldsw<u4v>(L_UA + (w - 5) * UA_CH + min(lane, 29) * 16, u4v{0u, 0u, 0u, 0u});
        WAIT_BAR(8);
        STAMP(2);
        u4v hp[4];
        unsigned ho;
        {
            f4v cs[2], bb[2];
#pragma unroll
            for (int u = 0; u < 2; ++u) { cs[u] = ldsr<f4v>(L_CB + (32 * fw + 16 * u + 4 * g) * 4); bb[u] = ldsr<f4v>(L_CB + 512 + (32 * fw + 16 * u + 4 * g) * 4); }
            const int s = 4 * cb + fw;
            ho = (unsigned)(WS_HA + ((2 * rb + tw) * 54 + s) * 4096 + lane * 16) | oob_if(s >= 54);
#pragma unroll
            for (int ti = 0; ti < 4; ++ti) {
                const f2v svt = ldsr<f2v>(L_ST + (64 * tw + 16 * ti + l16) * 8);
                const f2v rs2 = {svt[0], svt[0]}, nm2 = {-svt[1], -svt[1]};
#pragma unroll
                for (int i = 0; i < 4; ++i) {
                    const int u = i >> 1, e = 2 * (i & 1);
                    const f2v v = __builtin_elementwise_fma(rs2, f2v{acc1[u][ti][e], acc1[u][ti][e + 1]}, __builtin_elementwise_fma(nm2, f2v{cs[u][e], cs[u][e + 1]}, f2v{bb[u][e], bb[u][e + 1]}));
                    const f2v z = v * __builtin_elementwise_fma(v * v, f2v{-0.10294324f, -0.10294324f}, f2v{-2.30220820f, -2.30220820f});
                    const f2v d = f2v{__builtin_amdgcn_exp2f(z[0]), __builtin_amdgcn_exp2f(z[1])} + f2v{1.f, 1.f};
                    const f2v gl = v * f2v{__builtin_amdgcn_rcpf(d[0]), __builtin_amdgcn_rcpf(d[1])};
                    hp[ti][i] = pk2(gl[0], gl[1]);
                    if (4 * ti + i >= 16 - GL) { SB(); loadb(8 + 4 * ti + i - (16 - GL)); }
                }
                asm volatile("" : "+v"(hp[ti]));
                SB();
            }
        }
        STAMP(4);
        BAR_L();
        {
            const int typ = w < 3 ? 0 : w < 6 ? 1 : 2;
            const int c1 = w < 3 ? w : w < 6 ? w - 3 : 2 * (w - 6), c2 = w == 6 ? 1 : c1;
            const unsigned kl = L_KC + (l16 >> 2) * KC_CP + 56 - 16 * g + 8 * (lane & 3);
            const unsigned ul = L_UA + UA_PAD + 32 * l16 - 32 + 16 * g;
            unsigned ka[3], ua[3];
            ka[0] = kl + c1 * KC_CH;
            ka[1] = typ == 2 ? kl + c2 * KC_CH - 4 * 512 : ka[0];
            ka[2] = typ == 1 ? ka[0] - 6 * 512 : ka[1];
            ua[0] = ul + c1 * UA_CH - typ * 1024;
            ua[2] = ul + c2 * UA_CH;
            ua[1] = typ == 2 ? ua[2] : ua[0];
            bf8v uf[8], kf[8];
            auto rdk = [&](int sl) { const unsigned ad = ka[sl < 4 ? 0 : sl < 6 ? 1 : 2] + 512u * sl; kf[sl] = cat(trr(ad + 8u), trr(ad)); };
            auto rdu = [&](int j) { uf[j] = asbf(ldsr<u4v>(ua[j >= 4 ? 0 : j >= 2 ? 1 : 2] + 512u * j)); };
#pragma unroll
            for (int sl = 0; sl < 8; ++sl) { rdk(sl); rdu(7 - sl); }
            f4v A[3] = {}, B[5] = {};
            __builtin_amdgcn_raw_buffer_store_b128(hp[0], wsr, ho, 0, 16); __builtin_amdgcn_raw_buffer_store_b128(hp[1], wsr, ho + 1024, 0, 16);
            const unsigned fo = WS_FH + (rb * 14 + cb) * 128;
#pragma nounroll
            for (int rho = 0; rho < 7; ++rho) {
#pragma unroll
                for (int i = 0; i < 3; ++i) { ka[i] += 64u; ua[i] -= 64u; asm volatile("" : "+v"(ka[i]), "+v"(ua[i])); }
#pragma unroll
                for (int sl = 0; sl < 8; ++sl) {
                    SB();
                    const int ia = sl < 4 ? 0 : sl < 6 ? 1 : 2, ib = sl < 3 ? 0 : sl - 2;
                    A[ia] = __builtin_amdgcn_mfma_f32_16x16x32_bf16(kf[sl], uf[7 - sl], A[ia], 0, 0, 0);
                    if (sl < 7) B[ib] = __builtin_amdgcn_mfma_f32_16x16x32_bf16(kf[sl], uf[6 - sl], B[ib], 0, 0, 0);
                    SB();
                    rdk(sl); rdu(7 - sl);
                }
                SB();
                constexpr int NR = 30 - 8 - GL;
                if (rho == 0) { __builtin_amdgcn_raw_buffer_store_b128(hp[2], wsr, ho + 2048, 0, 16); __builtin_amdgcn_raw_buffer_store_b128(hp[3], wsr, ho + 3072, 0, 16); }
                if (rho == PUB_RHO) { WAIT_V(0); arrive_publish(L_CNT, ws, fo, tag); }
#pragma unroll
                for (int k = 0; k < 7 - PUB_RHO; ++k) {
                    constexpr int NL = (NR + 6 - PUB_RHO) / (7 - PUB_RHO);
                    if (rho == PUB_RHO + k) {
#pragma unroll
                        for (int n = 8 + GL + NL * k; n < 8 + GL + NL * (k + 1) && n < 30; ++n) loadb(n);
                    }
                }
            }
#pragma unroll
            for (int sl = 0; sl < 8; ++sl) {
                const int ia = sl < 4 ? 0 : sl < 6 ? 1 : 2, ib = sl < 3 ? 0 : sl - 2;
                A[ia] = __builtin_amdgcn_mfma_f32_16x16x32_bf16(kf[sl], uf[7 - sl], A[ia], 0, 0, 0);
                if (sl < 7) B[ib] = __builtin_amdgcn_mfma_f32_16x16x32_bf16(kf[sl], uf[6 - sl], B[ib], 0, 0, 0);
            }
            SB();
            f4v y[4];
            {
                const f4v z = {};
                const f4v a1 = typ < 2 ? A[1] : z, a1b = typ < 2 ? z : A[1], a2 = typ == 0 ? A[2] : z;
                const f4v b12 = B[1] + B[2], b34 = B[3] + B[4];
                y[0] = A[0] + a1 + a2;
                y[2] = A[2] + a1b;
                y[1] = B[0] + (typ < 2 ? b12 : z) + (typ == 0 ? b34 : z);
                y[3] = typ == 1 ? B[4] : B[2] + b34;
            }
            const unsigned yl = 32 * l16 + 8 * g;
#pragma unroll
            for (int i = 0; i < 4; ++i) {
                const int c = i < 2 ? c1 : c2, tau = (i < 2 ? 7 - 2 * typ : max(2 * typ - 1, 1)) - (i & 1);
                const unsigned off = oob_if(c >= nch || (i >= 2 && (typ == 0 || w == 7)));
                const u2v uv = ldsr<u2v>(yl + (L_UA + UA_PAD + c * UA_CH + 512 * tau));
                const float dk = __uint_as_float(((c == 2 ? dsk2 : dsk01) >> (c == 1 ? 16 : 0)) << 16);
                const u2v o = {pk2(y[i][0] + dk * bflo(uv[0]), y[i][1] + dk * bfhi(uv[0])), pk2(y[i][2] + dk * bflo(uv[1]), y[i][3] + dk * bfhi(uv[1]))};
                __builtin_amdgcn_raw_buffer_store_b64(o, wsr, yl, (WS_YT + (ch0 + c) * 4096 + 512 * tau) | off, 16);
            }
        }
        STAMP(5);
    } else {
        const int a = (r - 28) * 8 + xcd;
        const u4v rx = mkrsrc(x, 2048 * 1280);
        {
            unsigned vo[5];
#pragma unroll
            for (int i = 0; i < 5; ++i) {
                const int n = i * 64 + lane, rl = n / 80, cp = n - rl * 80;
                vo[i] = (a * 64 + 8 * w + rl) * 1280 + ((cp ^ (2 * w) ^ ((rl >> 1) << 3)) << 4);
                dma16(rx, vo[i], 0, L_AX + (w * 10 + i) * 1024);
            }
#pragma unroll
            for (int i = 5; i < 10; ++i) dma16(rx, vo[i - 5], 4 * 1280, L_AX + (w * 10 + i) * 1024);
        }
        const u64 e0 = ld_flag(ws, WS_E);
        if (w == 0) ldsw<unsigned>(L_ACNT + (lane & 7) * 4, 0u);
        STAMP(10);
        WAIT_V(1);
        STAMP(11);
        {
            const int row = 8 * w + (lane >> 3), sw = (2 * w) ^ (((row >> 1) & 1) << 3);
            u4v v[10];
#pragma unroll
            for (int i = 0; i < 10; ++i) v[i] = ldsr<u4v>(L_AX + row * 1280 + (((lane & 7) + 8 * i) ^ sw) * 16);
            SB();
            float s1[2] = {0.f, 0.f}, s2[2] = {0.f, 0.f};
#pragma unroll
            for (int i = 0; i < 10; ++i)
#pragma unroll
                for (int j = 0; j < 4; ++j) { s1[j & 1] = dot2(v[i][j], 0x3f803f80u, s1[j & 1]); s2[j & 1] = dot2(v[i][j], v[i][j], s2[j & 1]); }
            const f2v sv = ln_stats(sum8(s1[0] + s1[1]), sum8(s2[0] + s2[1]));
            ldsw<f2v>(L_AST + row * 8, sv);
            __builtin_amdgcn_raw_buffer_store_b64(__builtin_bit_cast(u2v, sv), wsr, (WS_STATS + (a * 64 + row) * 8) | oob_if(lane & 7), 0, 16);
        }
        u4v cv[10];
        {
            const auto rw1 = __builtin_amdgcn_make_buffer_rsrc((void*)(u64)w1, 0, 640 * 3456, 0x00020000);
#pragma unroll
            for (int i = 0; i < 10; ++i) cv[i] = __builtin_amdgcn_raw_buffer_load_b128(rw1, ((lane >> 3) * 3456 + (lane & 7) * 16) | oob_if(a >= 27), (80 * w + 8 * i) * 3456 + a * 128, 0);
        }
        BAR_L();
        STAMP(14);
        {
            const int o = lane & 7, cq = lane >> 3;
            f2v rs2[4], nm2[4];
#pragma unroll
            for (int i = 0; i < 4; ++i) { const f4v q = ldsr<f4v>(L_AST + o * 64 + i * 16); rs2[i] = f2v{q[0], q[2]}; nm2[i] = f2v{-q[1], -q[3]}; }
            u2v v[3][8];
#pragma unroll
            for (int k = 0; k < 3; ++k)
#pragma unroll
                for (int t = 0; t < 8; ++t) v[k][t] = ldsr<u2v>(L_AX + (8 * o + t) * 1280 + ((4 * min(w + 8 * k, 19) + (cq >> 1)) ^ ((2 * o) ^ (((t >> 1) & 1) << 3))) * 16 + (cq & 1) * 8);
            SB();
#pragma unroll
            for (int k = 0; k < 3; ++k) {
                if (k < 2 || w < 4) {
#pragma unroll
                    for (int e = 0; e < 4; ++e) {
                        u4v ov;
#pragma unroll
                        for (int p = 0; p < 4; ++p) {
                            const unsigned va = v[k][2 * p][e >> 1], vb = v[k][2 * p + 1][e >> 1];
                            const f2v n2 = __builtin_elementwise_fma((e & 1) ? f2v{bfhi(va), bfhi(vb)} : f2v{bflo(va), bflo(vb)}, rs2[p], nm2[p]);
                            ov[p] = pk2(n2[0], n2[1]);
                        }
                        __builtin_amdgcn_raw_buffer_store_b128(ov, wsr, WS_XT + (32 * (w + 8 * k) + 4 * cq + e) * 4096 + a * 128 + o * 16, 0, 16);
                        SB();
                    }
                }
                if (k == 0) {
                    WAIT_V(4);
                    tag = mk64(__builtin_amdgcn_readfirstlane((unsigned)e0), __builtin_amdgcn_readfirstlane((unsigned)(e0 >> 32))) + 1ull;
                    arrive_publish(L_ACNT + 8, ws, WS_FS + a * 128, tag);
                }
            }
        }
        STAMP(15);
        {
            unsigned selo = 0x00003f80u, sehi = 0x3f800000u;
            asm volatile("" : "+v"(selo), "+v"(sehi));
            float sm[8] = {0.f, 0.f, 0.f, 0.f, 0.f, 0.f, 0.f, 0.f};
#pragma unroll
            for (int i = 0; i < 10; ++i)
#pragma unroll
                for (int j = 0; j < 4; ++j) { sm[2 * j] = dot2(cv[i][j], selo, sm[2 * j]); sm[2 * j + 1] = dot2(cv[i][j], sehi, sm[2 * j + 1]); }
#pragma unroll
            for (int j = 0; j < 8; ++j) sm[j] = dpp_add<0x128>(sm[j]);
            ldsw<f4v>(L_ACS + (4 * w + g) * 256 + (lane & 7) * 32 + (lane & 8) * 2, (lane & 8) ? f4v{sm[4], sm[5], sm[6], sm[7]} : f4v{sm[0], sm[1], sm[2], sm[3]});
        }
        BAR_L();
        if (w != 7 || a >= 27) {
            WAIT_V(0);
            arrive_publish(L_ACNT + 12, ws, WS_FA + a * 128, tag);
        } else {
            const float bf = bf2f(b1[64 * a + lane]);
            float p[32], sum = 0.f;
#pragma unroll
            for (int i = 0; i < 32; ++i) p[i] = ldsr<float>(L_ACS + i * 256 + lane * 4);
#pragma unroll
            for (int i = 0; i < 32; i += 4) sum += (p[i] + p[i + 1]) + (p[i + 2] + p[i + 3]);
            __builtin_amdgcn_raw_buffer_store_b32(__builtin_bit_cast(unsigned, sum), wsr, WS_CS + (64 * a + lane) * 4, 0, 16);
            __builtin_amdgcn_raw_buffer_store_b32(__builtin_bit_cast(unsigned, bf), wsr, WS_B1 + (64 * a + lane) * 4, 0, 16);
            WAIT_V(0);
            raise_flag(ws, WS_FC + a * 128, tag);
            arrive_publish(L_ACNT + 12, ws, WS_FA + a * 128, tag);
        }
        STAMP(2);
        if (w < 2) wait_flags(ws, WS_FW + (2 * min(54 * w + lane, 107) + (cb2 >> 2)) * 128, tag);
        if (a == 31 && w == 2) {
#pragma unroll
            for (int i = 0; i < 4; ++i) wait_flags(ws, i * 64 + lane < 224 ? WS_FW + (i * 64 + lane) * 128 : WS_FS + (lane & 31) * 128, tag);
            raise_flag(ws, WS_E, tag);
        }
        BAR_L();
        STAMP(3);
        stage_x();
#pragma unroll
        for (int n = 0; n < 30; ++n) loadb(n);
        __builtin_amdgcn_s_waitcnt(0x0F70);
    }

    asm volatile("" : "+s"(rot2));
    if (w == 0) wait_flags(ws, WS_FH + ((rb2 >> 1) * 14 + min(lane, 13)) * 128, tag);
    BAR_L();
    STAMP(6);
    u4v ha[7][4];
    auto loada = [&](int i, int t) { ha[i][t] = __builtin_amdgcn_raw_buffer_load_b128(wsr, lane * 16, WS_HA + ((rb2 * 54 + sidx(i)) * 4 + t) * 1024, 16); asm volatile("" ::: "memory"); };
#pragma unroll
    for (int i = 0; i < 8; ++i) loada(i >> 2, i & 3);
    if (is_gemm) {
        WAIT_V(8);
        arrive_publish(L_CNT + 4, ws, WS_FY + bid * 128, tag);
    }
    const int c0 = 80 * cb2;
    int lz = lane;
    asm volatile("" : "+v"(lz));
    const int cA = c0 + 10 * w, cB = cA + 9;
    const int own0 = max((cA * 0xaaab) >> 17, (cA - 192) >> 1), own1 = max((cB * 0xaaab) >> 17, (cB - 192) >> 1);
    u64 ypoll = tag;
    u4v y0, y1;
    f4v acc2[4][5];
#pragma unroll
    for (int i = 0; i < 20; ++i) acc2[i / 5][i % 5] = f4v{};
#pragma unroll
    for (int i = 0; i < 5; ++i) {
#pragma unroll
        for (int t = 0; t < 4; ++t) {
            SB();
#pragma unroll
            for (int c = 0; c < 5; ++c) acc2[t][c] = __builtin_amdgcn_mfma_f32_16x16x32_bf16(asbf(ha[i][t]), asbf(bw[5 * i + c]), acc2[t][c], 0, 0, 0);
            SB();
            if (i > 0) loada(i + 2, t);
            else if (t == 3) { loada(2, 0); loada(2, 1); loada(2, 2); loada(2, 3); }
            if (i == 1 || (i == 2 && t == 0)) loadb(30 + 4 * (i - 1) + t);
            if (i == 3 && t == 0) ypoll = ld_flag(ws, WS_FY + min(own0 + lz, own1) * 128);
            if (i == 4 && t == 3) {
                if (!__all(ypoll == tag)) wait_flags(ws, WS_FY + min(own0 + lz, own1) * 128, tag);
                y0 = __builtin_amdgcn_raw_buffer_load_b128(wsr, (lz >> 3) * 4096 + (lz & 7) * 16, WS_YT + cA * 4096 + rb2 * 128, 16);
                y1 = __builtin_amdgcn_raw_buffer_load_b128(wsr, (unsigned)((8 + (lz >> 3)) * 4096 + (lz & 7) * 16) | oob_if(lz >= 16), WS_YT + cA * 4096 + rb2 * 128, 16);
            }
        }
    }
    STAMP(7);
    const unsigned da = L_RED + (lz >> 4) * 1024 + (w >> 1) * 256 + (lz & 15) * 16 + (w & 1) * 8;
    auto dump = [&](int t) {
#pragma unroll
        for (int c = 0; c < 5; ++c) ldsw<u2v>(da + (5 * t + c) * 4096, u2v{pk2(acc2[t][c][0], acc2[t][c][1]), pk2(acc2[t][c][2], acc2[t][c][3])});
    };
#pragma unroll
    for (int t = 0; t < 4; ++t) {
        SB();
#pragma unroll
        for (int c = 0; c < 5; ++c) acc2[t][c] = __builtin_amdgcn_mfma_f32_16x16x32_bf16(asbf(ha[5][t]), asbf(bw[25 + c]), acc2[t][c], 0, 0, 0);
        SB();
        if (t > 0) dump(t - 1);
        SB();
#pragma unroll
        for (int c = 0; c < 5; ++c) acc2[t][c] = __builtin_amdgcn_mfma_f32_16x16x32_bf16(asbf(ha[6][t]), asbf(bw[30 + c]), acc2[t][c], 0, 0, 0);
    }
    SB();
    ldsw<u4v>(L_YS + (10 * w + (lz >> 3)) * YS_ROW + (lz & 7) * 16, y0);
    ldsw<u4v>(L_YS + min(10 * w + 8 + (lz >> 3) + (lz >= 16 ? 80 : 0), 80) * YS_ROW + (lz & 7) * 16, y1);
    u4v sel[4], one[2];
    {
        const int n = lz & 15, gz = lz >> 4;
        const unsigned lo = (n & 2) ? 0u : 0x3f80u << (16 * (n & 1)), hi = (n & 2) ? 0x3f80u << (16 * (n & 1)) : 0u;
#pragma unroll
        for (int q = 0; q < 4; ++q) { const unsigned m = (n >> 2) == q ? ~0u : 0u; sel[q] = u4v{lo & m, hi & m, lo & m, hi & m}; }
#pragma unroll
        for (int q = 0; q < 2; ++q) {
            const unsigned m0 = gz == 2 * q + (n >> 3) && !(n & 4) ? ~0u : 0u, m1 = gz == 2 * q + (n >> 3) && (n & 4) ? ~0u : 0u;
            one[q] = u4v{lo & m0, hi & m0, lo & m1, hi & m1};
        }
    }
    dump(3);
    BAR_L();
    STAMP(8);
    {
        const int n = lz & 15, gz = lz >> 4;
#pragma unroll
        for (int k = 0; k < 3; ++k) {
            if (k < 2 || w < 4) {
                const int tl = w + 8 * k, t = (tl * 52) >> 8, c = tl - 5 * t;
                u4v p[4];
#pragma unroll
                for (int q = 0; q < 4; ++q) p[q] = ldsr<u4v>(L_RED + lz * 16 + (tl * 4096 + q * 1024));
                const u4v yf = ldsr<u4v>(L_YS + n * YS_ROW + gz * 16 + (16 * c * YS_ROW + (t >> 1) * 64));
                const u4v xf = ldsr<u4v>(L_XS + n * 160 + gz * 16 + (16 * t * 160 + (c >> 1) * 64));
                const u2v bv = ldsr<u2v>(L_XS + 10240 + gz * 8 + c * 32);
                f4v d = {bflo(bv[0]), bfhi(bv[0]), bflo(bv[1]), bfhi(bv[1])};
#pragma unroll
                for (int q = 0; q < 4; ++q) d = __builtin_amdgcn_mfma_f32_16x16x32_bf16(asbf(p[q]), asbf(sel[q]), d, 0, 0, 0);
                d = __builtin_amdgcn_mfma_f32_16x16x32_bf16(asbf(yf), asbf((t & 1) ? one[1] : one[0]), d, 0, 0, 0);
                d = __builtin_amdgcn_mfma_f32_16x16x32_bf16(asbf((c & 1) ? one[1] : one[0]), asbf(xf), d, 0, 0, 0);
                ldsw<u2v>(L_OUT + n * OUT_ROW + gz * 8 + (16 * t * OUT_ROW + c * 32), u2v{pk2(d[0], d[1]), pk2(d[2], d[3])});
            }
        }
        BAR_L();
        const auto ro = __builtin_amdgcn_make_buffer_rsrc((void*)(u64)out, 0, 2048 * 1280, 0x00020000);
#pragma unroll
        for (int k = 0; k < 2; ++k) {
            const int id = 64 * k + lz, row = (id * 205) >> 11, ch = id - 10 * row;
            const u4v v = ldsr<u4v>(L_OUT + (8 * w + min(row, 7)) * OUT_ROW + ch * 16);
            __builtin_amdgcn_raw_buffer_store_b128(v, ro, (unsigned)((rb2 * 64 + 8 * w + row) * 1280 + cb2 * 160 + ch * 16) | oob_if(id >= 80), 0, 0);
        }
    }
}

extern "C" void kernel_launch(void* const* d_in, const int* in_sizes, int n_in, void* d_out, int out_size, void* d_ws, size_t ws_size, hipStream_t stream) {
    (void)in_sizes; (void)n_in; (void)out_size; (void)ws_size;
    (void)hipFuncSetAttribute((const void*)s4ffn_fused, hipFuncAttributeMaxDynamicSharedMemorySize, LDS_BYTES);
    s4ffn_fused<<<256, 512, LDS_BYTES, stream>>>((const u16*)d_in[0], (const u16*)d_in[1], (const u16*)d_in[2], (const u16*)d_in[3], (const u16*)d_in[4],
                                                 (const u16*)d_in[5], (const u16*)d_in[6], (const u16*)d_in[7], (const u16*)d_in[8], (const u16*)d_in[10],
                                                 (const u16*)d_in[11], (const u16*)d_in[12], (const u16*)d_in[13], (u16*)d_out, (char*)d_ws);
}
```

```cpp
#include <hip/hip_runtime.h>
#include <stdint.h>
#define PUB_RHO 4
#define CSUM_ON 1
#define CODE_CHUNKS 4
#define CODE_BYTES 28672u
#define PACE 0
#define PT0 7
#define HFIRST 1
#define GL 4
#define TOUCH 1
#define ARR 0

typedef unsigned short u16;
typedef unsigned long long u64;
typedef short s4v __attribute__((ext_vector_type(4)));
typedef __bf16 bf8v __attribute__((ext_vector_type(8)));
typedef __bf16 bf2v __attribute__((ext_vector_type(2)));
typedef float f2v __attribute__((ext_vector_type(2)));
typedef float f4v __attribute__((ext_vector_type(4)));
typedef float f16v __attribute__((ext_vector_type(16)));
typedef unsigned u2v __attribute__((ext_vector_type(2)));
typedef unsigned short h2v __attribute__((ext_vector_type(2)));
typedef unsigned u4v __attribute__((ext_vector_type(4)));
typedef __attribute__((address_space(1))) u64 gu64;
#define LDSP(T) __attribute__((address_space(3))) T*
#define DI __device__ __forceinline__
#define RLX_AGENT __ATOMIC_RELAXED, __HIP_MEMORY_SCOPE_AGENT

constexpr unsigned WS_E = 0;
constexpr unsigned WS_FH = 4096;
constexpr unsigned WS_FA = 32768;
constexpr unsigned WS_FW = 81920;
constexpr unsigned WS_FS = 40960;
constexpr unsigned WS_FC = 45056;
constexpr unsigned WS_FY = 49152;
constexpr unsigned WS_STATS = 131072;
constexpr unsigned WS_CS = 196608, WS_B1 = 204800;
constexpr unsigned WS_STAMP = 262144;
constexpr unsigned WS_XT = 1u << 20;
constexpr unsigned WS_W2T = 4u << 20;
constexpr unsigned WS_YT = 8u << 20;
constexpr unsigned WS_HA = 12u << 20;
constexpr unsigned WS_BYTES = 20u << 20;
constexpr unsigned SLOT = 32768;
constexpr unsigned L_KC = 98304, KC_CH = 16640, KC_CP = 4160;
constexpr unsigned L_UA = 148224, UA_CH = 4576, UA_PAD = 480;
constexpr unsigned L_MISC = 162368, L_CB = L_MISC, L_CNT = L_MISC + 1024;
constexpr unsigned L_TBL = 65536, TBL_ROW = 544, TBL_CH = 53 * TBL_ROW;
constexpr unsigned L_PRM = 152064, L_TT = 158464, TT_CH = 1296, L_W2U = L_UA;
constexpr unsigned L_SCR = L_CB;
constexpr unsigned L_XS = 32768, L_ST = L_XS + 10752, L_RED = 49152, L_YS = L_RED + 81920, YS_ROW = 144, L_OUT = L_YS + 81 * YS_ROW + 112, OUT_ROW = 176;
constexpr unsigned L_AX = 0, L_AST = 81920, L_ACNT = L_AST + 512, L_ACS = L_AST + 1024;
static_assert(L_TT + 3 * TT_CH <= L_MISC && L_CNT + 64 <= 163840 && L_W2U + 10240 <= L_TT, "LDS map");
constexpr unsigned LDS_BYTES = 163840;
constexpr int SPIN_LIMIT = 400000;

#define STAMP(i)

DI float bf2f(unsigned v) { return __uint_as_float(v << 16); }
DI float bflo(unsigned v) { return __uint_as_float(v << 16); }
DI float bfhi(unsigned v) { return __uint_as_float(v & 0xffff0000u); }
DI unsigned pk2(float a, float b) { bf2v v; v[0] = (__bf16)a; v[1] = (__bf16)b; return __builtin_bit_cast(unsigned, v); }
DI float rbf(float a) { return bflo(pk2(a, a)); }
template <typename P> DI u4v mkrsrc(P p, unsigned bytes) { u64 a = (u64)p; u4v r; r[0] = (unsigned)a; r[1] = (unsigned)(a >> 32); r[2] = bytes; r[3] = 0x00020000u; return r; }
DI void dma16(u4v rs, unsigned voff, unsigned soff, unsigned lds) {
    asm volatile("s_mov_b32 m0, %3\n\ts_nop 0\n\tbuffer_load_dwordx4 %0, %1, %2 offen lds" ::"v"(voff), "s"(rs), "s"(soff), "s"(lds) : "memory");
}
DI void dma16g(const void* p, unsigned lds) {
    asm volatile("s_mov_b32 m0, %1\n\ts_nop 0\n\tglobal_load_lds_dwordx4 %0, off" ::"v"(p), "s"(lds) : "memory");
}
DI void dma16g_lanes3(const void* p, unsigned lds) {
    u64 ex;
    asm volatile("s_mov_b64 %0, exec\n\ts_mov_b64 exec, 7\n\ts_mov_b32 m0, %2\n\ts_nop 0\n\tglobal_load_lds_dwordx4 %1, off\n\ts_mov_b64 exec, %0" : "=&s"(ex) : "v"(p), "s"(lds) : "memory");
}
DI void dma16_lanes16(u4v rs, unsigned voff, unsigned soff, unsigned lds) {
    u64 ex;
    asm volatile("s_mov_b64 %0, exec\n\ts_mov_b64 exec, 0xffff\n\ts_mov_b32 m0, %4\n\ts_nop 0\n\tbuffer_load_dwordx4 %1, %2, %3 offen lds\n\ts_mov_b64 exec, %0"
                 : "=&s"(ex) : "v"(voff), "s"(rs), "s"(soff), "s"(lds) : "memory");
}
DI void touch_code(unsigned chunk, int lane, unsigned lds) {
    u64 pc;
    asm volatile("s_getpc_b64 %0" : "=s"(pc));
    const u64 p = (pc & ~127ull) + min(chunk * 8192u + lane * 128u, CODE_BYTES - 128u);
    asm volatile("s_mov_b32 m0, %1\n\ts_nop 0\n\tglobal_load_lds_dword %0, off" ::"v"(p), "s"(lds) : "memory");
}
DI void dma16_sc1(u4v rs, unsigned voff, unsigned soff, unsigned lds) {
    asm volatile("s_mov_b32 m0, %3\n\ts_nop 0\n\tbuffer_load_dwordx4 %0, %1, %2 offen sc1 lds" ::"v"(voff), "s"(rs), "s"(soff), "s"(lds) : "memory");
}
DI void touch(u64 p, unsigned lds) {
    asm volatile("s_mov_b32 m0, %1\n\ts_nop 0\n\tglobal_load_lds_dword %0, off" ::"v"(p), "s"(lds) : "memory");
}
#define SB() __builtin_amdgcn_sched_barrier(0)
#define WAIT_BAR(n) asm volatile("s_waitcnt vmcnt(" #n ") lgkmcnt(0)\n\ts_barrier" ::: "memory")
#define WAIT_BARN(n) asm volatile("s_waitcnt vmcnt(%0) lgkmcnt(0)\n\ts_barrier" ::"i"(n) : "memory")
#define BAR_L() asm volatile("s_waitcnt lgkmcnt(0)\n\ts_barrier" ::: "memory")
#define WAIT_V(n) asm volatile("s_waitcnt vmcnt(" #n ")" ::: "memory")
template <typename T> DI T ldsr(unsigned a) { return *(LDSP(T))a; }
template <typename T> DI void ldsw(unsigned a, T v) { *(LDSP(T))a = v; }
DI void stw(unsigned a, unsigned v, int off) { asm volatile("ds_write_b32 %0, %1 offset:%2" ::"v"(a), "v"(v), "i"(off) : "memory"); }
DI s4v trr(unsigned a) { return __builtin_amdgcn_ds_read_tr16_b64_v4i16((LDSP(s4v))a); }
DI bf8v cat(s4v a, s4v b) { return __builtin_bit_cast(bf8v, __builtin_shufflevector(a, b, 0, 1, 2, 3, 4, 5, 6, 7)); }
DI bf8v asbf(u4v a) { return __builtin_bit_cast(bf8v, a); }
template <int CTRL> DI float dpp_add(float v) { return v + __builtin_bit_cast(float, __builtin_amdgcn_update_dpp(0, __builtin_bit_cast(int, v), CTRL, 0xf, 0xf, true)); }
DI float sum8(float v) { return dpp_add<0x141>(dpp_add<0x4e>(dpp_add<0xb1>(v))); }
DI float dot2(unsigned a, unsigned b, float c) { return __builtin_amdgcn_fdot2_f32_bf16(__builtin_bit_cast(bf2v, a), __builtin_bit_cast(bf2v, b), c, false); }
DI u64 ld_flag(const char* ws, unsigned off) { return __hip_atomic_load((gu64*)(ws + off), RLX_AGENT); }
DI void wait_flags(const char* ws, unsigned off, u64 tag) {
#pragma nounroll
    for (int it = 0; it < SPIN_LIMIT; ++it) {
        if (__all(ld_flag(ws, off) == tag)) break;
        __builtin_amdgcn_s_sleep(1);
    }
    asm volatile("" ::: "memory");
}
DI unsigned lds_count(unsigned cnt_lds) {
    unsigned old, one = 1u;
    u64 ex;
    asm volatile("s_mov_b64 %1, exec\n\ts_mov_b64 exec, 1\n\ts_waitcnt lgkmcnt(0)\n\tds_add_rtn_u32 %0, %2, %3\n\ts_waitcnt lgkmcnt(0)\n\ts_mov_b64 exec, %1"
                 : "=&v"(old), "=&s"(ex) : "v"(cnt_lds), "v"(one) : "memory");
    return __builtin_amdgcn_readfirstlane(old);
}
DI void raise_flag(char* ws, unsigned flag_off, u64 tag) { __hip_atomic_store((gu64*)(ws + flag_off), tag, RLX_AGENT); }
DI void arrive_publish(unsigned cnt_lds, char* ws, unsigned flag_off, u64 tag, unsigned last = 7u) {
    if (lds_count(cnt_lds) == last) raise_flag(ws, flag_off, tag);
}
DI void lds_wait(unsigned cnt_lds, unsigned n) {
#pragma nounroll
    for (int it = 0; it < SPIN_LIMIT; ++it) {
        if (__builtin_amdgcn_readfirstlane(*(volatile LDSP(unsigned))cnt_lds) >= n) break;
        __builtin_amdgcn_s_sleep(1);
    }
    asm volatile("" ::: "memory");
}
DI unsigned oob_if(bool c) { return c ? 0x80000000u : 0u; }
DI f2v ln_stats(float s1, float s2) {
    const float mean = s1 * (1.f / 640.f), var = fmaxf(__builtin_fmaf(-mean, mean, s2 * (1.f / 640.f)), 0.f);
    const float rs = __builtin_amdgcn_rcpf(rbf(__builtin_amdgcn_sqrtf(rbf(rbf(var) + 1e-5f))));
    return f2v{rs, rbf(mean) * rs};
}
DI f2v cmul_a(f2v a, f2v b) { f2v t; asm("v_pk_mul_f32 %0, %1, %2 op_sel_hi:[0,1]" : "=v"(t) : "v"(a), "v"(b)); return t; }
DI f2v cmul_b(f2v a, f2v b, f2v t) {
    f2v o;
    asm("v_pk_fma_f32 %0, %1, %2, %3 op_sel:[1,1,0] op_sel_hi:[1,0,1] neg_lo:[0,1,0]" : "=v"(o) : "v"(a), "v"(b), "v"(t));
    return o;
}
DI f2v cmul(f2v a, f2v b) { return cmul_b(a, b, cmul_a(a, b)); }
DI f2v cexp_p(float are, float aim, float p) {
    const float m = __builtin_amdgcn_exp2f(are * p), an = __builtin_amdgcn_fractf(aim * p);
    return f2v{m * __builtin_amdgcn_cosf(an), m * __builtin_amdgcn_sinf(an)};
}

#define GP(T) __attribute__((address_space(1))) T*
typedef unsigned u8v __attribute__((ext_vector_type(8)));
DI u64 mk64(unsigned lo, unsigned hi) { return ((u64)hi << 32) | lo; }
__global__ __launch_bounds__(512, 2) void s4ffn_fused(const u16*, const u16*, const u16*, const u16*, const u16*, const u16*, const u16*, const u16*, const u16*,
                                                      const u16*, const u16*, const u16*, const u16*, u16*, char*) {
    const int tid = threadIdx.x, lane = tid & 63, w = __builtin_amdgcn_readfirstlane(tid >> 6);
    const int xcd = blockIdx.x & 7, r = ((blockIdx.x >> 3) + 28) & 31, bid = r * 8 + xcd;
    const bool is_gemm = r < 28;
    STAMP(0);
    if (w < CODE_CHUNKS && ((r + w) & 3) == 0) touch_code(w, lane, 163840 - 256);
    u64 a_x, a_prm;
    u8v a_m;
    u4v a_t;
    asm volatile("s_load_dwordx2 %0, %4, 0x0\n\ts_load_dwordx2 %1, %4, %5 offset:0x8\n\ts_load_dwordx8 %2, %4, 0x48\n\ts_load_dwordx4 %3, %4, 0x68\n\ts_waitcnt lgkmcnt(0)"
                 : "=&s"(a_x), "=&s"(a_prm), "=&s"(a_m), "=&s"(a_t) : "s"(__builtin_amdgcn_kernarg_segment_ptr()), "s"(8 * w));
    const GP(u16) x = (const GP(u16))a_x;
    const char* prm = (const char*)a_prm;
    const GP(u16) w1 = (const GP(u16))mk64(a_m[0], a_m[1]);
    const GP(u16) b1 = (const GP(u16))mk64(a_m[2], a_m[3]);
    const GP(u16) w2 = (const GP(u16))mk64(a_m[4], a_m[5]);
    const GP(u16) b2 = (const GP(u16))mk64(a_m[6], a_m[7]);
    GP(u16) out = (GP(u16))mk64(a_t[0], a_t[1]);
    char* ws = (char*)mk64(a_t[2], a_t[3]);
    u64 tag;
    const auto wsr = __builtin_amdgcn_make_buffer_rsrc((void*)ws, 0, WS_BYTES, 0x00020000);
    if (w == 7) touch(lane < 40 ? (u64)ws + lane * 524288u : (u64)out + (xcd * 4 + (r & 3)) * 81920u + (lane - 40) * 3400u, 163840 - 256);
    const int nch = bid < 192 ? 3 : 2, ch0 = 3 * bid - max(bid - 192, 0);
    const int g = lane >> 4, l16 = lane & 15;
    const int rb2 = xcd * 4 + (r & 3), cb2 = r >> 2;
    const bool has7 = w < 6;
    u4v bw[35];
    int rot2 = 7 * cb2 + 14 * (r & 3) + w;
    rot2 = rot2 >= 54 ? rot2 - 54 : rot2;
    auto sidx = [&](int i) { const int s = rot2 + 8 * i; return s >= 54 ? s - 54 : s; };
    auto loadb = [&](int n) {
        bw[n] = __builtin_amdgcn_raw_buffer_load_b128(wsr, lane * 16 | oob_if(n >= 30 && !has7), WS_W2T + ((cb2 * 54 + sidx(n / 5)) * 5 + n % 5) * 1024, 16);
        asm volatile("" ::: "memory");
    };

    auto stage_x = [&]() {
        const u4v rx = mkrsrc(x, 2048 * 1280);
        const int n1 = 64 + (lane & 15);
        dma16(rx, (lane / 10) * 1280 + (lane % 10) * 16, (rb2 * 64 + 8 * w) * 1280 + cb2 * 160, L_XS + w * 1280);
        dma16_lanes16(rx, (n1 / 10) * 1280 + (n1 % 10) * 16, (rb2 * 64 + 8 * w) * 1280 + cb2 * 160, L_XS + w * 1280 + 1024);
        if (w == 0) dma16_lanes16(mkrsrc(b2, 1280), min(lane, 9) * 16, cb2 * 160, L_XS + 10240);
    };
    if (__builtin_expect(is_gemm, 1)) {
        if (w < 6) dma16g(prm + (ch0 + min(g, nch - 1)) * 256 + l16 * 16, L_PRM + w * 1024);
        else dma16g_lanes3(lane == 2 ? (const char*)ws + WS_E : prm + min(((ch0 * 2) & ~15) + lane * 16, 1264), L_SCR + (w - 6) * 64);
        SB();
        const int gi = r & 3, gj = r >> 2, rb = (xcd >> 1) * 4 + gi, cb = (xcd & 1) * 7 + gj;
        const int fw = w & 3, tw = w >> 2;
        const u4v rx = mkrsrc(x, 2048 * 1280), rw = mkrsrc(w1, 640 * 3456);
        unsigned vx[2];
#pragma unroll
        for (int u = 0; u < 2; ++u) vx[u] = (rb * 128 + 16 * w + 8 * u + (lane >> 3)) * 1280 + (((lane & 7) ^ (4 * u + g)) << 4);
        const int wc = l16 ^ (2 * g) ^ ((w & 1) << 3);
        unsigned vw = (unsigned)((8 * w + g) * 3456 + cb * 256 + wc * 16) | (cb == 13 ? (unsigned)(wc & 8) << 28 : 0u);
        int rot = gj + 3 * gi + xcd;
        rot = rot >= 20 ? rot - 20 : rot >= 10 ? rot - 10 : rot;
        auto stage1w = [&](int t, int slot, int p, int dw) {
            int kt = t + rot; kt = kt >= 10 ? kt - 10 : kt;
            const unsigned lb = slot * SLOT + (w + dw) * 2048 + (p >> 1) * 1024;
            if (p & 1) dma16(rw, vw + dw * 8 * 3456, kt * (64 * 3456) + (p >> 1) * 4 * 3456, lb + 16384);
            else dma16(rx, vx[p >> 1] + dw * 16 * 1280, kt * 128, lb);
        };
        auto stage1 = [&](int t, int slot, int p) { stage1w(t, slot, p, 0); };
        unsigned ix[12];
        {
            const int l0 = 8 * (tid & 255) - 3;
            const float lf = (float)l0;
            const unsigned kb = L_TT + tw * TT_CH, kc = kb - 2u * (0x4300 - 128);
#pragma unroll
            for (int i = 0; i < 6; ++i) {
                const unsigned c2 = pk2(lf + (float)(2 * i), lf + (float)(2 * i + 1));
                asm("v_mad_u32_u16 %0, %1, 2, %2" : "=v"(ix[2 * i]) : "v"(c2), "s"(kc));
                asm("v_mad_u32_u16 %0, %1, 2, %2 op_sel:[1,0,0,0]" : "=v"(ix[2 * i + 1]) : "v"(c2), "s"(kc));
            }
            if (fw == 0) {
#pragma unroll
                for (int i = 0; i < 11; ++i) {
                    const int l = l0 + i;
                    const unsigned lo = kb + 2u * min((unsigned)l, 641u), m = (unsigned)((l - 128) >> 31);
                    ix[i] = (lo & m) | (ix[i] & ~m);
                }
            }
#pragma unroll
            for (int i = 0; i < 11; ++i) asm volatile("" : "+v"(ix[i]));
        }
        const int col = 16 * (w & 1) + l16;
        unsigned ta = L_TBL + (w >> 1) * TBL_CH + l16 * TBL_ROW + g * 16, te = ta + (21 + 16 * (w & 1)) * TBL_ROW;
        unsigned ta0, ta1, tb0, tb1, st1;
        {
            const unsigned tt = L_TT + (w >> 1) * TT_CH;
            ta0 = tt + ((col << (3 - max(g - 1, 0))) + ((0x30200100u >> (8 * g)) & 0xffu) * (g == 1 ? 4u : 8u)) * 2;
            ta1 = ta0 + (g < 3 ? 4 : 128);
            tb0 = tt + (g == 0 ? 512 + col : 644) * 2; st1 = g == 0 ? 64 : 0;
            tb1 = g == 1 && col < 2 ? tt + (640 + col) * 2 : tb0;
        }
        unsigned wa[2];
#pragma unroll
        for (int k = 0; k < 2; ++k) {
            const int P = 20 * (bid & 1) + 2 * min(w + 8 * k, 9) + (lane >> 5), cbb = (P * 52) >> 8;
            wa[k] = (unsigned)(WS_W2T + (bid >> 2) * 5120 + ((bid >> 1) & 1) * 512 + (cbb * 265 + P) * 1024 + (lane & 31) * 16) | oob_if(bid >= 216 || w + 8 * k > 9);
        }
        unsigned wtl = L_W2U + (4 * (g & 1) + (l16 >> 2)) * 640 + (lane >> 5) * 32 + (lane & 3) * 8 + w * 64;
        unsigned bx[2], aw[2];
#pragma unroll
        for (int i = 0; i < 2; ++i) {
            bx[i] = (64 * tw + l16) * 128 + (((4 * i + g) ^ (l16 >> 1)) << 4);
            aw[i] = 16384 + (8 * g + (l16 >> 2)) * 256 + (((2 * fw + i) ^ (l16 >> 2) ^ ((g & 1) << 2)) << 5) + (lane & 3) * 8;
        }
        unsigned f_off;
        {
            const unsigned f1a = WS_FS + 2 * rb * 128, f1b = WS_FC + 2 * cb * 128;
            const unsigned f_w = WS_FW + (2 * min(54 * w + lane, 107) + (cb2 >> 2)) * 128, f_a = WS_FA + (lane & 31) * 128;
            const unsigned f_s = min(((lane & 2) ? f1b : f1a) + (lane & 1) * 128, WS_FC + 26 * 128);
            f_off = w < 2 ? f_w : w == 2 ? f_a : f_s;
        }
        asm volatile("" : "+v"(wa[0]), "+v"(wa[1]), "+v"(wtl), "+v"(bx[0]), "+v"(bx[1]), "+v"(aw[0]), "+v"(aw[1]), "+v"(f_off));
        asm volatile("" : "+v"(ta), "+v"(te), "+v"(ta0), "+v"(ta1), "+v"(tb0), "+v"(tb1), "+v"(st1), "+v"(vx[0]), "+v"(vx[1]), "+v"(vw));
        if (w == 0) ldsw<unsigned>(L_CNT + (lane & 7) * 4, 0u);
        STAMP(10);
        WAIT_BAR(0);
        STAMP(11);
        const unsigned so = (ch0 * 2) & 15;
        { const u2v tv = ldsr<u2v>(L_SCR + 32); tag = mk64(__builtin_amdgcn_readfirstlane(tv[0]), __builtin_amdgcn_readfirstlane(tv[1])) + 1ull; }
        const unsigned dsk01 = __builtin_amdgcn_readfirstlane(ldsr<u16>(L_SCR + 64 + so) | ((unsigned)ldsr<u16>(L_SCR + 64 + so + 2) << 16));
        const unsigned dsk2 = __builtin_amdgcn_readfirstlane(ldsr<u16>(L_SCR + 64 + so + 4));
        {
            const int hc = w < 6 ? w : w - 2, c = hc >> 1, mode = (hc & 1) * 64 + lane;
            const float ldt = bf2f(ldsr<u16>(L_SCR + so + 2 * c));
            const unsigned pb = L_PRM + c * 256 + mode * 2;
            const float pa = bf2f(ldsr<u16>(pb)), pi = bf2f(ldsr<u16>(pb + 1024));
            const f2v Bc = {bf2f(ldsr<u16>(pb + 2048)), bf2f(ldsr<u16>(pb + 3072))}, Cc = {bf2f(ldsr<u16>(pb + 4096)), bf2f(ldsr<u16>(pb + 5120))};
            auto pc = [&](int i) { if (w >= 4) { stage1w(i >> 2, i >> 2, i & 3, 0); stage1w(i >> 2, i >> 2, i & 3, -4); } };
            if (c >= nch) {
#pragma unroll
                for (int i = 0; i < 8; ++i) pc(i);
            }
            if (c < nch) {
                const float dt = rbf(__builtin_amdgcn_exp2f(ldt * 1.4426950408889634f));
                const float are = dt * pa * 1.4426950408889634f, aim = dt * pi * 0.15915494309189535f;
                const unsigned tb = L_TBL + c * TBL_CH + mode * 4;
                if (w < 6) {
                    pc(0);
                    const f2v z8 = cexp_p(are, aim, 8.f), z16 = cexp_p(are, aim, 16.f), z64 = cexp_p(are, aim, 64.f);
                    pc(1);
                    f2v e[8];
                    e[0] = f2v{1.f, 0.f}; e[1] = z8; e[2] = z16; e[3] = cmul(z16, z8); e[4] = cmul(z16, z16);
                    e[5] = cmul(e[3], z16); e[6] = cmul(e[4], z16); e[7] = cmul(e[4], e[3]);
#pragma unroll
                    for (int i = 0; i < 4; ++i) {
                        f2v t[8];
                        if (i < 3) pc(2 + 2 * i);
#pragma unroll
                        for (int j = 0; j < 8; ++j) {
                            if (i < 3) t[j] = cmul_a(e[j], z64);
                            stw(tb, pk2(e[j][0], e[j][1]), (21 + 8 * i + j) * TBL_ROW);
                        }
                        if (i < 3) {
                            pc(3 + 2 * i);
#pragma unroll
                            for (int j = 0; j < 8; ++j) e[j] = cmul_b(e[j], z64, t[j]);
                        }
                    }
                }
                if (w < 4 || w >= 6) {
                    pc(0);
                    const f2v z1 = cexp_p(are, aim, 1.f), z2 = cexp_p(are, aim, 2.f), z4 = cexp_p(are, aim, 4.f);
                    pc(1);
                    const f2v z256 = cexp_p(are, aim, 256.f), z1024 = cexp_p(are, aim, 1024.f);
                    pc(2);
                    const f2v W = cmul(Cc, Bc * dt);
                    auto putx = [&](int row, f2v v) { stw(tb, pk2(v[0], -v[1]), row * TBL_ROW); };
                    const f2v x1 = cmul(W, z1), x2 = cmul(W, z2), x8 = cmul(W, z256), x16 = cmul(W, z1024);
                    pc(3);
                    const f2v x3 = cmul(x1, z2), x9 = cmul(x8, z2), x12 = cmul(x8, z256), x17 = cmul(x16, z256), x20 = cmul(x16, z1024);
                    const f2v x14 = cmul(x12, z256), x18 = cmul(x17, z256);
                    pc(4);
                    putx(0, W); putx(1, x1); putx(2, x2); putx(3, x3); putx(8, x8); putx(9, x9); putx(12, x12); putx(14, x14);
                    pc(5);
                    putx(16, x16); putx(17, x17); putx(18, x18); putx(20, x20);
                    pc(6);
                    putx(4, cmul(W, z4)); putx(5, cmul(x1, z4)); putx(6, cmul(x2, z4)); putx(7, cmul(x3, z4));
                    pc(7);
                    putx(10, cmul(x8, z4)); putx(11, cmul(x9, z4)); putx(13, cmul(x12, z4)); putx(15, cmul(x14, z4)); putx(19, cmul(x18, z256));
                }
            }
        }
        u4v w2a, w2b;
        {
            const unsigned so2 = ((bid >> 2) * 32 + ((bid >> 1) & 1) * 8 + (w >> 2) * 16 + (w & 3) * 2) * 1280 + (bid & 1) * 640;
            asm volatile("buffer_load_dwordx4 %0, %2, %4, %5 offen\n\tbuffer_load_dwordx4 %1, %3, %4, %5 offen" : "=&v"(w2a), "=&v"(w2b)
                         : "v"(lane * 16 + (lane >= 40 ? 640 : 0)), "v"((1280 + 384 + lane * 16) | oob_if(lane >= 16)), "s"(mkrsrc(w2, 1728 * 1280)), "s"(so2) : "memory");
        }
        STAMP(13);
        BAR_L();
        if (w < 2 * nch) {
            u4v fa[8], fb[8], fe[8];
#pragma unroll
            for (int s = 0; s < 8; ++s) { fe[s] = ldsr<u4v>(te + s * 64); fa[s] = ldsr<u4v>(ta + s * 64); fb[s] = ldsr<u4v>(ta + 16 * TBL_ROW + s * 64); }
            SB();
            f4v k0 = {}, k1 = {};
#pragma unroll
            for (int s = 0; s < 8; ++s) {
                k0 = __builtin_amdgcn_mfma_f32_16x16x32_bf16(asbf(fa[s]), asbf(fe[s]), k0, 0, 0, 0);
                k1 = __builtin_amdgcn_mfma_f32_16x16x32_bf16(asbf(fb[s]), asbf(fe[s]), k1, 0, 0, 0);
            }
            ldsw<unsigned>(ta0, pk2(k0[0], k0[1]));
            ldsw<unsigned>(ta1, pk2(k0[2], k0[3]));
            const unsigned q01 = pk2(k1[0], k1[1]), q23 = pk2(k1[2], k1[3]);
            ldsw<u16>(tb1, g == 1 && col == 1 ? (u16)0 : (u16)q01);
            ldsw<u16>(tb0 + st1, (u16)(q01 >> 16));
            ldsw<u16>(tb0 + 2 * st1, (u16)q23);
            ldsw<u16>(tb0 + 3 * st1, (u16)(q23 >> 16));
        }
        BAR_L();
        STAMP(14);
        stage1(2, 2, 0); stage1(2, 2, 1); stage1(2, 2, 2); stage1(2, 2, 3);
        {
            const bool two = w < 4 && nch == 3;
            const int t = tid & 255;
#pragma unroll
            for (int k = 0; k < 2; ++k) {
                if (k == 0 || two) {
                    unsigned pe[6], po[5];
#pragma unroll
                    for (int i = 0; i < 5; ++i) pe[i] = __builtin_bit_cast(unsigned, h2v{ldsr<u16>(ix[2 * i] + k * 2 * TT_CH), ldsr<u16>(ix[2 * i + 1] + k * 2 * TT_CH)});
                    pe[5] = ldsr<u16>(ix[10] + k * 2 * TT_CH);
#pragma unroll
                    for (int i = 0; i < 5; ++i) po[i] = __builtin_amdgcn_alignbit(pe[i + 1], pe[i], 16);
                    const unsigned cbase = L_KC + (tw + 2 * k) * KC_CH, d = cbase + 32 + t * 16;
                    ldsw<u4v>(d, u4v{po[1], po[2], po[3], po[4]});
                    ldsw<u4v>(d + KC_CP, u4v{pe[1], pe[2], pe[3], pe[4]});
                    ldsw<u4v>(d + 2 * KC_CP, u4v{po[0], po[1], po[2], po[3]});
                    ldsw<u4v>(d + 3 * KC_CP, u4v{pe[0], pe[1], pe[2], pe[3]});
                }
            }
            if (w == 7) ldsw<u4v>(L_KC + min(lane >> 1, 11) * KC_CP + (lane & 1) * 16, u4v{0u, 0u, 0u, 0u});
        }
        asm volatile("s_waitcnt vmcnt(4)" : "+v"(w2a), "+v"(w2b)::"memory");
        ldsw<u4v>(L_W2U + w * 1280 + lane * 16, w2a);
        ldsw<u4v>(lane < 16 ? L_W2U + w * 1280 + 1024 + lane * 16 : L_CNT + 48, w2b);
        STAMP(1);
        f4v acc1[2][4];
#pragma unroll
        for (int i = 0; i < 8; ++i) acc1[i >> 2][i & 3] = f4v{};
        bf8v fa[2][2][2], fb[2][2][4];
        auto rd = [&](int buf, int i) {
            const int ks = i >> 1, h = i & 1;
            fa[buf][ks][h] = cat(trr(aw[h] + ks * 8192), trr(aw[h] + ks * 8192 + 1024));
            fb[buf][ks][2 * h] = asbf(ldsr<u4v>(bx[ks] + h * 4096));
            fb[buf][ks][2 * h + 1] = asbf(ldsr<u4v>(bx[ks] + h * 4096 + 2048));
        };
        unsigned sb = 0;
        int kt = rot + 2; kt = kt >= 10 ? kt - 10 : kt;
        auto tile = [&](int cur, bool more, bool stage, auto&& side) {
            const unsigned d = sb == 2 * SLOT ? (unsigned)(-2 * (int)SLOT) : SLOT, lb = sb + w * 2048;
            kt = kt == 9 ? 0 : kt + 1;
            sb += d; aw[0] += d; aw[1] += d; bx[0] += d; bx[1] += d;
#pragma unroll
            for (int i = 0; i < 4; ++i) {
                SB();
                if (more) rd(cur ^ 1, i);
                SB();
#pragma unroll
                for (int j = 0; j < 4; ++j) acc1[i & 1][j] = __builtin_amdgcn_mfma_f32_16x16x32_bf16(fa[cur][i >> 1][i & 1], fb[cur][i >> 1][j], acc1[i & 1][j], 0, 0, 0);
                SB();
                if (stage) {
                    if (i & 1) dma16(rw, vw, kt * (64 * 3456) + (i >> 1) * 4 * 3456, lb + (i >> 1) * 1024 + 16384);
                    else dma16(rx, vx[i >> 1], kt * 128, lb + (i >> 1) * 1024);
                }
                side(i);
            }
            SB();
        };
        auto none = [&](int) {};
        BAR_L();
        STAMP(15);
#pragma unroll
        for (int i = 0; i < 4; ++i) rd(0, i);
        u64 fpa, fpb;
        const u4v wsq = mkrsrc(ws, WS_BYTES);
        auto poll = [&](u64& f, bool mine) { asm volatile("global_load_dwordx2 %0, %1, %2 sc1" : "=v"(f) : "v"(mine ? f_off : WS_FW), "s"(ws) : "memory"); };
        bf8v w2f[2];
        BAR_L();
        tile(0, true, true, [&](int i) {
            if (i < 2) w2f[i] = cat(trr(wtl + i * 512), trr(wtl + i * 512 + 8 * 640));
            if (i == 3) {
                __builtin_amdgcn_raw_buffer_store_b128(__builtin_bit_cast(u4v, w2f[0]), wsr, wa[0], 0, 16);
                __builtin_amdgcn_raw_buffer_store_b128(__builtin_bit_cast(u4v, w2f[1]), wsr, wa[1], 0, 16);
            }
        });
        WAIT_BAR(6); tile(1, true, true, none);
        WAIT_BAR(6); tile(0, true, true, none);
        WAIT_BAR(4);
        STAMP(12);
        tile(1, true, true, [&](int i) { if (i == 0) arrive_publish(L_CNT + 8, ws, WS_FW + bid * 128, tag); });
        WAIT_BAR(4);
        tile(0, true, true, [&](int i) { if (i == 3) poll(fpa, w == 2 || w == 3); });
        WAIT_BAR(5);
        tile(1, true, true, [&](int i) { if (i == 3) poll(fpb, w < 2); });
        asm volatile("s_waitcnt vmcnt(5)" : "+v"(fpa)::"memory");
        if ((w == 2 || w == 3) && !__all(fpa == tag)) wait_flags(ws, f_off, tag);
        BAR_L();
        tile(0, true, true, [&](int i) {
            if (i == 3) {
                dma16_sc1(wsq, lane * 16, WS_XT + (ch0 + tw) * 4096 + fw * 1024, L_UA + tw * UA_CH + UA_PAD + fw * 1024);
                if (w < 4) dma16_sc1(wsq, lane * 16, WS_XT + (ch0 + nch - 1) * 4096 + w * 1024, L_UA + 2 * UA_CH + UA_PAD + w * 1024);
                if (w == 4) dma16_sc1(wsq, (lane >> 5) * (WS_B1 - WS_CS) + (lane & 31) * 16, WS_CS + cb * 512, L_CB);
            }
        });
        asm volatile("s_waitcnt vmcnt(6)" : "+v"(fpb)::"memory");
        if (w >= 5) WAIT_V(5);
        if (w < 2 && !__all(fpb == tag)) wait_flags(ws, f_off, tag);
        BAR_L();
        STAMP(3);
        tile(1, true, false, [&](int i) {
            if (i == 0) {
                if (w == 5) dma16_sc1(wsq, lane * 16, WS_STATS + rb * 1024, L_ST);
                stage_x();
            } else loadb(i - 1);
        });
        if (w < 4) WAIT_V(7); else WAIT_V(6);
        BAR_L();
        tile(0, true, false, [&](int i) { loadb(3 + i); });
        tile(1, false, false, [&](int i) { if (i == 0) loadb(7); });
        if (w >= 5) ldsw<u4v>(L_UA + (w - 5) * UA_CH + min(lane, 29) * 16, u4v{0u, 0u, 0u, 0u});
        WAIT_BAR(8);
        STAMP(2);
        u4v hp[4];
        unsigned ho;
        {
            f4v cs[2], bb[2];
#pragma unroll
            for (int u = 0; u < 2; ++u) { cs[u] = ldsr<f4v>(L_CB + (32 * fw + 16 * u + 4 * g) * 4); bb[u] = ldsr<f4v>(L_CB + 512 + (32 * fw + 16 * u + 4 * g) * 4); }
            const int s = 4 * cb + fw;
            ho = (unsigned)(WS_HA + ((2 * rb + tw) * 54 + s) * 4096 + lane * 16) | oob_if(s >= 54);
#pragma unroll
            for (int ti = 0; ti < 4; ++ti) {
                const f2v svt = ldsr<f2v>(L_ST + (64 * tw + 16 * ti + l16) * 8);
                const f2v rs2 = {svt[0], svt[0]}, nm2 = {-svt[1], -svt[1]};
#pragma unroll
                for (int i = 0; i < 4; ++i) {
                    const int u = i >> 1, e = 2 * (i & 1);
                    const f2v v = __builtin_elementwise_fma(rs2, f2v{acc1[u][ti][e], acc1[u][ti][e + 1]}, __builtin_elementwise_fma(nm2, f2v{cs[u][e], cs[u][e + 1]}, f2v{bb[u][e], bb[u][e + 1]}));
                    const f2v z = v * __builtin_elementwise_fma(v * v, f2v{-0.10294324f, -0.10294324f}, f2v{-2.30220820f, -2.30220820f});
                    const f2v d = f2v{__builtin_amdgcn_exp2f(z[0]), __builtin_amdgcn_exp2f(z[1])} + f2v{1.f, 1.f};
                    const f2v gl = v * f2v{__builtin_amdgcn_rcpf(d[0]), __builtin_amdgcn_rcpf(d[1])};
                    hp[ti][i] = pk2(gl[0], gl[1]);
                    if (4 * ti + i >= 16 - GL) { SB(); loadb(8 + 4 * ti + i - (16 - GL)); }
                }
                asm volatile("" : "+v"(hp[ti]));
                SB();
            }
        }
        STAMP(4);
        {
            const int typ = w < 3 ? 0 : w < 6 ? 1 : 2;
            const int c1 = w < 3 ? w : w < 6 ? w - 3 : 2 * (w - 6), c2 = w == 6 ? 1 : c1;
            const unsigned kl = L_KC + (l16 >> 2) * KC_CP + 56 - 16 * g + 8 * (lane & 3);
            const unsigned ul = L_UA + UA_PAD + 32 * l16 - 32 + 16 * g;
            unsigned ka[3], ua[3];
            ka[0] = kl + c1 * KC_CH;
            ka[1] = typ == 2 ? kl + c2 * KC_CH - 4 * 512 : ka[0];
            ka[2] = typ == 1 ? ka[0] - 6 * 512 : ka[1];
            ua[0] = ul + c1 * UA_CH - typ * 1024;
            ua[2] = ul + c2 * UA_CH;
            ua[1] = typ == 2 ? ua[2] : ua[0];
            bf8v uf[8], kf[8];
            auto rdk = [&](int sl) { const unsigned ad = ka[sl < 4 ? 0 : sl < 6 ? 1 : 2] + 512u * sl; kf[sl] = cat(trr(ad + 8u), trr(ad)); };
            auto rdu = [&](int j) { uf[j] = asbf(ldsr<u4v>(ua[j >= 4 ? 0 : j >= 2 ? 1 : 2] + 512u * j)); };
#pragma unroll
            for (int sl = 0; sl < 8; ++sl) { rdk(sl); rdu(7 - sl); }
            f4v A[3] = {}, B[5] = {};
            __builtin_amdgcn_raw_buffer_store_b128(hp[0], wsr, ho, 0, 16); __builtin_amdgcn_raw_buffer_store_b128(hp[1], wsr, ho + 1024, 0, 16);
            const unsigned fo = WS_FH + (rb * 14 + cb) * 128;
#pragma nounroll
            for (int rho = 0; rho < 7; ++rho) {
#pragma unroll
                for (int i = 0; i < 3; ++i) { ka[i] += 64u; ua[i] -= 64u; asm volatile("" : "+v"(ka[i]), "+v"(ua[i])); }
#pragma unroll
                for (int sl = 0; sl < 8; ++sl) {
                    SB();
                    const int ia = sl < 4 ? 0 : sl < 6 ? 1 : 2, ib = sl < 3 ? 0 : sl - 2;
                    A[ia] = __builtin_amdgcn_mfma_f32_16x16x32_bf16(kf[sl], uf[7 - sl], A[ia], 0, 0, 0);
                    if (sl < 7) B[ib] = __builtin_amdgcn_mfma_f32_16x16x32_bf16(kf[sl], uf[6 - sl], B[ib], 0, 0, 0);
                    SB();
                    rdk(sl); rdu(7 - sl);
                }
                SB();
                constexpr int NR = 30 - 8 - GL;
                if (rho == 0) { __builtin_amdgcn_raw_buffer_store_b128(hp[2], wsr, ho + 2048, 0, 16); __builtin_amdgcn_raw_buffer_store_b128(hp[3], wsr, ho + 3072, 0, 16); }
                if (rho == PUB_RHO) { WAIT_V(0); arrive_publish(L_CNT, ws, fo, tag); }
#pragma unroll
                for (int k = 0; k < 7 - PUB_RHO; ++k) {
                    constexpr int NL = (NR + 6 - PUB_RHO) / (7 - PUB_RHO);
                    if (rho == PUB_RHO + k) {
#pragma unroll
                        for (int n = 8 + GL + NL * k; n < 8 + GL + NL * (k + 1) && n < 30; ++n) loadb(n);
                    }
                }
            }
#pragma unroll
            for (int sl = 0; sl < 8; ++sl) {
                const int ia = sl < 4 ? 0 : sl < 6 ? 1 : 2, ib = sl < 3 ? 0 : sl - 2;
                A[ia] = __builtin_amdgcn_mfma_f32_16x16x32_bf16(kf[sl], uf[7 - sl], A[ia], 0, 0, 0);
                if (sl < 7) B[ib] = __builtin_amdgcn_mfma_f32_16x16x32_bf16(kf[sl], uf[6 - sl], B[ib], 0, 0, 0);
            }
            SB();
            f4v y[4];
            {
                const f4v z = {};
                const f4v a1 = typ < 2 ? A[1] : z, a1b = typ < 2 ? z : A[1], a2 = typ == 0 ? A[2] : z;
                const f4v b12 = B[1] + B[2], b34 = B[3] + B[4];
                y[0] = A[0] + a1 + a2;
                y[2] = A[2] + a1b;
                y[1] = B[0] + (typ < 2 ? b12 : z) + (typ == 0 ? b34 : z);
                y[3] = typ == 1 ? B[4] : B[2] + b34;
            }
            const unsigned yl = 32 * l16 + 8 * g;
#pragma unroll
            for (int i = 0; i < 4; ++i) {
                const int c = i < 2 ? c1 : c2, tau = (i < 2 ? 7 - 2 * typ : max(2 * typ - 1, 1)) - (i & 1);
                const unsigned off = oob_if(c >= nch || (i >= 2 && (typ == 0 || w == 7)));
                const u2v uv = ldsr<u2v>(yl + (L_UA + UA_PAD + c * UA_CH + 512 * tau));
                const float dk = __uint_as_float(((c == 2 ? dsk2 : dsk01) >> (c == 1 ? 16 : 0)) << 16);
                const u2v o = {pk2(y[i][0] + dk * bflo(uv[0]), y[i][1] + dk * bfhi(uv[0])), pk2(y[i][2] + dk * bflo(uv[1]), y[i][3] + dk * bfhi(uv[1]))};
                __builtin_amdgcn_raw_buffer_store_b64(o, wsr, yl, (WS_YT + (ch0 + c) * 4096 + 512 * tau) | off, 16);
            }
        }
        STAMP(5);
    } else {
        const int a = (r - 28) * 8 + xcd;
        const u4v rx = mkrsrc(x, 2048 * 1280);
        {
            unsigned vo[5];
#pragma unroll
            for (int i = 0; i < 5; ++i) {
                const int n = i * 64 + lane, rl = n / 80, cp = n - rl * 80;
                vo[i] = (a * 64 + 8 * w + rl) * 1280 + ((cp ^ (2 * w) ^ ((rl >> 1) << 3)) << 4);
                dma16(rx, vo[i], 0, L_AX + (w * 10 + i) * 1024);
            }
#pragma unroll
            for (int i = 5; i < 10; ++i) dma16(rx, vo[i - 5], 4 * 1280, L_AX + (w * 10 + i) * 1024);
        }
        const u64 e0 = ld_flag(ws, WS_E);
        if (w == 0) ldsw<unsigned>(L_ACNT + (lane & 7) * 4, 0u);
        STAMP(10);
        WAIT_V(1);
        STAMP(11);
        {
            const int row = 8 * w + (lane >> 3), sw = (2 * w) ^ (((row >> 1) & 1) << 3);
            u4v v[10];
#pragma unroll
            for (int i = 0; i < 10; ++i) v[i] = ldsr<u4v>(L_AX + row * 1280 + (((lane & 7) + 8 * i) ^ sw) * 16);
            SB();
            float s1[2] = {0.f, 0.f}, s2[2] = {0.f, 0.f};
#pragma unroll
            for (int i = 0; i < 10; ++i)
#pragma unroll
                for (int j = 0; j < 4; ++j) { s1[j & 1] = dot2(v[i][j], 0x3f803f80u, s1[j & 1]); s2[j & 1] = dot2(v[i][j], v[i][j], s2[j & 1]); }
            const f2v sv = ln_stats(sum8(s1[0] + s1[1]), sum8(s2[0] + s2[1]));
            ldsw<f2v>(L_AST + row * 8, sv);
            __builtin_amdgcn_raw_buffer_store_b64(__builtin_bit_cast(u2v, sv), wsr, (WS_STATS + (a * 64 + row) * 8) | oob_if(lane & 7), 0, 16);
        }
        u4v cv[10];
        {
            const auto rw1 = __builtin_amdgcn_make_buffer_rsrc((void*)(u64)w1, 0, 640 * 3456, 0x00020000);
#pragma unroll
            for (int i = 0; i < 10; ++i) cv[i] = __builtin_amdgcn_raw_buffer_load_b128(rw1, ((lane >> 3) * 3456 + (lane & 7) * 16) | oob_if(a >= 27), (80 * w + 8 * i) * 3456 + a * 128, 0);
        }
        BAR_L();
        STAMP(14);
        {
            const int o = lane & 7, cq = lane >> 3;
            f2v rs2[4], nm2[4];
#pragma unroll
            for (int i = 0; i < 4; ++i) { const f4v q = ldsr<f4v>(L_AST + o * 64 + i * 16); rs2[i] = f2v{q[0], q[2]}; nm2[i] = f2v{-q[1], -q[3]}; }
            u2v v[3][8];
#pragma unroll
            for (int k = 0; k < 3; ++k)
#pragma unroll
                for (int t = 0; t < 8; ++t) v[k][t] = ldsr<u2v>(L_AX + (8 * o + t) * 1280 + ((4 * min(w + 8 * k, 19) + (cq >> 1)) ^ ((2 * o) ^ (((t >> 1) & 1) << 3))) * 16 + (cq & 1) * 8);
            SB();
#pragma unroll
            for (int k = 0; k < 3; ++k) {
                if (k < 2 || w < 4) {
#pragma unroll
                    for (int e = 0; e < 4; ++e) {
                        u4v ov;
#pragma unroll
                        for (int p = 0; p < 4; ++p) {
                            const unsigned va = v[k][2 * p][e >> 1], vb = v[k][2 * p + 1][e >> 1];
                            const f2v n2 = __builtin_elementwise_fma((e & 1) ? f2v{bfhi(va), bfhi(vb)} : f2v{bflo(va), bflo(vb)}, rs2[p], nm2[p]);
                            ov[p] = pk2(n2[0], n2[1]);
                        }
                        __builtin_amdgcn_raw_buffer_store_b128(ov, wsr, WS_XT + (32 * (w + 8 * k) + 4 * cq + e) * 4096 + a * 128 + o * 16, 0, 16);
                        SB();
                    }
                }
                if (k == 0) {
                    WAIT_V(4);
                    tag = mk64(__builtin_amdgcn_readfirstlane((unsigned)e0), __builtin_amdgcn_readfirstlane((unsigned)(e0 >> 32))) + 1ull;
                    arrive_publish(L_ACNT + 8, ws, WS_FS + a * 128, tag);
                }
            }
        }
        STAMP(15);
        {
            unsigned selo = 0x00003f80u, sehi = 0x3f800000u;
            asm volatile("" : "+v"(selo), "+v"(sehi));
            float sm[8] = {0.f, 0.f, 0.f, 0.f, 0.f, 0.f, 0.f, 0.f};
#pragma unroll
            for (int i = 0; i < 10; ++i)
#pragma unroll
                for (int j = 0; j < 4; ++j) { sm[2 * j] = dot2(cv[i][j], selo, sm[2 * j]); sm[2 * j + 1] = dot2(cv[i][j], sehi, sm[2 * j + 1]); }
#pragma unroll
            for (int j = 0; j < 8; ++j) sm[j] = dpp_add<0x128>(sm[j]);
            ldsw<f4v>(L_ACS + (4 * w + g) * 256 + (lane & 7) * 32 + (lane & 8) * 2, (lane & 8) ? f4v{sm[4], sm[5], sm[6], sm[7]} : f4v{sm[0], sm[1], sm[2], sm[3]});
        }
        BAR_L();
        if (w != 7 || a >= 27) {
            WAIT_V(0);
            arrive_publish(L_ACNT + 12, ws, WS_FA + a * 128, tag);
        } else {
            const float bf = bf2f(b1[64 * a + lane]);
            float p[32], sum = 0.f;
#pragma unroll
            for (int i = 0; i < 32; ++i) p[i] = ldsr<float>(L_ACS + i * 256 + lane * 4);
#pragma unroll
            for (int i = 0; i < 32; i += 4) sum += (p[i] + p[i + 1]) + (p[i + 2] + p[i + 3]);
            __builtin_amdgcn_raw_buffer_store_b32(__builtin_bit_cast(unsigned, sum), wsr, WS_CS + (64 * a + lane) * 4, 0, 16);
            __builtin_amdgcn_raw_buffer_store_b32(__builtin_bit_cast(unsigned, bf), wsr, WS_B1 + (64 * a + lane) * 4, 0, 16);
            WAIT_V(0);
            raise_flag(ws, WS_FC + a * 128, tag);
            arrive_publish(L_ACNT + 12, ws, WS_FA + a * 128, tag);
        }
        STAMP(2);
        if (w < 2) wait_flags(ws, WS_FW + (2 * min(54 * w + lane, 107) + (cb2 >> 2)) * 128, tag);
        if (a == 31 && w == 2) {
#pragma unroll
            for (int i = 0; i < 4; ++i) wait_flags(ws, i * 64 + lane < 224 ? WS_FW + (i * 64 + lane) * 128 : WS_FS + (lane & 31) * 128, tag);
            raise_flag(ws, WS_E, tag);
        }
        BAR_L();
        STAMP(3);
        stage_x();
#pragma unroll
        for (int n = 0; n < 30; ++n) loadb(n);
        __builtin_amdgcn_s_waitcnt(0x0F70);
    }

    asm volatile("" : "+s"(rot2));
    if (w == 0) wait_flags(ws, WS_FH + ((rb2 >> 1) * 14 + min(lane, 13)) * 128, tag);
    BAR_L();
    STAMP(6);
    u4v ha[7][4];
    auto loada = [&](int i, int t) { ha[i][t] = __builtin_amdgcn_raw_buffer_load_b128(wsr, lane * 16, WS_HA + ((rb2 * 54 + sidx(i)) * 4 + t) * 1024, 16); asm volatile("" ::: "memory"); };
#pragma unroll
    for (int i = 0; i < 8; ++i) loada(i >> 2, i & 3);
    if (is_gemm) {
        WAIT_V(8);
        arrive_publish(L_CNT + 4, ws, WS_FY + bid * 128, tag);
    }
    const int c0 = 80 * cb2;
    int lz = lane;
    asm volatile("" : "+v"(lz));
    const int cA = c0 + 10 * w, cB = cA + 9;
    const int own0 = max((cA * 0xaaab) >> 17, (cA - 192) >> 1), own1 = max((cB * 0xaaab) >> 17, (cB - 192) >> 1);
    u64 ypoll = tag;
    u4v y0, y1;
    f4v acc2[4][5];
#pragma unroll
    for (int i = 0; i < 20; ++i) acc2[i / 5][i % 5] = f4v{};
#pragma unroll
    for (int i = 0; i < 5; ++i) {
#pragma unroll
        for (int t = 0; t < 4; ++t) {
            SB();
#pragma unroll
            for (int c = 0; c < 5; ++c) acc2[t][c] = __builtin_amdgcn_mfma_f32_16x16x32_bf16(asbf(ha[i][t]), asbf(bw[5 * i + c]), acc2[t][c], 0, 0, 0);
            SB();
            if (i > 0) loada(i + 2, t);
            else if (t == 3) { loada(2, 0); loada(2, 1); loada(2, 2); loada(2, 3); }
            if (i == 1 || (i == 2 && t == 0)) loadb(30 + 4 * (i - 1) + t);
            if (i == 3 && t == 0) ypoll = ld_flag(ws, WS_FY + min(own0 + lz, own1) * 128);
            if (i == 4 && t == 3) {
                if (!__all(ypoll == tag)) wait_flags(ws, WS_FY + min(own0 + lz, own1) * 128, tag);
                y0 = __builtin_amdgcn_raw_buffer_load_b128(wsr, (lz >> 3) * 4096 + (lz & 7) * 16, WS_YT + cA * 4096 + rb2 * 128, 16);
                y1 = __builtin_amdgcn_raw_buffer_load_b128(wsr, (unsigned)((8 + (lz >> 3)) * 4096 + (lz & 7) * 16) | oob_if(lz >= 16), WS_YT + cA * 4096 + rb2 * 128, 16);
            }
        }
    }
    STAMP(7);
    const unsigned da = L_RED + (lz >> 4) * 1024 + (w >> 1) * 256 + (lz & 15) * 16 + (w & 1) * 8;
    auto dump = [&](int t) {
#pragma unroll
        for (int c = 0; c < 5; ++c) ldsw<u2v>(da + (5 * t + c) * 4096, u2v{pk2(acc2[t][c][0], acc2[t][c][1]), pk2(acc2[t][c][2], acc2[t][c][3])});
    };
#pragma unroll
    for (int t = 0; t < 4; ++t) {
        SB();
#pragma unroll
        for (int c = 0; c < 5; ++c) acc2[t][c] = __builtin_amdgcn_mfma_f32_16x16x32_bf16(asbf(ha[5][t]), asbf(bw[25 + c]), acc2[t][c], 0, 0, 0);
        SB();
        if (t > 0) dump(t - 1);
        SB();
#pragma unroll
        for (int c = 0; c < 5; ++c) acc2[t][c] = __builtin_amdgcn_mfma_f32_16x16x32_bf16(asbf(ha[6][t]), asbf(bw[30 + c]), acc2[t][c], 0, 0, 0);
    }
    SB();
    ldsw<u4v>(L_YS + (10 * w + (lz >> 3)) * YS_ROW + (lz & 7) * 16, y0);
    ldsw<u4v>(L_YS + min(10 * w + 8 + (lz >> 3) + (lz >= 16 ? 80 : 0), 80) * YS_ROW + (lz & 7) * 16, y1);
    u4v sel[4], one[2];
    {
        const int n = lz & 15, gz = lz >> 4;
        const unsigned lo = (n & 2) ? 0u : 0x3f80u << (16 * (n & 1)), hi = (n & 2) ? 0x3f80u << (16 * (n & 1)) : 0u;
#pragma unroll
        for (int q = 0; q < 4; ++q) { const unsigned m = (n >> 2) == q ? ~0u : 0u; sel[q] = u4v{lo & m, hi & m, lo & m, hi & m}; }
#pragma unroll
        for (int q = 0; q < 2; ++q) {
            const unsigned m0 = gz == 2 * q + (n >> 3) && !(n & 4) ? ~0u : 0u, m1 = gz == 2 * q + (n >> 3) && (n & 4) ? ~0u : 0u;
            one[q] = u4v{lo & m0, hi & m0, lo & m1, hi & m1};
        }
    }
    dump(3);
    BAR_L();
    STAMP(8);
    {
        const int n = lz & 15, gz = lz >> 4;
#pragma unroll
        for (int k = 0; k < 3; ++k) {
            if (k < 2 || w < 4) {
                const int tl = w + 8 * k, t = (tl * 52) >> 8, c = tl - 5 * t;
                u4v p[4];
#pragma unroll
                for (int q = 0; q < 4; ++q) p[q] = ldsr<u4v>(L_RED + lz * 16 + (tl * 4096 + q * 1024));
                const u4v yf = ldsr<u4v>(L_YS + n * YS_ROW + gz * 16 + (16 * c * YS_ROW + (t >> 1) * 64));
                const u4v xf = ldsr<u4v>(L_XS + n * 160 + gz * 16 + (16 * t * 160 + (c >> 1) * 64));
                const u2v bv = ldsr<u2v>(L_XS + 10240 + gz * 8 + c * 32);
                f4v d = {bflo(bv[0]), bfhi(bv[0]), bflo(bv[1]), bfhi(bv[1])};
#pragma unroll
                for (int q = 0; q < 4; ++q) d = __builtin_amdgcn_mfma_f32_16x16x32_bf16(asbf(p[q]), asbf(sel[q]), d, 0, 0, 0);
                d = __builtin_amdgcn_mfma_f32_16x16x32_bf16(asbf(yf), asbf((t & 1) ? one[1] : one[0]), d, 0, 0, 0);
                d = __builtin_amdgcn_mfma_f32_16x16x32_bf16(asbf((c & 1) ? one[1] : one[0]), asbf(xf), d, 0, 0, 0);
                ldsw<u2v>(L_OUT + n * OUT_ROW + gz * 8 + (16 * t * OUT_ROW + c * 32), u2v{pk2(d[0], d[1]), pk2(d[2], d[3])});
            }
        }
        BAR_L();
        const auto ro = __builtin_amdgcn_make_buffer_rsrc((void*)(u64)out, 0, 2048 * 1280, 0x00020000);
#pragma unroll
        for (int k = 0; k < 2; ++k) {
            const int id = 64 * k + lz, row = (id * 205) >> 11, ch = id - 10 * row;
            const u4v v = ldsr<u4v>(L_OUT + (8 * w + min(row, 7)) * OUT_ROW + ch * 16);
            __builtin_amdgcn_raw_buffer_store_b128(v, ro, (unsigned)((rb2 * 64 + 8 * w + row) * 1280 + cb2 * 160 + ch * 16) | oob_if(id >= 80), 0, 0);
        }
    }
}

extern "C" void kernel_launch(void* const* d_in, const int* in_sizes, int n_in, void* d_out, int out_size, void* d_ws, size_t ws_size, hipStream_t stream) {
    (void)in_sizes; (void)n_in; (void)out_size; (void)ws_size;
    (void)hipFuncSetAttribute((const void*)s4ffn_fused, hipFuncAttributeMaxDynamicSharedMemorySize, LDS_BYTES);
    s4ffn_fused<<<256, 512, LDS_BYTES, stream>>>((const u16*)d_in[0], (const u16*)d_in[1], (const u16*)d_in[2], (const u16*)d_in[3], (const u16*)d_in[4],
                                                 (const u16*)d_in[5], (const u16*)d_in[6], (const u16*)d_in[7], (const u16*)d_in[8], (const u16*)d_in[10],
                                                 (const u16*)d_in[11], (const u16*)d_in[12], (const u16*)d_in[13], (u16*)d_out, (char*)d_ws);
}
```

```cpp
#define STAMPS
#include <hip/hip_runtime.h>
#include <stdint.h>
#define PUB_RHO 4
#define CSUM_ON 1
#define CODE_CHUNKS 4
#define CODE_BYTES 28672u
#define PACE 0
#define PT0 7
#define HFIRST 1
#define GL 4
#define ARR 0

typedef unsigned short u16;
typedef unsigned long long u64;
typedef short s4v __attribute__((ext_vector_type(4)));
typedef __bf16 bf8v __attribute__((ext_vector_type(8)));
typedef __bf16 bf2v __attribute__((ext_vector_type(2)));
typedef float f2v __attribute__((ext_vector_type(2)));
typedef float f4v __attribute__((ext_vector_type(4)));
typedef float f16v __attribute__((ext_vector_type(16)));
typedef unsigned u2v __attribute__((ext_vector_type(2)));
typedef unsigned short h2v __attribute__((ext_vector_type(2)));
typedef unsigned u4v __attribute__((ext_vector_type(4)));
typedef __attribute__((address_space(1))) u64 gu64;
#define LDSP(T) __attribute__((address_space(3))) T*
#define DI __device__ __forceinline__
#define RLX_AGENT __ATOMIC_RELAXED, __HIP_MEMORY_SCOPE_AGENT

constexpr unsigned WS_E = 0;
constexpr unsigned WS_FH = 4096;
constexpr unsigned WS_FA = 32768;
constexpr unsigned WS_FW = 81920;
constexpr unsigned WS_FS = 40960;
constexpr unsigned WS_FC = 45056;
constexpr unsigned WS_FY = 49152;
constexpr unsigned WS_STATS = 131072;
constexpr unsigned WS_CS = 196608, WS_B1 = 204800;
constexpr unsigned WS_STAMP = 262144;
constexpr unsigned WS_XT = 1u << 20;
constexpr unsigned WS_W2T = 4u << 20;
constexpr unsigned WS_YT = 8u << 20;
constexpr unsigned WS_HA = 12u << 20;
constexpr unsigned WS_BYTES = 20u << 20;
constexpr unsigned SLOT = 32768;
constexpr unsigned L_KC = 98304, KC_CH = 16640, KC_CP = 4160;
constexpr unsigned L_UA = 148224, UA_CH = 4576, UA_PAD = 480;
constexpr unsigned L_MISC = 162368, L_CB = L_MISC, L_CNT = L_MISC + 1024;
constexpr unsigned L_TBL = 65536, TBL_ROW = 544, TBL_CH = 53 * TBL_ROW;
constexpr unsigned L_PRM = 152064, L_TT = 158464, TT_CH = 1296, L_W2U = L_UA;
constexpr unsigned L_SCR = L_CB;
constexpr unsigned L_XS = 32768, L_ST = L_XS + 10752, L_RED = 49152, L_YS = L_RED + 81920, YS_ROW = 144, L_OUT = L_YS + 81 * YS_ROW + 112, OUT_ROW = 176;
constexpr unsigned L_AX = 0, L_AST = 81920, L_ACNT = L_AST + 512, L_ACS = L_AST + 1024;
static_assert(L_TT + 3 * TT_CH <= L_MISC && L_CNT + 64 <= 163840 && L_W2U + 10240 <= L_TT, "LDS map");
constexpr unsigned LDS_BYTES = 163840;
constexpr int SPIN_LIMIT = 400000;

constexpr int NSTAMP = 16;
#define STAMP(i) do { st[i] = __builtin_amdgcn_s_memrealtime(); } while (0)

DI float bf2f(unsigned v) { return __uint_as_float(v << 16); }
DI float bflo(unsigned v) { return __uint_as_float(v << 16); }
DI float bfhi(unsigned v) { return __uint_as_float(v & 0xffff0000u); }
DI unsigned pk2(float a, float b) { bf2v v; v[0] = (__bf16)a; v[1] = (__bf16)b; return __builtin_bit_cast(unsigned, v); }
DI float rbf(float a) { return bflo(pk2(a, a)); }
template <typename P> DI u4v mkrsrc(P p, unsigned bytes) { u64 a = (u64)p; u4v r; r[0] = (unsigned)a; r[1] = (unsigned)(a >> 32); r[2] = bytes; r[3] = 0x00020000u; return r; }
DI void dma16(u4v rs, unsigned voff, unsigned soff, unsigned lds) {
    asm volatile("s_mov_b32 m0, %3\n\ts_nop 0\n\tbuffer_load_dwordx4 %0, %1, %2 offen lds" ::"v"(voff), "s"(rs), "s"(soff), "s"(lds) : "memory");
}
DI void dma16g(const void* p, unsigned lds) {
    asm volatile("s_mov_b32 m0, %1\n\ts_nop 0\n\tglobal_load_lds_dwordx4 %0, off" ::"v"(p), "s"(lds) : "memory");
}
DI void dma16g_lanes3(const void* p, unsigned lds) {
    u64 ex;
    asm volatile("s_mov_b64 %0, exec\n\ts_mov_b64 exec, 7\n\ts_mov_b32 m0, %2\n\ts_nop 0\n\tglobal_load_lds_dwordx4 %1, off\n\ts_mov_b64 exec, %0" : "=&s"(ex) : "v"(p), "s"(lds) : "memory");
}
DI void dma16_lanes16(u4v rs, unsigned voff, unsigned soff, unsigned lds) {
    u64 ex;
    asm volatile("s_mov_b64 %0, exec\n\ts_mov_b64 exec, 0xffff\n\ts_mov_b32 m0, %4\n\ts_nop 0\n\tbuffer_load_dwordx4 %1, %2, %3 offen lds\n\ts_mov_b64 exec, %0"
                 : "=&s"(ex) : "v"(voff), "s"(rs), "s"(soff), "s"(lds) : "memory");
}
DI void touch_code(unsigned chunk, int lane, unsigned lds) {
    u64 pc;
    asm volatile("s_getpc_b64 %0" : "=s"(pc));
    const u64 p = (pc & ~127ull) + min(chunk * 8192u + lane * 128u, CODE_BYTES - 128u);
    asm volatile("s_mov_b32 m0, %1\n\ts_nop 0\n\tglobal_load_lds_dword %0, off" ::"v"(p), "s"(lds) : "memory");
}
DI void dma16_sc1(u4v rs, unsigned voff, unsigned soff, unsigned lds) {
    asm volatile("s_mov_b32 m0, %3\n\ts_nop 0\n\tbuffer_load_dwordx4 %0, %1, %2 offen sc1 lds" ::"v"(voff), "s"(rs), "s"(soff), "s"(lds) : "memory");
}
#define SB() __builtin_amdgcn_sched_barrier(0)
#define WAIT_BAR(n) asm volatile("s_waitcnt vmcnt(" #n ") lgkmcnt(0)\n\ts_barrier" ::: "memory")
#define WAIT_BARN(n) asm volatile("s_waitcnt vmcnt(%0) lgkmcnt(0)\n\ts_barrier" ::"i"(n) : "memory")
#define BAR_L() asm volatile("s_waitcnt lgkmcnt(0)\n\ts_barrier" ::: "memory")
#define WAIT_V(n) asm volatile("s_waitcnt vmcnt(" #n ")" ::: "memory")
template <typename T> DI T ldsr(unsigned a) { return *(LDSP(T))a; }
template <typename T> DI void ldsw(unsigned a, T v) { *(LDSP(T))a = v; }
DI void stw(unsigned a, unsigned v, int off) { asm volatile("ds_write_b32 %0, %1 offset:%2" ::"v"(a), "v"(v), "i"(off) : "memory"); }
DI s4v trr(unsigned a) { return __builtin_amdgcn_ds_read_tr16_b64_v4i16((LDSP(s4v))a); }
DI bf8v cat(s4v a, s4v b) { return __builtin_bit_cast(bf8v, __builtin_shufflevector(a, b, 0, 1, 2, 3, 4, 5, 6, 7)); }
DI bf8v asbf(u4v a) { return __builtin_bit_cast(bf8v, a); }
template <int CTRL> DI float dpp_add(float v) { return v + __builtin_bit_cast(float, __builtin_amdgcn_update_dpp(0, __builtin_bit_cast(int, v), CTRL, 0xf, 0xf, true)); }
DI float sum8(float v) { return dpp_add<0x141>(dpp_add<0x4e>(dpp_add<0xb1>(v))); }
DI float dot2(unsigned a, unsigned b, float c) { return __builtin_amdgcn_fdot2_f32_bf16(__builtin_bit_cast(bf2v, a), __builtin_bit_cast(bf2v, b), c, false); }
DI u64 ld_flag(const char* ws, unsigned off) { return __hip_atomic_load((gu64*)(ws + off), RLX_AGENT); }
DI void wait_flags(const char* ws, unsigned off, u64 tag) {
#pragma nounroll
    for (int it = 0; it < SPIN_LIMIT; ++it) {
        if (__all(ld_flag(ws, off) == tag)) break;
        __builtin_amdgcn_s_sleep(1);
    }
    asm volatile("" ::: "memory");
}
DI unsigned lds_count(unsigned cnt_lds) {
    unsigned old, one = 1u;
    u64 ex;
    asm volatile("s_mov_b64 %1, exec\n\ts_mov_b64 exec, 1\n\ts_waitcnt lgkmcnt(0)\n\tds_add_rtn_u32 %0, %2, %3\n\ts_waitcnt lgkmcnt(0)\n\ts_mov_b64 exec, %1"
                 : "=&v"(old), "=&s"(ex) : "v"(cnt_lds), "v"(one) : "memory");
    return __builtin_amdgcn_readfirstlane(old);
}
DI void raise_flag(char* ws, unsigned flag_off, u64 tag) { __hip_atomic_store((gu64*)(ws + flag_off), tag, RLX_AGENT); }
DI void arrive_publish(unsigned cnt_lds, char* ws, unsigned flag_off, u64 tag, unsigned last = 7u) {
    if (lds_count(cnt_lds) == last) raise_flag(ws, flag_off, tag);
}
DI void lds_wait(unsigned cnt_lds, unsigned n) {
#pragma nounroll
    for (int it = 0; it < SPIN_LIMIT; ++it) {
        if (__builtin_amdgcn_readfirstlane(*(volatile LDSP(unsigned))cnt_lds) >= n) break;
        __builtin_amdgcn_s_sleep(1);
    }
    asm volatile("" ::: "memory");
}
DI unsigned oob_if(bool c) { return c ? 0x80000000u : 0u; }
DI f2v ln_stats(float s1, float s2) {
    const float mean = s1 * (1.f / 640.f), var = fmaxf(__builtin_fmaf(-mean, mean, s2 * (1.f / 640.f)), 0.f);
    const float rs = __builtin_amdgcn_rcpf(rbf(__builtin_amdgcn_sqrtf(rbf(rbf(var) + 1e-5f))));
    return f2v{rs, rbf(mean) * rs};
}
DI f2v cmul_a(f2v a, f2v b) { f2v t; asm("v_pk_mul_f32 %0, %1, %2 op_sel_hi:[0,1]" : "=v"(t) : "v"(a), "v"(b)); return t; }
DI f2v cmul_b(f2v a, f2v b, f2v t) {
    f2v o;
    asm("v_pk_fma_f32 %0, %1, %2, %3 op_sel:[1,1,0] op_sel_hi:[1,0,1] neg_lo:[0,1,0]" : "=v"(o) : "v"(a), "v"(b), "v"(t));
    return o;
}
DI f2v cmul(f2v a, f2v b) { return cmul_b(a, b, cmul_a(a, b)); }
DI f2v cexp_p(float are, float aim, float p) {
    const float m = __builtin_amdgcn_exp2f(are * p), an = __builtin_amdgcn_fractf(aim * p);
    return f2v{m * __builtin_amdgcn_cosf(an), m * __builtin_amdgcn_sinf(an)};
}

#define GP(T) __attribute__((address_space(1))) T*
typedef unsigned u8v __attribute__((ext_vector_type(8)));
DI u64 mk64(unsigned lo, unsigned hi) { return ((u64)hi << 32) | lo; }
__global__ __launch_bounds__(512, 2) void s4ffn_fused(const u16*, const u16*, const u16*, const u16*, const u16*, const u16*, const u16*, const u16*, const u16*,
                                                      const u16*, const u16*, const u16*, const u16*, u16*, char*) {
    const int tid = threadIdx.x, lane = tid & 63, w = __builtin_amdgcn_readfirstlane(tid >> 6);
    const int xcd = blockIdx.x & 7, r = ((blockIdx.x >> 3) + 28) & 31, bid = r * 8 + xcd;
    const bool is_gemm = r < 28;
    u64 st[NSTAMP];
    for (int i = 0; i < NSTAMP; ++i) st[i] = 0;
    u64 cyc0 = __builtin_amdgcn_s_memtime();
    STAMP(0);
    if (w < CODE_CHUNKS && ((r + w) & 3) == 0) touch_code(w, lane, 163840 - 256);
    u64 a_x, a_prm;
    u8v a_m;
    u4v a_t;
    asm volatile("s_load_dwordx2 %0, %4, 0x0\n\ts_load_dwordx2 %1, %4, %5 offset:0x8\n\ts_load_dwordx8 %2, %4, 0x48\n\ts_load_dwordx4 %3, %4, 0x68\n\ts_waitcnt lgkmcnt(0)"
                 : "=&s"(a_x), "=&s"(a_prm), "=&s"(a_m), "=&s"(a_t) : "s"(__builtin_amdgcn_kernarg_segment_ptr()), "s"(8 * w));
    const GP(u16) x = (const GP(u16))a_x;
    const char* prm = (const char*)a_prm;
    const GP(u16) w1 = (const GP(u16))mk64(a_m[0], a_m[1]);
    const GP(u16) b1 = (const GP(u16))mk64(a_m[2], a_m[3]);
    const GP(u16) w2 = (const GP(u16))mk64(a_m[4], a_m[5]);
    const GP(u16) b2 = (const GP(u16))mk64(a_m[6], a_m[7]);
    GP(u16) out = (GP(u16))mk64(a_t[0], a_t[1]);
    char* ws = (char*)mk64(a_t[2], a_t[3]);
    u64 tag;
    const auto wsr = __builtin_amdgcn_make_buffer_rsrc((void*)ws, 0, WS_BYTES, 0x00020000);
    const int nch = bid < 192 ? 3 : 2, ch0 = 3 * bid - max(bid - 192, 0);
    const int g = lane >> 4, l16 = lane & 15;
    const int rb2 = xcd * 4 + (r & 3), cb2 = r >> 2;
    const bool has7 = w < 6;
    u4v bw[35];
    int rot2 = 7 * cb2 + 14 * (r & 3) + w;
    rot2 = rot2 >= 54 ? rot2 - 54 : rot2;
    auto sidx = [&](int i) { const int s = rot2 + 8 * i; return s >= 54 ? s - 54 : s; };
    auto loadb = [&](int n) {
        bw[n] = __builtin_amdgcn_raw_buffer_load_b128(wsr, lane * 16 | oob_if(n >= 30 && !has7), WS_W2T + ((cb2 * 54 + sidx(n / 5)) * 5 + n % 5) * 1024, 16);
        asm volatile("" ::: "memory");
    };

    auto stage_x = [&]() {
        const u4v rx = mkrsrc(x, 2048 * 1280);
        const int n1 = 64 + (lane & 15);
        dma16(rx, (lane / 10) * 1280 + (lane % 10) * 16, (rb2 * 64 + 8 * w) * 1280 + cb2 * 160, L_XS + w * 1280);
        dma16_lanes16(rx, (n1 / 10) * 1280 + (n1 % 10) * 16, (rb2 * 64 + 8 * w) * 1280 + cb2 * 160, L_XS + w * 1280 + 1024);
        if (w == 0) dma16_lanes16(mkrsrc(b2, 1280), min(lane, 9) * 16, cb2 * 160, L_XS + 10240);
    };
    if (__builtin_expect(is_gemm, 1)) {
        if (w < 6) dma16g(prm + (ch0 + min(g, nch - 1)) * 256 + l16 * 16, L_PRM + w * 1024);
        else dma16g_lanes3(lane == 2 ? (const char*)ws + WS_E : prm + min(((ch0 * 2) & ~15) + lane * 16, 1264), L_SCR + (w - 6) * 64);
        SB();
        const int gi = r & 3, gj = r >> 2, rb = (xcd >> 1) * 4 + gi, cb = (xcd & 1) * 7 + gj;
        const int fw = w & 3, tw = w >> 2;
        const u4v rx = mkrsrc(x, 2048 * 1280), rw = mkrsrc(w1, 640 * 3456);
        unsigned vx[2];
#pragma unroll
        for (int u = 0; u < 2; ++u) vx[u] = (rb * 128 + 16 * w + 8 * u + (lane >> 3)) * 1280 + (((lane & 7) ^ (4 * u + g)) << 4);
        const int wc = l16 ^ (2 * g) ^ ((w & 1) << 3);
        unsigned vw = (unsigned)((8 * w + g) * 3456 + cb * 256 + wc * 16) | (cb == 13 ? (unsigned)(wc & 8) << 28 : 0u);
        int rot = gj + 3 * gi + xcd;
        rot = rot >= 20 ? rot - 20 : rot >= 10 ? rot - 10 : rot;
        auto stage1w = [&](int t, int slot, int p, int dw) {
            int kt = t + rot; kt = kt >= 10 ? kt - 10 : kt;
            const unsigned lb = slot * SLOT + (w + dw) * 2048 + (p >> 1) * 1024;
            if (p & 1) dma16(rw, vw + dw * 8 * 3456, kt * (64 * 3456) + (p >> 1) * 4 * 3456, lb + 16384);
            else dma16(rx, vx[p >> 1] + dw * 16 * 1280, kt * 128, lb);
        };
        auto stage1 = [&](int t, int slot, int p) { stage1w(t, slot, p, 0); };
        unsigned ix[12];
        {
            const int l0 = 8 * (tid & 255) - 3;
            const float lf = (float)l0;
            const unsigned kb = L_TT + tw * TT_CH, kc = kb - 2u * (0x4300 - 128);
#pragma unroll
            for (int i = 0; i < 6; ++i) {
                const unsigned c2 = pk2(lf + (float)(2 * i), lf + (float)(2 * i + 1));
                asm("v_mad_u32_u16 %0, %1, 2, %2" : "=v"(ix[2 * i]) : "v"(c2), "s"(kc));
                asm("v_mad_u32_u16 %0, %1, 2, %2 op_sel:[1,0,0,0]" : "=v"(ix[2 * i + 1]) : "v"(c2), "s"(kc));
            }
            if (fw == 0) {
#pragma unroll
                for (int i = 0; i < 11; ++i) {
                    const int l = l0 + i;
                    const unsigned lo = kb + 2u * min((unsigned)l, 641u), m = (unsigned)((l - 128) >> 31);
                    ix[i] = (lo & m) | (ix[i] & ~m);
                }
            }
#pragma unroll
            for (int i = 0; i < 11; ++i) asm volatile("" : "+v"(ix[i]));
        }
        const int col = 16 * (w & 1) + l16;
        unsigned ta = L_TBL + (w >> 1) * TBL_CH + l16 * TBL_ROW + g * 16, te = ta + (21 + 16 * (w & 1)) * TBL_ROW;
        unsigned ta0, ta1, tb0, tb1, st1;
        {
            const unsigned tt = L_TT + (w >> 1) * TT_CH;
            ta0 = tt + ((col << (3 - max(g - 1, 0))) + ((0x30200100u >> (8 * g)) & 0xffu) * (g == 1 ? 4u : 8u)) * 2;
            ta1 = ta0 + (g < 3 ? 4 : 128);
            tb0 = tt + (g == 0 ? 512 + col : 644) * 2; st1 = g == 0 ? 64 : 0;
            tb1 = g == 1 && col < 2 ? tt + (640 + col) * 2 : tb0;
        }
        unsigned wa[2];
#pragma unroll
        for (int k = 0; k < 2; ++k) {
            const int P = 20 * (bid & 1) + 2 * min(w + 8 * k, 9) + (lane >> 5), cbb = (P * 52) >> 8;
            wa[k] = (unsigned)(WS_W2T + (bid >> 2) * 5120 + ((bid >> 1) & 1) * 512 + (cbb * 265 + P) * 1024 + (lane & 31) * 16) | oob_if(bid >= 216 || w + 8 * k > 9);
        }
        unsigned wtl = L_W2U + (4 * (g & 1) + (l16 >> 2)) * 640 + (lane >> 5) * 32 + (lane & 3) * 8 + w * 64;
        unsigned bx[2], aw[2];
#pragma unroll
        for (int i = 0; i < 2; ++i) {
            bx[i] = (64 * tw + l16) * 128 + (((4 * i + g) ^ (l16 >> 1)) << 4);
            aw[i] = 16384 + (8 * g + (l16 >> 2)) * 256 + (((2 * fw + i) ^ (l16 >> 2) ^ ((g & 1) << 2)) << 5) + (lane & 3) * 8;
        }
        unsigned f_off;
        {
            const unsigned f1a = WS_FS + 2 * rb * 128, f1b = WS_FC + 2 * cb * 128;
            const unsigned f_w = WS_FW + (2 * min(54 * w + lane, 107) + (cb2 >> 2)) * 128, f_a = WS_FA + (lane & 31) * 128;
            const unsigned f_s = min(((lane & 2) ? f1b : f1a) + (lane & 1) * 128, WS_FC + 26 * 128);
            f_off = w < 2 ? f_w : w == 2 ? f_a : f_s;
        }
        asm volatile("" : "+v"(wa[0]), "+v"(wa[1]), "+v"(wtl), "+v"(bx[0]), "+v"(bx[1]), "+v"(aw[0]), "+v"(aw[1]), "+v"(f_off));
        asm volatile("" : "+v"(ta), "+v"(te), "+v"(ta0), "+v"(ta1), "+v"(tb0), "+v"(tb1), "+v"(st1), "+v"(vx[0]), "+v"(vx[1]), "+v"(vw));
        if (w == 0) ldsw<unsigned>(L_CNT + (lane & 7) * 4, 0u);
        STAMP(10);
        WAIT_BAR(0);
        STAMP(11);
        const unsigned so = (ch0 * 2) & 15;
        { const u2v tv = ldsr<u2v>(L_SCR + 32); tag = mk64(__builtin_amdgcn_readfirstlane(tv[0]), __builtin_amdgcn_readfirstlane(tv[1])) + 1ull; }
        const unsigned dsk01 = __builtin_amdgcn_readfirstlane(ldsr<u16>(L_SCR + 64 + so) | ((unsigned)ldsr<u16>(L_SCR + 64 + so + 2) << 16));
        const unsigned dsk2 = __builtin_amdgcn_readfirstlane(ldsr<u16>(L_SCR + 64 + so + 4));
        {
            const int hc = w < 6 ? w : w - 2, c = hc >> 1, mode = (hc & 1) * 64 + lane;
            const float ldt = bf2f(ldsr<u16>(L_SCR + so + 2 * c));
            const unsigned pb = L_PRM + c * 256 + mode * 2;
            const float pa = bf2f(ldsr<u16>(pb)), pi = bf2f(ldsr<u16>(pb + 1024));
            const f2v Bc = {bf2f(ldsr<u16>(pb + 2048)), bf2f(ldsr<u16>(pb + 3072))}, Cc = {bf2f(ldsr<u16>(pb + 4096)), bf2f(ldsr<u16>(pb + 5120))};
            auto pc = [&](int i) { if (w >= 4) { stage1w(i >> 2, i >> 2, i & 3, 0); stage1w(i >> 2, i >> 2, i & 3, -4); } };
            if (c >= nch) {
#pragma unroll
                for (int i = 0; i < 8; ++i) pc(i);
            }
            if (c < nch) {
                const float dt = rbf(__builtin_amdgcn_exp2f(ldt * 1.4426950408889634f));
                const float are = dt * pa * 1.4426950408889634f, aim = dt * pi * 0.15915494309189535f;
                const unsigned tb = L_TBL + c * TBL_CH + mode * 4;
                if (w < 6) {
                    pc(0);
                    const f2v z8 = cexp_p(are, aim, 8.f), z16 = cexp_p(are, aim, 16.f), z64 = cexp_p(are, aim, 64.f);
                    pc(1);
                    f2v e[8];
                    e[0] = f2v{1.f, 0.f}; e[1] = z8; e[2] = z16; e[3] = cmul(z16, z8); e[4] = cmul(z16, z16);
                    e[5] = cmul(e[3], z16); e[6] = cmul(e[4], z16); e[7] = cmul(e[4], e[3]);
#pragma unroll
                    for (int i = 0; i < 4; ++i) {
                        f2v t[8];
                        if (i < 3) pc(2 + 2 * i);
#pragma unroll
                        for (int j = 0; j < 8; ++j) {
                            if (i < 3) t[j] = cmul_a(e[j], z64);
                            stw(tb, pk2(e[j][0], e[j][1]), (21 + 8 * i + j) * TBL_ROW);
                        }
                        if (i < 3) {
                            pc(3 + 2 * i);
#pragma unroll
                            for (int j = 0; j < 8; ++j) e[j] = cmul_b(e[j], z64, t[j]);
                        }
                    }
                }
                if (w < 4 || w >= 6) {
                    pc(0);
                    const f2v z1 = cexp_p(are, aim, 1.f), z2 = cexp_p(are, aim, 2.f), z4 = cexp_p(are, aim, 4.f);
                    pc(1);
                    const f2v z256 = cexp_p(are, aim, 256.f), z1024 = cexp_p(are, aim, 1024.f);
                    pc(2);
                    const f2v W = cmul(Cc, Bc * dt);
                    auto putx = [&](int row, f2v v) { stw(tb, pk2(v[0], -v[1]), row * TBL_ROW); };
                    const f2v x1 = cmul(W, z1), x2 = cmul(W, z2), x8 = cmul(W, z256), x16 = cmul(W, z1024);
                    pc(3);
                    const f2v x3 = cmul(x1, z2), x9 = cmul(x8, z2), x12 = cmul(x8, z256), x17 = cmul(x16, z256), x20 = cmul(x16, z1024);
                    const f2v x14 = cmul(x12, z256), x18 = cmul(x17, z256);
                    pc(4);
                    putx(0, W); putx(1, x1); putx(2, x2); putx(3, x3); putx(8, x8); putx(9, x9); putx(12, x12); putx(14, x14);
                    pc(5);
                    putx(16, x16); putx(17, x17); putx(18, x18); putx(20, x20);
                    pc(6);
                    putx(4, cmul(W, z4)); putx(5, cmul(x1, z4)); putx(6, cmul(x2, z4)); putx(7, cmul(x3, z4));
                    pc(7);
                    putx(10, cmul(x8, z4)); putx(11, cmul(x9, z4)); putx(13, cmul(x12, z4)); putx(15, cmul(x14, z4)); putx(19, cmul(x18, z256));
                }
            }
        }
        u4v w2a, w2b;
        {
            const unsigned so2 = ((bid >> 2) * 32 + ((bid >> 1) & 1) * 8 + (w >> 2) * 16 + (w & 3) * 2) * 1280 + (bid & 1) * 640;
            asm volatile("buffer_load_dwordx4 %0, %2, %4, %5 offen\n\tbuffer_load_dwordx4 %1, %3, %4, %5 offen" : "=&v"(w2a), "=&v"(w2b)
                         : "v"(lane * 16 + (lane >= 40 ? 640 : 0)), "v"((1280 + 384 + lane * 16) | oob_if(lane >= 16)), "s"(mkrsrc(w2, 1728 * 1280)), "s"(so2) : "memory");
        }
        STAMP(13);
        BAR_L();
        if (w < 2 * nch) {
            u4v fa[8], fb[8], fe[8];
#pragma unroll
            for (int s = 0; s < 8; ++s) { fe[s] = ldsr<u4v>(te + s * 64); fa[s] = ldsr<u4v>(ta + s * 64); fb[s] = ldsr<u4v>(ta + 16 * TBL_ROW + s * 64); }
            SB();
            f4v k0 = {}, k1 = {};
#pragma unroll
            for (int s = 0; s < 8; ++s) {
                k0 = __builtin_amdgcn_mfma_f32_16x16x32_bf16(asbf(fa[s]), asbf(fe[s]), k0, 0, 0, 0);
                k1 = __builtin_amdgcn_mfma_f32_16x16x32_bf16(asbf(fb[s]), asbf(fe[s]), k1, 0, 0, 0);
            }
            ldsw<unsigned>(ta0, pk2(k0[0], k0[1]));
            ldsw<unsigned>(ta1, pk2(k0[2], k0[3]));
            const unsigned q01 = pk2(k1[0], k1[1]), q23 = pk2(k1[2], k1[3]);
            ldsw<u16>(tb1, g == 1 && col == 1 ? (u16)0 : (u16)q01);
            ldsw<u16>(tb0 + st1, (u16)(q01 >> 16));
            ldsw<u16>(tb0 + 2 * st1, (u16)q23);
            ldsw<u16>(tb0 + 3 * st1, (u16)(q23 >> 16));
        }
        BAR_L();
        STAMP(14);
        stage1(2, 2, 0); stage1(2, 2, 1); stage1(2, 2, 2); stage1(2, 2, 3);
        {
            const bool two = w < 4 && nch == 3;
            const int t = tid & 255;
#pragma unroll
            for (int k = 0; k < 2; ++k) {
                if (k == 0 || two) {
                    unsigned pe[6], po[5];
#pragma unroll
                    for (int i = 0; i < 5; ++i) pe[i] = __builtin_bit_cast(unsigned, h2v{ldsr<u16>(ix[2 * i] + k * 2 * TT_CH), ldsr<u16>(ix[2 * i + 1] + k * 2 * TT_CH)});
                    pe[5] = ldsr<u16>(ix[10] + k * 2 * TT_CH);
#pragma unroll
                    for (int i = 0; i < 5; ++i) po[i] = __builtin_amdgcn_alignbit(pe[i + 1], pe[i], 16);
                    const unsigned cbase = L_KC + (tw + 2 * k) * KC_CH, d = cbase + 32 + t * 16;
                    ldsw<u4v>(d, u4v{po[1], po[2], po[3], po[4]});
                    ldsw<u4v>(d + KC_CP, u4v{pe[1], pe[2], pe[3], pe[4]});
                    ldsw<u4v>(d + 2 * KC_CP, u4v{po[0], po[1], po[2], po[3]});
                    ldsw<u4v>(d + 3 * KC_CP, u4v{pe[0], pe[1], pe[2], pe[3]});
                }
            }
            if (w == 7) ldsw<u4v>(L_KC + min(lane >> 1, 11) * KC_CP + (lane & 1) * 16, u4v{0u, 0u, 0u, 0u});
        }
        asm volatile("s_waitcnt vmcnt(4)" : "+v"(w2a), "+v"(w2b)::"memory");
        ldsw<u4v>(L_W2U + w * 1280 + lane * 16, w2a);
        ldsw<u4v>(lane < 16 ? L_W2U + w * 1280 + 1024 + lane * 16 : L_CNT + 48, w2b);
        STAMP(1);
        f4v acc1[2][4];
#pragma unroll
        for (int i = 0; i < 8; ++i) acc1[i >> 2][i & 3] = f4v{};
        bf8v fa[2][2][2], fb[2][2][4];
        auto rd = [&](int buf, int i) {
            const int ks = i >> 1, h = i & 1;
            fa[buf][ks][h] = cat(trr(aw[h] + ks * 8192), trr(aw[h] + ks * 8192 + 1024));
            fb[buf][ks][2 * h] = asbf(ldsr<u4v>(bx[ks] + h * 4096));
            fb[buf][ks][2 * h + 1] = asbf(ldsr<u4v>(bx[ks] + h * 4096 + 2048));
        };
        unsigned sb = 0;
        int kt = rot + 2; kt = kt >= 10 ? kt - 10 : kt;
        auto tile = [&](int cur, bool more, bool stage, auto&& side) {
            const unsigned d = sb == 2 * SLOT ? (unsigned)(-2 * (int)SLOT) : SLOT, lb = sb + w * 2048;
            kt = kt == 9 ? 0 : kt + 1;
            sb += d; aw[0] += d; aw[1] += d; bx[0] += d; bx[1] += d;
#pragma unroll
            for (int i = 0; i < 4; ++i) {
                SB();
                if (more) rd(cur ^ 1, i);
                SB();
#pragma unroll
                for (int j = 0; j < 4; ++j) acc1[i & 1][j] = __builtin_amdgcn_mfma_f32_16x16x32_bf16(fa[cur][i >> 1][i & 1], fb[cur][i >> 1][j], acc1[i & 1][j], 0, 0, 0);
                SB();
                if (stage) {
                    if (i & 1) dma16(rw, vw, kt * (64 * 3456) + (i >> 1) * 4 * 3456, lb + (i >> 1) * 1024 + 16384);
                    else dma16(rx, vx[i >> 1], kt * 128, lb + (i >> 1) * 1024);
                }
                side(i);
            }
            SB();
        };
        auto none = [&](int) {};
        BAR_L();
        STAMP(15);
#pragma unroll
        for (int i = 0; i < 4; ++i) rd(0, i);
        u64 fpa, fpb;
        const u4v wsq = mkrsrc(ws, WS_BYTES);
        auto poll = [&](u64& f, bool mine) { asm volatile("global_load_dwordx2 %0, %1, %2 sc1" : "=v"(f) : "v"(mine ? f_off : WS_FW), "s"(ws) : "memory"); };
        bf8v w2f[2];
        BAR_L();
        tile(0, true, true, [&](int i) {
            if (i < 2) w2f[i] = cat(trr(wtl + i * 512), trr(wtl + i * 512 + 8 * 640));
            if (i == 3) {
                __builtin_amdgcn_raw_buffer_store_b128(__builtin_bit_cast(u4v, w2f[0]), wsr, wa[0], 0, 16);
                __builtin_amdgcn_raw_buffer_store_b128(__builtin_bit_cast(u4v, w2f[1]), wsr, wa[1], 0, 16);
            }
        });
        WAIT_BAR(6); tile(1, true, true, none);
        WAIT_BAR(6); tile(0, true, true, none);
        WAIT_BAR(4);
        STAMP(12);
        tile(1, true, true, [&](int i) { if (i == 0) arrive_publish(L_CNT + 8, ws, WS_FW + bid * 128, tag); });
        WAIT_BAR(4);
        tile(0, true, true, [&](int i) { if (i == 3) poll(fpa, w == 2 || w == 3); });
        WAIT_BAR(5);
        tile(1, true, true, [&](int i) { if (i == 3) poll(fpb, w < 2); });
        asm volatile("s_waitcnt vmcnt(5)" : "+v"(fpa)::"memory");
        if ((w == 2 || w == 3) && !__all(fpa == tag)) wait_flags(ws, f_off, tag);
        BAR_L();
        tile(0, true, true, [&](int i) {
            if (i == 3) {
                dma16_sc1(wsq, lane * 16, WS_XT + (ch0 + tw) * 4096 + fw * 1024, L_UA + tw * UA_CH + UA_PAD + fw * 1024);
                if (w < 4) dma16_sc1(wsq, lane * 16, WS_XT + (ch0 + nch - 1) * 4096 + w * 1024, L_UA + 2 * UA_CH + UA_PAD + w * 1024);
                if (w == 4) dma16_sc1(wsq, (lane >> 5) * (WS_B1 - WS_CS) + (lane & 31) * 16, WS_CS + cb * 512, L_CB);
            }
        });
        asm volatile("s_waitcnt vmcnt(6)" : "+v"(fpb)::"memory");
        if (w >= 5) WAIT_V(5);
        if (w < 2 && !__all(fpb == tag)) wait_flags(ws, f_off, tag);
        BAR_L();
        STAMP(3);
        tile(1, true, false, [&](int i) {
            if (i == 0) {
                if (w == 5) dma16_sc1(wsq, lane * 16, WS_STATS + rb * 1024, L_ST);
                stage_x();
            } else loadb(i - 1);
        });
        if (w < 4) WAIT_V(7); else WAIT_V(6);
        BAR_L();
        tile(0, true, false, [&](int i) { loadb(3 + i); });
        tile(1, false, false, [&](int i) { if (i == 0) loadb(7); });
        if (w >= 5) ldsw<u4v>(L_UA + (w - 5) * UA_CH + min(lane, 29) * 16, u4v{0u, 0u, 0u, 0u});
        WAIT_BAR(8);
        STAMP(2);
        u4v hp[4];
        unsigned ho;
        {
            f4v cs[2], bb[2];
#pragma unroll
            for (int u = 0; u < 2; ++u) { cs[u] = ldsr<f4v>(L_CB + (32 * fw + 16 * u + 4 * g) * 4); bb[u] = ldsr<f4v>(L_CB + 512 + (32 * fw + 16 * u + 4 * g) * 4); }
            const int s = 4 * cb + fw;
            ho = (unsigned)(WS_HA + ((2 * rb + tw) * 54 + s) * 4096 + lane * 16) | oob_if(s >= 54);
#pragma unroll
            for (int ti = 0; ti < 4; ++ti) {
                const f2v svt = ldsr<f2v>(L_ST + (64 * tw + 16 * ti + l16) * 8);
                const f2v rs2 = {svt[0], svt[0]}, nm2 = {-svt[1], -svt[1]};
#pragma unroll
                for (int i = 0; i < 4; ++i) {
                    const int u = i >> 1, e = 2 * (i & 1);
                    const f2v v = __builtin_elementwise_fma(rs2, f2v{acc1[u][ti][e], acc1[u][ti][e + 1]}, __builtin_elementwise_fma(nm2, f2v{cs[u][e], cs[u][e + 1]}, f2v{bb[u][e], bb[u][e + 1]}));
                    const f2v z = v * __builtin_elementwise_fma(v * v, f2v{-0.10294324f, -0.10294324f}, f2v{-2.30220820f, -2.30220820f});
                    const f2v d = f2v{__builtin_amdgcn_exp2f(z[0]), __builtin_amdgcn_exp2f(z[1])} + f2v{1.f, 1.f};
                    const f2v gl = v * f2v{__builtin_amdgcn_rcpf(d[0]), __builtin_amdgcn_rcpf(d[1])};
                    hp[ti][i] = pk2(gl[0], gl[1]);
                    if (4 * ti + i >= 16 - GL) { SB(); loadb(8 + 4 * ti + i - (16 - GL)); }
                }
                asm volatile("" : "+v"(hp[ti]));
                SB();
            }
        }
        STAMP(4);
        {
            const int typ = w < 3 ? 0 : w < 6 ? 1 : 2;
            const int c1 = w < 3 ? w : w < 6 ? w - 3 : 2 * (w - 6), c2 = w == 6 ? 1 : c1;
            const unsigned kl = L_KC + (l16 >> 2) * KC_CP + 56 - 16 * g + 8 * (lane & 3);
            const unsigned ul = L_UA + UA_PAD + 32 * l16 - 32 + 16 * g;
            unsigned ka[3], ua[3];
            ka[0] = kl + c1 * KC_CH;
            ka[1] = typ == 2 ? kl + c2 * KC_CH - 4 * 512 : ka[0];
            ka[2] = typ == 1 ? ka[0] - 6 * 512 : ka[1];
            ua[0] = ul + c1 * UA_CH - typ * 1024;
            ua[2] = ul + c2 * UA_CH;
            ua[1] = typ == 2 ? ua[2] : ua[0];
            bf8v uf[8], kf[8];
            auto rdk = [&](int sl) { const unsigned ad = ka[sl < 4 ? 0 : sl < 6 ? 1 : 2] + 512u * sl; kf[sl] = cat(trr(ad + 8u), trr(ad)); };
            auto rdu = [&](int j) { uf[j] = asbf(ldsr<u4v>(ua[j >= 4 ? 0 : j >= 2 ? 1 : 2] + 512u * j)); };
#pragma unroll
            for (int sl = 0; sl < 8; ++sl) { rdk(sl); rdu(7 - sl); }
            f4v A[3] = {}, B[5] = {};
            __builtin_amdgcn_raw_buffer_store_b128(hp[0], wsr, ho, 0, 16); __builtin_amdgcn_raw_buffer_store_b128(hp[1], wsr, ho + 1024, 0, 16);
            const unsigned fo = WS_FH + (rb * 14 + cb) * 128;
#pragma nounroll
            for (int rho = 0; rho < 7; ++rho) {
#pragma unroll
                for (int i = 0; i < 3; ++i) { ka[i] += 64u; ua[i] -= 64u; asm volatile("" : "+v"(ka[i]), "+v"(ua[i])); }
#pragma unroll
                for (int sl = 0; sl < 8; ++sl) {
                    SB();
                    const int ia = sl < 4 ? 0 : sl < 6 ? 1 : 2, ib = sl < 3 ? 0 : sl - 2;
                    A[ia] = __builtin_amdgcn_mfma_f32_16x16x32_bf16(kf[sl], uf[7 - sl], A[ia], 0, 0, 0);
                    if (sl < 7) B[ib] = __builtin_amdgcn_mfma_f32_16x16x32_bf16(kf[sl], uf[6 - sl], B[ib], 0, 0, 0);
                    SB();
                    rdk(sl); rdu(7 - sl);
                }
                SB();
                constexpr int NR = 30 - 8 - GL;
                if (rho == 0) { __builtin_amdgcn_raw_buffer_store_b128(hp[2], wsr, ho + 2048, 0, 16); __builtin_amdgcn_raw_buffer_store_b128(hp[3], wsr, ho + 3072, 0, 16); }
                if (rho == PUB_RHO) { WAIT_V(0); arrive_publish(L_CNT, ws, fo, tag); }
#pragma unroll
                for (int k = 0; k < 7 - PUB_RHO; ++k) {
                    constexpr int NL = (NR + 6 - PUB_RHO) / (7 - PUB_RHO);
                    if (rho == PUB_RHO + k) {
#pragma unroll
                        for (int n = 8 + GL + NL * k; n < 8 + GL + NL * (k + 1) && n < 30; ++n) loadb(n);
                    }
                }
            }
#pragma unroll
            for (int sl = 0; sl < 8; ++sl) {
                const int ia = sl < 4 ? 0 : sl < 6 ? 1 : 2, ib = sl < 3 ? 0 : sl - 2;
                A[ia] = __builtin_amdgcn_mfma_f32_16x16x32_bf16(kf[sl], uf[7 - sl], A[ia], 0, 0, 0);
                if (sl < 7) B[ib] = __builtin_amdgcn_mfma_f32_16x16x32_bf16(kf[sl], uf[6 - sl], B[ib], 0, 0, 0);
            }
            SB();
            f4v y[4];
            {
                const f4v z = {};
                const f4v a1 = typ < 2 ? A[1] : z, a1b = typ < 2 ? z : A[1], a2 = typ == 0 ? A[2] : z;
                const f4v b12 = B[1] + B[2], b34 = B[3] + B[4];
                y[0] = A[0] + a1 + a2;
                y[2] = A[2] + a1b;
                y[1] = B[0] + (typ < 2 ? b12 : z) + (typ == 0 ? b34 : z);
                y[3] = typ == 1 ? B[4] : B[2] + b34;
            }
            const unsigned yl = 32 * l16 + 8 * g;
#pragma unroll
            for (int i = 0; i < 4; ++i) {
                const int c = i < 2 ? c1 : c2, tau = (i < 2 ? 7 - 2 * typ : max(2 * typ - 1, 1)) - (i & 1);
                const unsigned off = oob_if(c >= nch || (i >= 2 && (typ == 0 || w == 7)));
                const u2v uv = ldsr<u2v>(yl + (L_UA + UA_PAD + c * UA_CH + 512 * tau));
                const float dk = __uint_as_float(((c == 2 ? dsk2 : dsk01) >> (c == 1 ? 16 : 0)) << 16);
                const u2v o = {pk2(y[i][0] + dk * bflo(uv[0]), y[i][1] + dk * bfhi(uv[0])), pk2(y[i][2] + dk * bflo(uv[1]), y[i][3] + dk * bfhi(uv[1]))};
                __builtin_amdgcn_raw_buffer_store_b64(o, wsr, yl, (WS_YT + (ch0 + c) * 4096 + 512 * tau) | off, 16);
            }
        }
        STAMP(5);
    } else {
        const int a = (r - 28) * 8 + xcd;
        const u4v rx = mkrsrc(x, 2048 * 1280);
        {
            unsigned vo[5];
#pragma unroll
            for (int i = 0; i < 5; ++i) {
                const int n = i * 64 + lane, rl = n / 80, cp = n - rl * 80;
                vo[i] = (a * 64 + 8 * w + rl) * 1280 + ((cp ^ (2 * w) ^ ((rl >> 1) << 3)) << 4);
                dma16(rx, vo[i], 0, L_AX + (w * 10 + i) * 1024);
            }
#pragma unroll
            for (int i = 5; i < 10; ++i) dma16(rx, vo[i - 5], 4 * 1280, L_AX + (w * 10 + i) * 1024);
        }
        const u64 e0 = ld_flag(ws, WS_E);
        if (w == 0) ldsw<unsigned>(L_ACNT + (lane & 7) * 4, 0u);
        STAMP(10);
        WAIT_V(1);
        STAMP(11);
        {
            const int row = 8 * w + (lane >> 3), sw = (2 * w) ^ (((row >> 1) & 1) << 3);
            u4v v[10];
#pragma unroll
            for (int i = 0; i < 10; ++i) v[i] = ldsr<u4v>(L_AX + row * 1280 + (((lane & 7) + 8 * i) ^ sw) * 16);
            SB();
            float s1[2] = {0.f, 0.f}, s2[2] = {0.f, 0.f};
#pragma unroll
            for (int i = 0; i < 10; ++i)
#pragma unroll
                for (int j = 0; j < 4; ++j) { s1[j & 1] = dot2(v[i][j], 0x3f803f80u, s1[j & 1]); s2[j & 1] = dot2(v[i][j], v[i][j], s2[j & 1]); }
            const f2v sv = ln_stats(sum8(s1[0] + s1[1]), sum8(s2[0] + s2[1]));
            ldsw<f2v>(L_AST + row * 8, sv);
            __builtin_amdgcn_raw_buffer_store_b64(__builtin_bit_cast(u2v, sv), wsr, (WS_STATS + (a * 64 + row) * 8) | oob_if(lane & 7), 0, 16);
        }
        u4v cv[10];
        {
            const auto rw1 = __builtin_amdgcn_make_buffer_rsrc((void*)(u64)w1, 0, 640 * 3456, 0x00020000);
#pragma unroll
            for (int i = 0; i < 10; ++i) cv[i] = __builtin_amdgcn_raw_buffer_load_b128(rw1, ((lane >> 3) * 3456 + (lane & 7) * 16) | oob_if(a >= 27), (80 * w + 8 * i) * 3456 + a * 128, 0);
        }
        BAR_L();
        STAMP(14);
        {
            const int o = lane & 7, cq = lane >> 3;
            f2v rs2[4], nm2[4];
#pragma unroll
            for (int i = 0; i < 4; ++i) { const f4v q = ldsr<f4v>(L_AST + o * 64 + i * 16); rs2[i] = f2v{q[0], q[2]}; nm2[i] = f2v{-q[1], -q[3]}; }
            u2v v[3][8];
#pragma unroll
            for (int k = 0; k < 3; ++k)
#pragma unroll
                for (int t = 0; t < 8; ++t) v[k][t] = ldsr<u2v>(L_AX + (8 * o + t) * 1280 + ((4 * min(w + 8 * k, 19) + (cq >> 1)) ^ ((2 * o) ^ (((t >> 1) & 1) << 3))) * 16 + (cq & 1) * 8);
            SB();
#pragma unroll
            for (int k = 0; k < 3; ++k) {
                if (k < 2 || w < 4) {
#pragma unroll
                    for (int e = 0; e < 4; ++e) {
                        u4v ov;
#pragma unroll
                        for (int p = 0; p < 4; ++p) {
                            const unsigned va = v[k][2 * p][e >> 1], vb = v[k][2 * p + 1][e >> 1];
                            const f2v n2 = __builtin_elementwise_fma((e & 1) ? f2v{bfhi(va), bfhi(vb)} : f2v{bflo(va), bflo(vb)}, rs2[p], nm2[p]);
                            ov[p] = pk2(n2[0], n2[1]);
                        }
                        __builtin_amdgcn_raw_buffer_store_b128(ov, wsr, WS_XT + (32 * (w + 8 * k) + 4 * cq + e) * 4096 + a * 128 + o * 16, 0, 16);
                        SB();
                    }
                }
                if (k == 0) {
                    WAIT_V(4);
                    tag = mk64(__builtin_amdgcn_readfirstlane((unsigned)e0), __builtin_amdgcn_readfirstlane((unsigned)(e0 >> 32))) + 1ull;
                    arrive_publish(L_ACNT + 8, ws, WS_FS + a * 128, tag);
                }
            }
        }
        STAMP(15);
        {
            unsigned selo = 0x00003f80u, sehi = 0x3f800000u;
            asm volatile("" : "+v"(selo), "+v"(sehi));
            float sm[8] = {0.f, 0.f, 0.f, 0.f, 0.f, 0.f, 0.f, 0.f};
#pragma unroll
            for (int i = 0; i < 10; ++i)
#pragma unroll
                for (int j = 0; j < 4; ++j) { sm[2 * j] = dot2(cv[i][j], selo, sm[2 * j]); sm[2 * j + 1] = dot2(cv[i][j], sehi, sm[2 * j + 1]); }
#pragma unroll
            for (int j = 0; j < 8; ++j) sm[j] = dpp_add<0x128>(sm[j]);
            ldsw<f4v>(L_ACS + (4 * w + g) * 256 + (lane & 7) * 32 + (lane & 8) * 2, (lane & 8) ? f4v{sm[4], sm[5], sm[6], sm[7]} : f4v{sm[0], sm[1], sm[2], sm[3]});
        }
        BAR_L();
        if (w != 7 || a >= 27) {
            WAIT_V(0);
            arrive_publish(L_ACNT + 12, ws, WS_FA + a * 128, tag);
        } else {
            const float bf = bf2f(b1[64 * a + lane]);
            float p[32], sum = 0.f;
#pragma unroll
            for (int i = 0; i < 32; ++i) p[i] = ldsr<float>(L_ACS + i * 256 + lane * 4);
#pragma unroll
            for (int i = 0; i < 32; i += 4) sum += (p[i] + p[i + 1]) + (p[i + 2] + p[i + 3]);
            __builtin_amdgcn_raw_buffer_store_b32(__builtin_bit_cast(unsigned, sum), wsr, WS_CS + (64 * a + lane) * 4, 0, 16);
            __builtin_amdgcn_raw_buffer_store_b32(__builtin_bit_cast(unsigned, bf), wsr, WS_B1 + (64 * a + lane) * 4, 0, 16);
            WAIT_V(0);
            raise_flag(ws, WS_FC + a * 128, tag);
            arrive_publish(L_ACNT + 12, ws, WS_FA + a * 128, tag);
        }
        STAMP(2);
        if (w < 2) wait_flags(ws, WS_FW + (2 * min(54 * w + lane, 107) + (cb2 >> 2)) * 128, tag);
        if (a == 31 && w == 2) {
#pragma unroll
            for (int i = 0; i < 4; ++i) wait_flags(ws, i * 64 + lane < 224 ? WS_FW + (i * 64 + lane) * 128 : WS_FS + (lane & 31) * 128, tag);
            raise_flag(ws, WS_E, tag);
        }
        BAR_L();
        STAMP(3);
        stage_x();
#pragma unroll
        for (int n = 0; n < 30; ++n) loadb(n);
        __builtin_amdgcn_s_waitcnt(0x0F70);
    }

    asm volatile("" : "+s"(rot2));
    if (w == 0) wait_flags(ws, WS_FH + ((rb2 >> 1) * 14 + min(lane, 13)) * 128, tag);
    BAR_L();
    STAMP(6);
    u4v ha[7][4];
    auto loada = [&](int i, int t) { ha[i][t] = __builtin_amdgcn_raw_buffer_load_b128(wsr, lane * 16, WS_HA + ((rb2 * 54 + sidx(i)) * 4 + t) * 1024, 16); asm volatile("" ::: "memory"); };
#pragma unroll
    for (int i = 0; i < 8; ++i) loada(i >> 2, i & 3);
    if (is_gemm) {
        WAIT_V(8);
        arrive_publish(L_CNT + 4, ws, WS_FY + bid * 128, tag);
    }
    const int c0 = 80 * cb2;
    int lz = lane;
    asm volatile("" : "+v"(lz));
    const int cA = c0 + 10 * w, cB = cA + 9;
    const int own0 = max((cA * 0xaaab) >> 17, (cA - 192) >> 1), own1 = max((cB * 0xaaab) >> 17, (cB - 192) >> 1);
    u64 ypoll = tag;
    u4v y0, y1;
    f4v acc2[4][5];
#pragma unroll
    for (int i = 0; i < 20; ++i) acc2[i / 5][i % 5] = f4v{};
#pragma unroll
    for (int i = 0; i < 5; ++i) {
#pragma unroll
        for (int t = 0; t < 4; ++t) {
            SB();
#pragma unroll
            for (int c = 0; c < 5; ++c) acc2[t][c] = __builtin_amdgcn_mfma_f32_16x16x32_bf16(asbf(ha[i][t]), asbf(bw[5 * i + c]), acc2[t][c], 0, 0, 0);
            SB();
            if (i > 0) loada(i + 2, t);
            else if (t == 3) { loada(2, 0); loada(2, 1); loada(2, 2); loada(2, 3); }
            if (i == 1 || (i == 2 && t == 0)) loadb(30 + 4 * (i - 1) + t);
            if (i == 3 && t == 0) ypoll = ld_flag(ws, WS_FY + min(own0 + lz, own1) * 128);
            if (i == 4 && t == 3) {
                if (!__all(ypoll == tag)) wait_flags(ws, WS_FY + min(own0 + lz, own1) * 128, tag);
                y0 = __builtin_amdgcn_raw_buffer_load_b128(wsr, (lz >> 3) * 4096 + (lz & 7) * 16, WS_YT + cA * 4096 + rb2 * 128, 16);
                y1 = __builtin_amdgcn_raw_buffer_load_b128(wsr, (unsigned)((8 + (lz >> 3)) * 4096 + (lz & 7) * 16) | oob_if(lz >= 16), WS_YT + cA * 4096 + rb2 * 128, 16);
            }
        }
    }
    STAMP(7);
    const unsigned da = L_RED + (lz >> 4) * 1024 + (w >> 1) * 256 + (lz & 15) * 16 + (w & 1) * 8;
    auto dump = [&](int t) {
#pragma unroll
        for (int c = 0; c < 5; ++c) ldsw<u2v>(da + (5 * t + c) * 4096, u2v{pk2(acc2[t][c][0], acc2[t][c][1]), pk2(acc2[t][c][2], acc2[t][c][3])});
    };
#pragma unroll
    for (int t = 0; t < 4; ++t) {
        SB();
#pragma unroll
        for (int c = 0; c < 5; ++c) acc2[t][c] = __builtin_amdgcn_mfma_f32_16x16x32_bf16(asbf(ha[5][t]), asbf(bw[25 + c]), acc2[t][c], 0, 0, 0);
        SB();
        if (t > 0) dump(t - 1);
        SB();
#pragma unroll
        for (int c = 0; c < 5; ++c) acc2[t][c] = __builtin_amdgcn_mfma_f32_16x16x32_bf16(asbf(ha[6][t]), asbf(bw[30 + c]), acc2[t][c], 0, 0, 0);
    }
    SB();
    ldsw<u4v>(L_YS + (10 * w + (lz >> 3)) * YS_ROW + (lz & 7) * 16, y0);
    ldsw<u4v>(L_YS + min(10 * w + 8 + (lz >> 3) + (lz >= 16 ? 80 : 0), 80) * YS_ROW + (lz & 7) * 16, y1);
    u4v sel[4], one[2];
    {
        const int n = lz & 15, gz = lz >> 4;
        const unsigned lo = (n & 2) ? 0u : 0x3f80u << (16 * (n & 1)), hi = (n & 2) ? 0x3f80u << (16 * (n & 1)) : 0u;
#pragma unroll
        for (int q = 0; q < 4; ++q) { const unsigned m = (n >> 2) == q ? ~0u : 0u; sel[q] = u4v{lo & m, hi & m, lo & m, hi & m}; }
#pragma unroll
        for (int q = 0; q < 2; ++q) {
            const unsigned m0 = gz == 2 * q + (n >> 3) && !(n & 4) ? ~0u : 0u, m1 = gz == 2 * q + (n >> 3) && (n & 4) ? ~0u : 0u;
            one[q] = u4v{lo & m0, hi & m0, lo & m1, hi & m1};
        }
    }
    dump(3);
    BAR_L();
    STAMP(8);
    {
        const int n = lz & 15, gz = lz >> 4;
#pragma unroll
        for (int k = 0; k < 3; ++k) {
            if (k < 2 || w < 4) {
                const int tl = w + 8 * k, t = (tl * 52) >> 8, c = tl - 5 * t;
                u4v p[4];
#pragma unroll
                for (int q = 0; q < 4; ++q) p[q] = ldsr<u4v>(L_RED + lz * 16 + (tl * 4096 + q * 1024));
                const u4v yf = ldsr<u4v>(L_YS + n * YS_ROW + gz * 16 + (16 * c * YS_ROW + (t >> 1) * 64));
                const u4v xf = ldsr<u4v>(L_XS + n * 160 + gz * 16 + (16 * t * 160 + (c >> 1) * 64));
                const u2v bv = ldsr<u2v>(L_XS + 10240 + gz * 8 + c * 32);
                f4v d = {bflo(bv[0]), bfhi(bv[0]), bflo(bv[1]), bfhi(bv[1])};
#pragma unroll
                for (int q = 0; q < 4; ++q) d = __builtin_amdgcn_mfma_f32_16x16x32_bf16(asbf(p[q]), asbf(sel[q]), d, 0, 0, 0);
                d = __builtin_amdgcn_mfma_f32_16x16x32_bf16(asbf(yf), asbf((t & 1) ? one[1] : one[0]), d, 0, 0, 0);
                d = __builtin_amdgcn_mfma_f32_16x16x32_bf16(asbf((c & 1) ? one[1] : one[0]), asbf(xf), d, 0, 0, 0);
                ldsw<u2v>(L_OUT + n * OUT_ROW + gz * 8 + (16 * t * OUT_ROW + c * 32), u2v{pk2(d[0], d[1]), pk2(d[2], d[3])});
            }
        }
        BAR_L();
        const auto ro = __builtin_amdgcn_make_buffer_rsrc((void*)(u64)out, 0, 2048 * 1280, 0x00020000);
#pragma unroll
        for (int k = 0; k < 2; ++k) {
            const int id = 64 * k + lz, row = (id * 205) >> 11, ch = id - 10 * row;
            const u4v v = ldsr<u4v>(L_OUT + (8 * w + min(row, 7)) * OUT_ROW + ch * 16);
            __builtin_amdgcn_raw_buffer_store_b128(v, ro, (unsigned)((rb2 * 64 + 8 * w + row) * 1280 + cb2 * 160 + ch * 16) | oob_if(id >= 80), 0, 0);
        }
    }
    STAMP(9);
    if (w == 0 && bid == 5) { u64 c1 = __builtin_amdgcn_s_memtime(); *(u64*)(ws + WS_STAMP + 400000) = c1 - cyc0; *(u64*)(ws + WS_STAMP + 400008) = st[9] - st[0]; }
    for (int i = 0; i < NSTAMP; ++i) *(u64*)(ws + WS_STAMP + ((bid * 8 + w) * NSTAMP + i) * 8) = st[i];
}

__global__ void k_spin(const u64* st, int which, int mode) {
  u64 mn=~0ull; for(int i=0;i<2048;i++){ u64 a=st[i*NSTAMP]; mn=a<mn?a:mn; }
  u64 mx=0,sum=0; int n=0;
  for(int b=0;b<256;b++){ bool aux=(b>>3)>=28; u64 h=0; for(int w=0;w<8;w++){ u64 v=st[(b*8+w)*NSTAMP+which]; h=v>h?v:h; } if(h){ h-=mn; mx=h>mx?h:mx; if(aux==(mode==2)){ sum+=h; n++; } } }
  u64 d = (mode==0? mx : sum/(n?n:1))*100ull;
  if(d>1000000ull) d=1000000ull;
  u64 t0=__builtin_amdgcn_s_memrealtime();
  while(__builtin_amdgcn_s_memrealtime()-t0<d){ __builtin_amdgcn_s_sleep(8); }
}

extern "C" void kernel_launch(void* const* d_in, const int* in_sizes, int n_in, void* d_out, int out_size, void* d_ws, size_t ws_size, hipStream_t stream) {
    (void)in_sizes; (void)n_in; (void)out_size; (void)ws_size;
    (void)hipFuncSetAttribute((const void*)s4ffn_fused, hipFuncAttributeMaxDynamicSharedMemorySize, LDS_BYTES);
    s4ffn_fused<<<256, 512, LDS_BYTES, stream>>>((const u16*)d_in[0], (const u16*)d_in[1], (const u16*)d_in[2], (const u16*)d_in[3], (const u16*)d_in[4],
                                                 (const u16*)d_in[5], (const u16*)d_in[6], (const u16*)d_in[7], (const u16*)d_in[8], (const u16*)d_in[10],
                                                 (const u16*)d_in[11], (const u16*)d_in[12], (const u16*)d_in[13], (u16*)d_out, (char*)d_ws);
    k_spin<<<1,1,0,stream>>>((const u64*)((char*)d_ws+WS_STAMP),0,0);
}
```

```cpp
#include <hip/hip_runtime.h>
#include <stdint.h>
#include <utility>
#define PUB_RHO 2
#define CSUM_ON 1
#define CODE_CHUNKS 5
#define CODE_BYTES 34816u
#define PACE 0
#define PT0 7
#define HFIRST 1
#define GL 4
#define EXMODE 0
#define GBAR 1
#define HPROG 1
#define HPF 0
#define NB2 29
#define YPI 2
#define YLI 3
#define HW 14
#define GQ0 12
#define HQ0 16
#define HQ1 52
#define PREP0 1
#define HPRIO 0
#define HPW 0
#define ARR 0

typedef unsigned short u16;
typedef unsigned long long u64;
typedef short s4v __attribute__((ext_vector_type(4)));
typedef __bf16 bf8v __attribute__((ext_vector_type(8)));
typedef __bf16 bf2v __attribute__((ext_vector_type(2)));
typedef float f2v __attribute__((ext_vector_type(2)));
typedef float f4v __attribute__((ext_vector_type(4)));
typedef float f16v __attribute__((ext_vector_type(16)));
typedef unsigned u2v __attribute__((ext_vector_type(2)));
typedef unsigned short h2v __attribute__((ext_vector_type(2)));
typedef unsigned u4v __attribute__((ext_vector_type(4)));
typedef __attribute__((address_space(1))) u64 gu64;
#define LDSP(T) __attribute__((address_space(3))) T*
#define DI __device__ __forceinline__
#define RLX_AGENT __ATOMIC_RELAXED, __HIP_MEMORY_SCOPE_AGENT

constexpr unsigned WS_E = 0;
constexpr unsigned FL = 128;
constexpr unsigned WS_FH = 4096;
constexpr unsigned WS_FA = 36864;
constexpr unsigned WS_FS = 40960;
constexpr unsigned WS_FC = 45056;
constexpr unsigned WS_FY = 49152;
constexpr unsigned WS_FW = 81920;
constexpr unsigned WS_STATS = 131072;
constexpr unsigned WS_CS = 196608, WS_B1 = 204800;
constexpr unsigned WS_STAMP = 262144;
constexpr unsigned WS_XT = 1u << 20;
constexpr unsigned WS_W2T = 4u << 20;
constexpr unsigned WS_YT = 8u << 20;
constexpr unsigned WS_HA = 12u << 20;
constexpr unsigned WS_BYTES = 20u << 20;
constexpr unsigned SLOT = 32768;
constexpr unsigned L_KC = 98304, KC_CH = 16640, KC_CP = 4160;
constexpr unsigned L_UA = 148224, UA_CH = 4576, UA_PAD = 480;
constexpr unsigned L_MISC = 162368, L_CB = L_MISC, L_CNT = L_MISC + 1024;
constexpr unsigned L_TBL = 65536, TBL_ROW = 544, TBL_CH = 53 * TBL_ROW;
constexpr unsigned L_PRM = 152064, L_TT = 158464, TT_CH = 1296, L_W2U = SLOT, W2U_HI = 16384;
constexpr unsigned L_SCR = L_CB;
constexpr unsigned L_XS = 32768, L_ST = L_XS + 10752, L_SEL = L_ST + 1024, L_RED = 51200, L_YS = L_RED + 81920, YS_ROW = 144, L_OUT = L_YS + 81 * YS_ROW + 112, OUT_ROW = 176;
constexpr unsigned L_AX = 0, L_AST = 81920, L_ACNT = L_AST + 512, L_ACS = L_AST + 1024;
static_assert(L_TT + 3 * TT_CH <= L_MISC && L_CNT + 64 <= 163840, "LDS map");
constexpr unsigned LDS_BYTES = 163840;
constexpr int SPIN_LIMIT = 400000;

#define STAMP(i)

DI float bf2f(unsigned v) { return __uint_as_float(v << 16); }
DI float bflo(unsigned v) { return __uint_as_float(v << 16); }
DI float bfhi(unsigned v) { return __uint_as_float(v & 0xffff0000u); }
DI unsigned pk2(float a, float b) { bf2v v; v[0] = (__bf16)a; v[1] = (__bf16)b; return __builtin_bit_cast(unsigned, v); }
DI float rbf(float a) { return bflo(pk2(a, a)); }
template <typename P> DI u4v mkrsrc(P p, unsigned bytes) { u64 a = (u64)p; u4v r; r[0] = (unsigned)a; r[1] = (unsigned)(a >> 32); r[2] = bytes; r[3] = 0x00020000u; return r; }
DI void dma16(u4v rs, unsigned voff, unsigned soff, unsigned lds) {
    asm volatile("s_mov_b32 m0, %3\n\ts_nop 0\n\tbuffer_load_dwordx4 %0, %1, %2 offen lds" ::"v"(voff), "s"(rs), "s"(soff), "s"(lds) : "memory");
}
DI void dma16g(const void* p, unsigned lds) {
    asm volatile("s_mov_b32 m0, %1\n\ts_nop 0\n\tglobal_load_lds_dwordx4 %0, off" ::"v"(p), "s"(lds) : "memory");
}
DI void dma16g_lanes3(const void* p, unsigned lds) {
    u64 ex;
    asm volatile("s_mov_b64 %0, exec\n\ts_mov_b64 exec, 7\n\ts_mov_b32 m0, %2\n\ts_nop 0\n\tglobal_load_lds_dwordx4 %1, off\n\ts_mov_b64 exec, %0" : "=&s"(ex) : "v"(p), "s"(lds) : "memory");
}
DI void dma16_lanes16(u4v rs, unsigned voff, unsigned soff, unsigned lds) {
    u64 ex;
    asm volatile("s_mov_b64 %0, exec\n\ts_mov_b64 exec, 0xffff\n\ts_mov_b32 m0, %4\n\ts_nop 0\n\tbuffer_load_dwordx4 %1, %2, %3 offen lds\n\ts_mov_b64 exec, %0"
                 : "=&s"(ex) : "v"(voff), "s"(rs), "s"(soff), "s"(lds) : "memory");
}
DI void touch(u64 p, unsigned lds) {
    asm volatile("s_mov_b32 m0, %1\n\ts_nop 0\n\tglobal_load_lds_dword %0, off" ::"v"(p), "s"(lds) : "memory");
}
DI void touch_code(unsigned chunk, int lane, unsigned lds) {
    u64 pc;
    asm volatile("s_getpc_b64 %0" : "=s"(pc));
    const u64 p = (pc & ~127ull) + min(chunk * 8192u + lane * 128u, CODE_BYTES - 128u);
    asm volatile("s_mov_b32 m0, %1\n\ts_nop 0\n\tglobal_load_lds_dword %0, off" ::"v"(p), "s"(lds) : "memory");
}
DI void dma16_sc1(u4v rs, unsigned voff, unsigned soff, unsigned lds) {
    asm volatile("s_mov_b32 m0, %3\n\ts_nop 0\n\tbuffer_load_dwordx4 %0, %1, %2 offen sc1 lds" ::"v"(voff), "s"(rs), "s"(soff), "s"(lds) : "memory");
}
template <int IMM> DI void set_m0(unsigned base) { asm volatile("s_add_u32 m0, %0, %1" ::"s"(base), "i"(IMM) : "scc"); }
template <int IMM> DI void dma_at(unsigned base, u4v rs, unsigned voff, unsigned soff) {
    asm volatile("s_add_u32 m0, %3, %4\n\ts_nop 0\n\tbuffer_load_dwordx4 %0, %1, %2 offen lds" ::"v"(voff), "s"(rs), "s"(soff), "s"(base), "i"(IMM) : "memory", "scc");
}
DI void dma_m0(u4v rs, unsigned voff, unsigned soff) { asm volatile("buffer_load_dwordx4 %0, %1, %2 offen lds" ::"v"(voff), "s"(rs), "s"(soff) : "memory"); }
#define SB() __builtin_amdgcn_sched_barrier(0)
#define LGKM0() __builtin_amdgcn_s_waitcnt(0xC07F)
#define WAIT_BAR(n) do { LGKM0(); asm volatile("s_waitcnt vmcnt(" #n ") lgkmcnt(0)\n\ts_barrier" ::: "memory"); } while (0)
#define WAIT_BARN(n) do { LGKM0(); asm volatile("s_waitcnt vmcnt(%0) lgkmcnt(0)\n\ts_barrier" ::"i"(n) : "memory"); } while (0)
#define BAR_L() do { LGKM0(); asm volatile("s_waitcnt lgkmcnt(0)\n\ts_barrier" ::: "memory"); } while (0)
#define WAIT_V(n) asm volatile("s_waitcnt vmcnt(" #n ")" ::: "memory")
template <typename T> DI T ldsr(unsigned a) { return *(LDSP(T))a; }
template <typename T> DI void ldsw(unsigned a, T v) { *(LDSP(T))a = v; }
DI void stw(unsigned a, unsigned v, int off) { asm volatile("ds_write_b32 %0, %1 offset:%2" ::"v"(a), "v"(v), "i"(off) : "memory"); }
DI s4v trr(unsigned a) { return __builtin_amdgcn_ds_read_tr16_b64_v4i16((LDSP(s4v))a); }
DI bf8v cat(s4v a, s4v b) { return __builtin_bit_cast(bf8v, __builtin_shufflevector(a, b, 0, 1, 2, 3, 4, 5, 6, 7)); }
DI bf8v asbf(u4v a) { return __builtin_bit_cast(bf8v, a); }
template <int CTRL> DI float dpp_add(float v) { return v + __builtin_bit_cast(float, __builtin_amdgcn_update_dpp(0, __builtin_bit_cast(int, v), CTRL, 0xf, 0xf, true)); }
DI float sum8(float v) { return dpp_add<0x141>(dpp_add<0x4e>(dpp_add<0xb1>(v))); }
DI float dot2(unsigned a, unsigned b, float c) { return __builtin_amdgcn_fdot2_f32_bf16(__builtin_bit_cast(bf2v, a), __builtin_bit_cast(bf2v, b), c, false); }
DI u64 ld_flag(const char* ws, unsigned off) { return __hip_atomic_load((gu64*)(ws + off), RLX_AGENT); }
DI void wait_flags(const char* ws, unsigned off, u64 tag) {
#pragma nounroll
    for (int it = 0; it < SPIN_LIMIT; ++it) {
        if (__all(ld_flag(ws, off) == tag)) break;
        __builtin_amdgcn_s_sleep(1);
    }
    asm volatile("" ::: "memory");
}
DI unsigned lds_count(unsigned cnt_lds) {
    unsigned old, one = 1u;
    u64 ex;
    asm volatile("s_mov_b64 %1, exec\n\ts_mov_b64 exec, 1\n\ts_waitcnt lgkmcnt(0)\n\tds_add_rtn_u32 %0, %2, %3\n\ts_waitcnt lgkmcnt(0)\n\ts_mov_b64 exec, %1"
                 : "=&v"(old), "=&s"(ex) : "v"(cnt_lds), "v"(one) : "memory");
    return __builtin_amdgcn_readfirstlane(old);
}
DI void raise_flag(char* ws, unsigned flag_off, u64 tag) { __hip_atomic_store((gu64*)(ws + flag_off), tag, RLX_AGENT); }
DI void arrive_publish(unsigned cnt_lds, char* ws, unsigned flag_off, u64 tag, unsigned last = 7u) {
    if (lds_count(cnt_lds) == last) raise_flag(ws, flag_off, tag);
}
DI void lds_wait(unsigned cnt_lds, unsigned n) {
#pragma nounroll
    for (int it = 0; it < SPIN_LIMIT; ++it) {
        if (__builtin_amdgcn_readfirstlane(*(volatile LDSP(unsigned))cnt_lds) >= n) break;
        __builtin_amdgcn_s_sleep(1);
    }
    asm volatile("" ::: "memory");
}
DI unsigned oob_if(bool c) { return c ? 0x80000000u : 0u; }
DI f2v ln_stats(float s1, float s2) {
    const float mean = s1 * (1.f / 640.f), var = fmaxf(__builtin_fmaf(-mean, mean, s2 * (1.f / 640.f)), 0.f);
    const float rs = __builtin_amdgcn_rcpf(rbf(__builtin_amdgcn_sqrtf(rbf(rbf(var) + 1e-5f))));
    return f2v{rs, rbf(mean) * rs};
}
DI f2v cmul_a(f2v a, f2v b) { f2v t; asm("v_pk_mul_f32 %0, %1, %2 op_sel_hi:[0,1]" : "=v"(t) : "v"(a), "v"(b)); return t; }
DI f2v cmul_b(f2v a, f2v b, f2v t) {
    f2v o;
    asm("v_pk_fma_f32 %0, %1, %2, %3 op_sel:[1,1,0] op_sel_hi:[1,0,1] neg_lo:[0,1,0]" : "=v"(o) : "v"(a), "v"(b), "v"(t));
    return o;
}
DI f2v cmul(f2v a, f2v b) { return cmul_b(a, b, cmul_a(a, b)); }
DI f2v cexp_p(float are, float aim, float p) {
    const float m = __builtin_amdgcn_exp2f(are * p), an = __builtin_amdgcn_fractf(aim * p);
    return f2v{m * __builtin_amdgcn_cosf(an), m * __builtin_amdgcn_sinf(an)};
}

struct WRow { unsigned v[8][32]; };
enum { P_SB = 0, P_SA = 7, P_SX0 = 14, P_SW0, P_SXK, P_SWK, P_SX1, P_SW1, P_WA0, P_WA1, P_WA2, P_WA3, P_SO2, P_FB, P_XS, P_HO, P_YT1, P_YT2, P_FY, P_YL };
constexpr unsigned oobc(bool c) { return c ? 0x80000000u : 0u; }
constexpr WRow mk_row(int blk) {
    WRow o{};
    const int xcd = blk & 7, r = ((blk >> 3) + 28) & 31, bid = r * 8 + xcd;
    const int nch = bid < 192 ? 3 : 2, ch0 = bid < 192 ? 3 * bid : 2 * bid + 192;
    const int rb2 = xcd * 4 + (r & 3), cb2 = r >> 2;
    const int gi = ARR == 0 ? (r & 3) : (r & 1), gj = ARR == 0 ? (r >> 2) : (r >> 1);
    const int rb = ARR == 0 ? (xcd >> 1) * 4 + gi : xcd * 2 + gi, cb = ARR == 0 ? (xcd & 1) * 7 + gj : gj;
    const int rot = (gj + 3 * gi + xcd) % 10;
    for (int w = 0; w < 8; ++w) {
        unsigned* p = o.v[w];
        for (int i = 0; i < 7; ++i) {
            const int st = (7 * cb2 + 14 * (r & 3) + w + 8 * i) % 54;
            p[P_SB + i] = (WS_W2T + (cb2 * 54 + st) * 5120) | oobc(w + 8 * i >= 54);
            p[P_SA + i] = WS_HA + (rb2 * 54 + st) * 4096;
        }
        p[P_SX0] = (rb * 128 + 16 * w) * 1280; p[P_SW0] = 8 * w * 3456 + cb * 256;
        p[P_SXK] = p[P_SX0] + rot * 128; p[P_SWK] = p[P_SW0] + rot * (64 * 3456);
        p[P_SX1] = p[P_SX0] + (rot + 1) % 10 * 128; p[P_SW1] = p[P_SW0] + (rot + 1) % 10 * (64 * 3456);
        p[P_WA0] = (WS_W2T + (bid >> 2) * 5120 + ((bid >> 1) & 1) * 512) | oobc(bid >= 216);
        p[P_WA1] = WS_XT + ch0 * 4096 + (w & 3) * 1024;
        p[P_WA2] = w == 4 ? WS_CS + cb * 512 : w == 5 ? WS_STATS + rb * 1024 : 0x80000000u;
        p[P_WA3] = WS_FW + ((cb2 >> 2) * 128 + (w & 1) * 54) * FL;
        p[P_SO2] = ((bid >> 2) * 32 + ((bid >> 1) & 1) * 8) * 1280 + (bid & 1) * 640;
        p[P_FB] = w == 4 ? WS_FA : w == 5 ? WS_FS + 2 * rb * FL : WS_FC + 2 * cb * FL;
        p[P_XS] = (rb2 * 64 + 8 * w) * 1280 + cb2 * 160;
        p[P_HO] = (WS_HA + ((2 * rb + ((w >> 1) & 1)) * 54 + 4 * cb + 2 * (w & 1)) * 4096) | oobc(4 * cb + 2 * (w & 1) >= 54);
        const int typ = w < 3 ? 0 : w < 6 ? 1 : 2, c1 = w < 3 ? w : w < 6 ? w - 3 : 2 * (w - 6), c2 = w == 6 ? 1 : c1;
        p[P_YT1] = (WS_YT - 512 + (ch0 + c1) * 4096 + 512 * (7 - 2 * typ)) | oobc(c1 >= nch);
        p[P_YT2] = (WS_YT - 512 + (ch0 + c2) * 4096 + 512 * (typ == 2 ? 3 : 1)) | oobc(c2 >= nch || typ == 0 || w == 7);
        const int cA = 80 * cb2 + 10 * w, cB = cA + 9;
        const int own0 = cA < 576 ? cA / 3 : (cA - 192) / 2, own1 = cB < 576 ? cB / 3 : (cB - 192) / 2;
        p[P_FY] = (WS_FY + own0 * FL) | (unsigned)(own1 - own0) << 24;
        p[P_YL] = WS_YT + cA * 4096 + rb2 * 128;
    }
    return o;
}
template <int B> constexpr WRow wrow = mk_row(B);
template <typename S> struct WTab;
template <int... B> struct WTab<std::integer_sequence<int, B...>> { WRow r[sizeof...(B)] = {wrow<B>...}; };
static __device__ __attribute__((aligned(128))) const WTab<std::make_integer_sequence<int, 256>> wtab{};
typedef unsigned u16v __attribute__((ext_vector_type(16)));
struct SelTab { unsigned v[6][64][4]; };
constexpr SelTab mk_sel() {
    SelTab o{};
    for (int l = 0; l < 64; ++l)
        for (int j = 0; j < 8; ++j) {
            const int n = l & 15, k = 8 * (l >> 4) + j;
            for (int q = 0; q < 4; ++q) if (n == 4 * q + (j & 3)) o.v[q][l][j >> 1] |= 0x3f80u << (16 * (j & 1));
            for (int q = 0; q < 2; ++q) if (k == 16 * q + n) o.v[4 + q][l][j >> 1] |= 0x3f80u << (16 * (j & 1));
        }
    return o;
}
static __device__ __attribute__((aligned(128))) const SelTab seltab = mk_sel();

template <int N> struct IC { static constexpr int value = N; };
#define GP(T) __attribute__((address_space(1))) T*
typedef unsigned u8v __attribute__((ext_vector_type(8)));
DI u64 mk64(unsigned lo, unsigned hi) { return ((u64)hi << 32) | lo; }
__global__ __launch_bounds__(512, 2) void s4ffn_fused(const u16*, const u16*, const u16*, const u16*, const u16*, const u16*, const u16*, const u16*, const u16*,
                                                      const u16*, const u16*, const u16*, const u16*, u16*, char*) {
    const int tid = threadIdx.x, lane = tid & 63, w = __builtin_amdgcn_readfirstlane(tid >> 6);
    const int xcd = blockIdx.x & 7, r = ((blockIdx.x >> 3) + 28) & 31, bid = r * 8 + xcd;
    const bool is_gemm = r < 28;
    STAMP(0);
    if (w == 7) touch((u64)&wtab + blockIdx.x * 1024u + (lane & 7) * 128u, 163840 - 256);
    else if (w < CODE_CHUNKS && ((r + w) & 3) == 0) touch_code(w, lane, 163840 - 256);
    u64 a_x, a_prm;
    u8v a_m;
    u4v a_t;
    u16v pa_, pb_;
    asm volatile("s_load_dwordx2 %0, %4, 0x0\n\ts_load_dwordx2 %1, %4, %5 offset:0x8\n\ts_load_dwordx8 %2, %4, 0x48\n\ts_load_dwordx4 %3, %4, 0x68\n\ts_waitcnt lgkmcnt(0)"
                 : "=&s"(a_x), "=&s"(a_prm), "=&s"(a_m), "=&s"(a_t) : "s"(__builtin_amdgcn_kernarg_segment_ptr()), "s"(8 * w));
    auto load_table = [&]() {
        asm volatile("s_load_dwordx16 %0, %2, %3\n\ts_load_dwordx16 %1, %2, %3 offset:0x40\n\ts_waitcnt lgkmcnt(0)" : "=&s"(pa_), "=&s"(pb_) : "s"(&wtab), "s"((blockIdx.x * 8 + w) * 128));
    };
    auto P = [&](int i) { return i < 16 ? pa_[i] : pb_[i - 16]; };
    const GP(u16) x = (const GP(u16))a_x;
    const char* prm = (const char*)a_prm;
    const GP(u16) w1 = (const GP(u16))mk64(a_m[0], a_m[1]);
    const GP(u16) b1 = (const GP(u16))mk64(a_m[2], a_m[3]);
    const GP(u16) w2 = (const GP(u16))mk64(a_m[4], a_m[5]);
    const GP(u16) b2 = (const GP(u16))mk64(a_m[6], a_m[7]);
    GP(u16) out = (GP(u16))mk64(a_t[0], a_t[1]);
    char* ws = (char*)mk64(a_t[2], a_t[3]);
    u64 tag;
    const auto wsr = __builtin_amdgcn_make_buffer_rsrc((void*)ws, 0, WS_BYTES, 0x00020000);
    const int nch = bid < 192 ? 3 : 2, ch0 = 3 * bid - max(bid - 192, 0);
    const int g = lane >> 4, l16 = lane & 15;
    const int rb2 = xcd * 4 + (r & 3), cb2 = r >> 2;
    u4v bw[35];
    const unsigned lo16 = lane * 16;
    auto loadb = [&](int n) {
        bw[n] = __builtin_amdgcn_raw_buffer_load_b128(wsr, lo16 + (n % 5) * 1024, P(P_SB + n / 5), 16);
        asm volatile("" ::: "memory");
    };
    unsigned xs0, xs1;
    auto stage_x_init = [&]() {
        const int q0 = (lane * 205) >> 11, n1 = 64 + l16, q1 = (n1 * 205) >> 11;
        xs0 = lo16 + q0 * 1120; xs1 = n1 * 16 + q1 * 1120;
        asm volatile("" : "+v"(xs0), "+v"(xs1));
    };
    auto stage_x = [&]() {
        const u4v rx = mkrsrc(x, 2048 * 1280);
        dma16(rx, xs0, P(P_XS), L_XS + w * 1280);
        dma16_lanes16(rx, xs1, P(P_XS), L_XS + w * 1280 + 1024);
        if (w == 0) dma16_lanes16(mkrsrc(b2, 1280), lo16, cb2 * 160, L_XS + 10240);
    };
    if (__builtin_expect(is_gemm, 1)) {
        if (w < 6) dma16g(prm + (ch0 + min(g, nch - 1)) * 256 + l16 * 16, L_PRM + w * 1024);
        else dma16g_lanes3(lane == 2 ? (const char*)ws + WS_E : prm + min(((ch0 * 2) & ~15) + lane * 16, 1264), L_SCR + (w - 6) * 64);
        SB();
        const int gi = r & 3, gj = r >> 2, rb = (xcd >> 1) * 4 + gi, cb = (xcd & 1) * 7 + gj;
        const int fw = w & 3, tw = w >> 2;
        const u4v rx = mkrsrc(x, 2048 * 1280), rw = mkrsrc(w1, 640 * 3456);
        unsigned vx[2], vw[2];
        vx[0] = (lane >> 3) * 1280 + (((lane & 7) ^ g) << 4);
        vx[1] = (vx[0] ^ 64u) + 8 * 1280;
        {
            const int wc = l16 ^ (2 * g) ^ ((w & 1) << 3);
            vw[0] = (unsigned)(g * 3456 + wc * 16) | (cb == 13 ? (unsigned)(wc & 8) << 28 : 0u);
            vw[1] = vw[0] + 4 * 3456;
        }
        const unsigned lw = w * 2048;
        const int col = 16 * (w & 1) + l16;
        unsigned ta = L_TBL + (w >> 1) * TBL_CH + l16 * TBL_ROW + g * 16, te = ta + (21 + 16 * (w & 1)) * TBL_ROW;
        unsigned ta0, ta1, tb0, tb1, st1;
        {
            const unsigned tt = L_TT + (w >> 1) * TT_CH;
            ta0 = tt + ((col << (3 - max(g - 1, 0))) + ((0x30200100u >> (8 * g)) & 0xffu) * (g == 1 ? 4u : 8u)) * 2;
            ta1 = ta0 + (g < 3 ? 4 : 128);
            tb0 = tt + (g == 0 ? 512 + col : 644) * 2; st1 = g == 0 ? 64 : 0;
            tb1 = g == 1 && col < 2 ? tt + (640 + col) * 2 : tb0;
        }
        unsigned ix[12], wo[3], wa[3], wtl = 0, fa_off = 0, fb_off = 0;
        if (w >= 4) {
        {
            const int l0 = 8 * (tid & 255) - 3;
            const float lf = (float)l0;
            const unsigned kb = L_TT, kc = kb - 2u * (0x4300 - 128);
#pragma unroll
            for (int i = 0; i < 6; ++i) {
                const unsigned c2 = pk2(lf + (float)(2 * i), lf + (float)(2 * i + 1));
                asm("v_mad_u32_u16 %0, %1, 2, %2" : "=v"(ix[2 * i]) : "v"(c2), "s"(kc));
                asm("v_mad_u32_u16 %0, %1, 2, %2 op_sel:[1,0,0,0]" : "=v"(ix[2 * i + 1]) : "v"(c2), "s"(kc));
            }
            if (w == 4) {
#pragma unroll
                for (int i = 0; i < 11; ++i) {
                    const int l = l0 + i;
                    const unsigned lo = kb + 2u * min((unsigned)l, 641u), m = (unsigned)((l - 128) >> 31);
                    ix[i] = (lo & m) | (ix[i] & ~m);
                }
            }
#pragma unroll
            for (int i = 0; i < 11; ++i) asm volatile("" : "+v"(ix[i]));
        }
#pragma unroll
            for (int k = 0; k < 3; ++k) {
                const int pc = fw + 4 * k, n = 64 * (pc >= 5 ? pc - 5 : pc) + lane, rl = (n * 205) >> 13;
                wo[k] = n * 16 + rl * 640 + (pc >= 5 ? 16 * 1280 : 0);
            }
#pragma unroll
            for (int k = 0; k < 3; ++k) {
                const int Pq = 20 * (bid & 1) + 2 * (fw + 4 * k) + (lane >> 5), cbb = (Pq * 52) >> 8;
                wa[k] = ((cbb * 265 + Pq) * 1024 + (lane & 31) * 16) | oob_if(fw + 4 * k > 9);
            }
            wtl = L_W2U + (4 * (g & 1) + (l16 >> 2)) * 640 + (lane >> 5) * 32 + (lane & 3) * 8 + fw * 64;
            fa_off = min(lane, w == 4 ? 31 : cb == 13 && w >= 6 ? 0 : 1) * FL;
            fb_off = min(lane, w < 6 ? 53 : 0) * FL;
            asm volatile("" : "+v"(wo[0]), "+v"(wo[1]), "+v"(wo[2]), "+v"(wa[0]), "+v"(wa[1]), "+v"(wa[2]), "+v"(wtl), "+v"(fa_off), "+v"(fb_off));
            stage_x_init();
        } else {
#pragma unroll
            for (int i = 0; i < 2; ++i) ix[i] = (64 * (w >> 1) + l16) * 128 + (((4 * i + g) ^ (l16 >> 1)) << 4);
#pragma unroll
            for (int i = 0; i < 4; ++i) ix[2 + i] = 16384 + (8 * g + (l16 >> 2)) * 256 + (((4 * (w & 1) + i) ^ (l16 >> 2) ^ ((g & 1) << 2)) << 5) + (lane & 3) * 8;
#pragma unroll
            for (int i = 0; i < 6; ++i) asm volatile("" : "+v"(ix[i]));
        }
        const int typ = w < 3 ? 0 : w < 6 ? 1 : 2;
        const int c1 = w < 3 ? w : w < 6 ? w - 3 : 2 * (w - 6), c2 = w == 6 ? 1 : c1;
        unsigned ka[3], ua[3];
        unsigned yl = 32 * l16 + 8 * g;
        {
            const unsigned kl = (l16 >> 2) * KC_CP - 16 * g + 8 * (lane & 3), ul = yl + 8 * g;
            const unsigned k0 = L_KC + 56 + c1 * KC_CH, k1 = typ == 2 ? L_KC + 56 + c2 * KC_CH - 4 * 512 : k0, k2 = typ == 1 ? k0 - 6 * 512 : k1;
            const unsigned u0 = L_UA + UA_PAD - 32 - 448 + c1 * UA_CH - typ * 1024, u2 = L_UA + UA_PAD - 32 - 448 + c2 * UA_CH, u1 = typ == 2 ? u2 : u0;
            ka[0] = kl + k0; ka[1] = kl + k1; ka[2] = kl + k2; ua[0] = ul + u0; ua[1] = ul + u1; ua[2] = ul + u2;
        }
        asm volatile("" : "+v"(ka[0]), "+v"(ka[1]), "+v"(ka[2]), "+v"(ua[0]), "+v"(ua[1]), "+v"(ua[2]), "+v"(yl));
        asm volatile("" : "+v"(ta), "+v"(te), "+v"(ta0), "+v"(ta1), "+v"(tb0), "+v"(tb1), "+v"(st1), "+v"(vx[0]), "+v"(vx[1]), "+v"(vw[0]), "+v"(vw[1]));
        if (w == 0) ldsw<unsigned>(L_CNT + (lane & 7) * 4, 0u);
        SB();
        load_table();
        const unsigned sx0 = P(P_SX0), sw0 = P(P_SW0);
        fa_off += P(P_FB); fb_off += P(P_WA3);
#pragma unroll
        for (int k = 0; k < 3; ++k) wa[k] = (wa[k] + (P(P_WA0) & 0x7fffffffu)) | (P(P_WA0) & 0x80000000u);
        STAMP(10);
        WAIT_BAR(0);
        STAMP(11);
        const unsigned so = (ch0 * 2) & 15;
        { const u2v tv = ldsr<u2v>(L_SCR + 32); tag = mk64(__builtin_amdgcn_readfirstlane(tv[0]), __builtin_amdgcn_readfirstlane(tv[1])) + 1ull; }
        const unsigned dsk01 = __builtin_amdgcn_readfirstlane(ldsr<u16>(L_SCR + 64 + so) | ((unsigned)ldsr<u16>(L_SCR + 64 + so + 2) << 16));
        const unsigned dsk2 = __builtin_amdgcn_readfirstlane(ldsr<u16>(L_SCR + 64 + so + 4));
        {
            const int hc = w < 6 ? w : w - 2, c = hc >> 1, mode = (hc & 1) * 64 + lane;
            const float ldt = bf2f(ldsr<u16>(L_SCR + so + 2 * c));
            const unsigned pb = L_PRM + c * 256 + mode * 2;
            const float pa = bf2f(ldsr<u16>(pb)), pi = bf2f(ldsr<u16>(pb + 1024));
            const f2v Bc = {bf2f(ldsr<u16>(pb + 2048)), bf2f(ldsr<u16>(pb + 3072))}, Cc = {bf2f(ldsr<u16>(pb + 4096)), bf2f(ldsr<u16>(pb + 5120))};
            const float dt = rbf(__builtin_amdgcn_exp2f(ldt * 1.4426950408889634f));
            const float are = dt * pa * 1.4426950408889634f, aim = dt * pi * 0.15915494309189535f;
            const unsigned tb = L_TBL + c * TBL_CH + mode * 4;
            auto colsE = [&](auto&& pc) {
                pc(0);
                const f2v z8 = cexp_p(are, aim, 8.f), z16 = cexp_p(are, aim, 16.f), z64 = cexp_p(are, aim, 64.f);
                pc(1);
                f2v e[8];
                e[0] = f2v{1.f, 0.f}; e[1] = z8; e[2] = z16; e[3] = cmul(z16, z8); e[4] = cmul(z16, z16);
                e[5] = cmul(e[3], z16); e[6] = cmul(e[4], z16); e[7] = cmul(e[4], e[3]);
#pragma unroll
                for (int i = 0; i < 4; ++i) {
                    f2v t[8];
                    if (i < 3) pc(2 + 2 * i);
#pragma unroll
                    for (int j = 0; j < 8; ++j) {
                        if (i < 3) t[j] = cmul_a(e[j], z64);
                        stw(tb, pk2(e[j][0], e[j][1]), (21 + 8 * i + j) * TBL_ROW);
                    }
                    if (i < 3) {
                        pc(3 + 2 * i);
#pragma unroll
                        for (int j = 0; j < 8; ++j) e[j] = cmul_b(e[j], z64, t[j]);
                    }
                }
            };
            auto rowsX = [&](auto&& pc) {
                pc(0);
                const f2v z1 = cexp_p(are, aim, 1.f), z2 = cexp_p(are, aim, 2.f), z4 = cexp_p(are, aim, 4.f);
                pc(1);
                const f2v z256 = cexp_p(are, aim, 256.f), z1024 = cexp_p(are, aim, 1024.f);
                pc(2);
                const f2v W = cmul(Cc, Bc * dt);
                auto putx = [&](int row, f2v v) { stw(tb, pk2(v[0], -v[1]), row * TBL_ROW); };
                auto mul4 = [&](f2v& o0, f2v a0, f2v b0, f2v& o1, f2v a1, f2v b1, f2v& o2, f2v a2, f2v b2, f2v& o3, f2v a3, f2v b3) {
                    const f2v t0 = cmul_a(a0, b0), t1 = cmul_a(a1, b1), t2 = cmul_a(a2, b2), t3 = cmul_a(a3, b3);
                    o0 = cmul_b(a0, b0, t0); o1 = cmul_b(a1, b1, t1); o2 = cmul_b(a2, b2, t2); o3 = cmul_b(a3, b3, t3);
                };
                f2v x1, x2, x3, x4, x5, x6, x7, x8, x9, x10, x11, x12, x13, x14, x15, x16, x17, x18, x19, x20;
                mul4(x1, W, z1, x2, W, z2, x8, W, z256, x16, W, z1024);
                putx(0, W);
                pc(3);
                mul4(x3, x1, z2, x9, x8, z2, x12, x8, z256, x17, x16, z256);
                putx(1, x1); putx(2, x2); putx(8, x8); putx(16, x16);
                pc(4);
                mul4(x4, W, z4, x5, x1, z4, x14, x12, z256, x18, x17, z256);
                putx(3, x3); putx(9, x9); putx(12, x12); putx(17, x17);
                pc(5);
                mul4(x6, x2, z4, x7, x3, z4, x10, x8, z4, x20, x16, z1024);
                putx(4, x4); putx(5, x5); putx(14, x14); putx(18, x18);
                pc(6);
                mul4(x11, x9, z4, x13, x12, z4, x15, x14, z4, x19, x18, z256);
                putx(6, x6); putx(7, x7); putx(10, x10); putx(20, x20);
                pc(7);
                putx(11, x11); putx(13, x13); putx(15, x15); putx(19, x19);
            };
            const unsigned sxa[2] = {P(P_SXK), P(P_SX1)}, swa[2] = {P(P_SWK), P(P_SW1)};
            auto stage2 = [&](int i) {
                if (i == 0) {
                    const u4v rw2 = mkrsrc(w2, 1728 * 1280);
                    dma16(rw2, wo[0], P(P_SO2), L_W2U + fw * 1024);
                    dma16(rw2, wo[1], P(P_SO2), fw == 0 ? L_W2U + 4096 : L_W2U + W2U_HI + (fw - 1) * 1024);
                    if (fw < 2) dma16(rw2, wo[2], P(P_SO2), L_W2U + W2U_HI + (fw + 3) * 1024);
                } else if (i < 5) {
                    const int p = i - 1;
                    const unsigned lb = lw + (p >> 1) * 1024 + (p & 1) * 16384;
                    if (p & 1) { dma16(rw, vw[p >> 1], swa[0], lb); dma16(rw, vw[p >> 1], swa[0] - 32 * 3456, lb - 8192); }
                    else { dma16(rx, vx[p >> 1], sxa[0], lb); dma16(rx, vx[p >> 1], sxa[0] - 64 * 1280, lb - 8192); }
                } else if (i < 7) {
                    dma16(rx, vx[i - 5], sxa[1], lw + SLOT + (i - 5) * 1024); dma16(rw, vw[i - 5], swa[1], lw + SLOT + 16384 + (i - 5) * 1024);
                }
            };
            auto nop = [&](int) {};
            if (w < 4) { colsE(nop); rowsX(nop); }
            else if (w < 6) colsE(stage2);
            else rowsX(stage2);
        }
        if (w >= 4) {
            WAIT_V(12);
            lds_count(L_CNT + 12);
            lds_wait(L_CNT + 12, 4u);
            bf8v f[3];
#pragma unroll
            for (int k = 0; k < 3; ++k) f[k] = cat(trr(wtl + k * 256), trr(wtl + k * 256 + W2U_HI));
            lds_count(L_CNT + 16);
#pragma unroll
            for (int k = 0; k < 3; ++k) __builtin_amdgcn_raw_buffer_store_b128(__builtin_bit_cast(u4v, f[k]), wsr, wa[k], 0, 16);
            lds_wait(L_CNT + 16, 4u);
            dma_at<SLOT + 8192>(lw - 16384, rx, vx[0], P(P_SX1) - 64 * 1280); dma_at<SLOT + 8192>(lw, rw, vw[0], P(P_SW1) - 32 * 3456);
            dma_at<SLOT + 9216>(lw - 16384, rx, vx[1], P(P_SX1) - 64 * 1280); dma_at<SLOT + 9216>(lw, rw, vw[1], P(P_SW1) - 32 * 3456);
        }
        STAMP(13);
        BAR_L();
        if (w < 2 * nch) {
            u4v fa[8], fb[8], fe[8];
#pragma unroll
            for (int s = 0; s < 8; ++s) { fe[s] = ldsr<u4v>(te + s * 64); fa[s] = ldsr<u4v>(ta + s * 64); fb[s] = ldsr<u4v>(ta + 16 * TBL_ROW + s * 64); }
            SB();
            f4v k0 = {}, k1 = {}, k2 = {}, k3 = {};
#pragma unroll
            for (int s = 0; s < 8; s += 2) {
                k0 = __builtin_amdgcn_mfma_f32_16x16x32_bf16(asbf(fa[s]), asbf(fe[s]), k0, 0, 0, 0);
                k1 = __builtin_amdgcn_mfma_f32_16x16x32_bf16(asbf(fb[s]), asbf(fe[s]), k1, 0, 0, 0);
                k2 = __builtin_amdgcn_mfma_f32_16x16x32_bf16(asbf(fa[s + 1]), asbf(fe[s + 1]), k2, 0, 0, 0);
                k3 = __builtin_amdgcn_mfma_f32_16x16x32_bf16(asbf(fb[s + 1]), asbf(fe[s + 1]), k3, 0, 0, 0);
            }
            k0 += k2; k1 += k3;
            ldsw<unsigned>(ta0, pk2(k0[0], k0[1]));
            ldsw<unsigned>(ta1, pk2(k0[2], k0[3]));
            const unsigned q01 = pk2(k1[0], k1[1]), q23 = pk2(k1[2], k1[3]);
            ldsw<u16>(tb1, g == 1 && col == 1 ? (u16)0 : (u16)q01);
            ldsw<u16>(tb0 + st1, (u16)(q01 >> 16));
            ldsw<u16>(tb0 + 2 * st1, (u16)q23);
            ldsw<u16>(tb0 + 3 * st1, (u16)(q23 >> 16));
        }
        WAIT_BAR(11);
        STAMP(14);
        auto conv = [&](auto nb0c, auto pubc) {
            constexpr int NB0 = decltype(nb0c)::value, Q0 = decltype(pubc)::value;
            bf8v uf[8], kf[8];
            auto rdk = [&](int sl, int rho) { const unsigned ad = ka[sl < 4 ? 0 : sl < 6 ? 1 : 2] + 512u * sl + 64u * rho; kf[sl] = cat(trr(ad + 8u), trr(ad)); };
            auto rdu = [&](int j, int rho) { uf[j] = asbf(ldsr<u4v>(ua[j >= 4 ? 0 : j >= 2 ? 1 : 2] + 448u - 64u * rho + 512u * j)); };
            constexpr int ord[8] = {0, 4, 6, 1, 5, 7, 2, 3}, pos[8] = {0, 3, 6, 7, 1, 4, 2, 5};
#pragma unroll
            for (int p = 0; p < 8; ++p) { rdk(ord[p], 0); rdu(7 - ord[p], 0); }
            f4v A[3] = {}, B[5] = {};
            const unsigned fo = WS_FH + (rb * 16 + cb) * FL;
#pragma unroll
            for (int rho = 0; rho < 8; ++rho) {
#pragma unroll
                for (int p = 0; p < 8; ++p) {
                    const int sl = ord[p], ia = sl < 4 ? 0 : sl < 6 ? 1 : 2, ib = sl < 3 ? 0 : sl - 2;
                    SB();
                    A[ia] = __builtin_amdgcn_mfma_f32_16x16x32_bf16(kf[sl], uf[7 - sl], A[ia], 0, 0, 0);
                    if (sl < 7) B[ib] = __builtin_amdgcn_mfma_f32_16x16x32_bf16(kf[sl], uf[6 - sl], B[ib], 0, 0, 0);
                    SB();
                    if (rho < 7) {
                        rdk(sl, rho + 1);
                        if (sl == 0 || pos[sl - 1] < p) rdu(7 - sl, rho + 1);
                        if (sl < 7 && pos[sl + 1] < p) rdu(6 - sl, rho + 1);
                    }
                    SB();
                    const int q = rho * 8 + p - Q0;
                    if (q == 0) { if (NB0 == 0) {
                        WAIT_V(0); arrive_publish(L_CNT, ws, fo, tag, 3u);
                        if (HPF) { for (int i = 0; i < 8; ++i) dma16_sc1(mkrsrc(ws, WS_BYTES), lo16, P(P_HO) + i * 1024, w * 1024); }
                    } else lds_wait(L_CNT, 4u); }
                    constexpr int NSL = (NB0 == 0 ? 62 : HQ1) - Q0;
                    if (q >= 0 && q < NSL && (q + 1) * (NB2 - NB0) / NSL > q * (NB2 - NB0) / NSL) loadb(NB0 + q * (NB2 - NB0) / NSL);
                }
            }
            SB();
            const int ta_ = 7 - 2 * typ, tb_ = max(2 * typ - 1, 1);
            const unsigned ul1 = yl + (L_UA + UA_PAD - 512 + c1 * UA_CH + 512 * ta_), ul2 = yl + (L_UA + UA_PAD - 512 + c2 * UA_CH + 512 * tb_);
            const u2v uv[4] = {ldsr<u2v>(ul1 + 512), ldsr<u2v>(ul1), ldsr<u2v>(ul2 + 512), ldsr<u2v>(ul2)};
            if (typ == 0) { A[0] += A[1] + A[2]; B[0] += (B[1] + B[2]) + (B[3] + B[4]); }
            else if (typ == 1) { A[0] += A[1]; B[0] += B[1] + B[2]; }
            else { A[2] += A[1]; B[4] += B[2] + B[3]; }
            const unsigned y1 = P(P_YT1), y2 = P(P_YT2);
            const float d1 = __uint_as_float(((c1 == 2 ? dsk2 : dsk01) >> (c1 == 1 ? 16 : 0)) << 16), d2 = __uint_as_float(((c2 == 2 ? dsk2 : dsk01) >> (c2 == 1 ? 16 : 0)) << 16);
#pragma unroll
            for (int i = 0; i < 4; ++i) {
                const f4v y = i == 0 ? A[0] : i == 1 ? B[0] : i == 2 ? A[2] : B[4];
                const float dk = i < 2 ? d1 : d2;
                const u2v o = {pk2(y[0] + dk * bflo(uv[i][0]), y[1] + dk * bfhi(uv[i][0])), pk2(y[2] + dk * bflo(uv[i][1]), y[3] + dk * bfhi(uv[i][1]))};
                __builtin_amdgcn_raw_buffer_store_b64(o, wsr, yl + ((i & 1) ? 0 : 512), i < 2 ? y1 : y2, 16);
            }
        };
        if (w < 4) {
            f4v acc1[4][4];
#pragma unroll
            for (int i = 0; i < 16; ++i) acc1[i >> 2][i & 3] = f4v{};
            bf8v fa[2][2][4], fb[2][2][4];
            auto rd = [&](int buf, int i) {
                const int ks = i >> 2, h = i & 3;
                fa[buf][ks][h] = cat(trr(ix[2 + h] + ks * 8192), trr(ix[2 + h] + ks * 8192 + 1024));
                fb[buf][ks][h] = asbf(ldsr<u4v>(ix[ks] + h * 2048));
            };
            unsigned sb = 0;
            auto tile = [&](int cur, bool more) {
                if (more) {
                    const unsigned d = sb == 2 * SLOT ? (unsigned)(-2 * (int)SLOT) : SLOT;
                    sb += d;
#pragma unroll
                    for (int i = 0; i < 6; ++i) { ix[i] += d; asm volatile("" : "+v"(ix[i])); }
                    BAR_L();
                }
#pragma unroll
                for (int i = 0; i < 8; ++i) {
                    SB();
                    if (more) rd(cur ^ 1, i);
                    SB();
#pragma unroll
                    for (int j = 0; j < 4; ++j) acc1[i & 3][j] = __builtin_amdgcn_mfma_f32_16x16x32_bf16(fa[cur][i >> 2][i & 3], fb[cur][i >> 2][j], acc1[i & 3][j], 0, 0, 0);
                }
                SB();
            };
#pragma unroll
            for (int i = 0; i < 8; ++i) rd(0, i);
#pragma nounroll
            for (int t = 0; t < 4; ++t) { tile(0, true); tile(1, true); }
            STAMP(3);
            tile(0, true);
            tile(1, false);
            BAR_L();
            STAMP(2);
            const unsigned ho = lo16 + P(P_HO), ca = L_CB + (64 * (w & 1) + 4 * g) * 4, sa = L_ST + (64 * (w >> 1) + l16) * 8;
            f2v sv[4];
#pragma unroll
            for (int ti = 0; ti < 4; ++ti) sv[ti] = ldsr<f2v>(sa + ti * 128);
#pragma unroll
            for (int sp = 0; sp < 2; ++sp) {
                f4v cs[2], bb[2];
#pragma unroll
                for (int u = 0; u < 2; ++u) { cs[u] = ldsr<f4v>(ca + (32 * sp + 16 * u) * 4); bb[u] = ldsr<f4v>(ca + 512 + (32 * sp + 16 * u) * 4); }
#pragma unroll
                for (int ti = 0; ti < 4; ++ti) {
                    const f2v rs2 = {sv[ti][0], sv[ti][0]}, nm2 = {-sv[ti][1], -sv[ti][1]};
                    u4v hp;
#pragma unroll
                    for (int i = 0; i < 4; ++i) {
                        const int u = i >> 1, e = 2 * (i & 1);
                        const f2v v = __builtin_elementwise_fma(rs2, f2v{acc1[2 * sp + u][ti][e], acc1[2 * sp + u][ti][e + 1]}, __builtin_elementwise_fma(nm2, f2v{cs[u][e], cs[u][e + 1]}, f2v{bb[u][e], bb[u][e + 1]}));
                        const f2v z = v * __builtin_elementwise_fma(v * v, f2v{-0.10294324f, -0.10294324f}, f2v{-2.30220820f, -2.30220820f});
                        const f2v d = f2v{__builtin_amdgcn_exp2f(z[0]), __builtin_amdgcn_exp2f(z[1])} + f2v{1.f, 1.f};
                        const f2v gl = v * f2v{__builtin_amdgcn_rcpf(d[0]), __builtin_amdgcn_rcpf(d[1])};
                        hp[i] = pk2(gl[0], gl[1]);
                    }
                    __builtin_amdgcn_raw_buffer_store_b128(hp, wsr, ho + 4096 * sp + 1024 * ti, 0, 16);
                    SB();
                }
            }
            STAMP(4);
            asm volatile("s_barrier" ::: "memory");
            conv(IC<0>{}, IC<GQ0>{});
        } else {
            unsigned sxk = P(P_SX1), swk = P(P_SW1);
            const unsigned sx_end = sx0 + 1280;
            auto stage8 = [&](auto sc, auto&& side) {
                constexpr int S = decltype(sc)::value;
                sxk += 128; swk += 64 * 3456;
                const bool wrap = sxk == sx_end;
                sxk = wrap ? sx0 : sxk; swk = wrap ? sw0 : swk;
                const unsigned sxl = sxk - 64 * 1280, swl = swk - 32 * 3456;
                dma_at<S * SLOT>(lw, rx, vx[0], sxk); side(0);
                dma_at<S * SLOT + 16384>(lw, rw, vw[0], swk); side(1);
                dma_at<S * SLOT + 1024>(lw, rx, vx[1], sxk); side(2);
                dma_at<S * SLOT + 17408>(lw, rw, vw[1], swk); side(3);
                dma_at<S * SLOT + 8192>(lw - 16384, rx, vx[0], sxl); side(4);
                dma_at<S * SLOT + 8192>(lw, rw, vw[0], swl); side(5);
                dma_at<S * SLOT + 9216>(lw - 16384, rx, vx[1], sxl); side(6);
                dma_at<S * SLOT + 9216>(lw, rw, vw[1], swl); side(7);
            };
            auto gap = [&](int) {};
            unsigned kv[11], pe[5], po[5];
            const unsigned kd = L_KC + 32 + (tid & 255) * 16;
            auto ex_read = [&](int c, int i) {
                if (i < 6) { kv[2 * i] = ldsr<u16>(ix[2 * i] + c * TT_CH); if (i < 5) kv[2 * i + 1] = ldsr<u16>(ix[2 * i + 1] + c * TT_CH); asm volatile("" ::: "memory"); }
            };
            auto ex_write = [&](int c, int i) {
                const unsigned d = kd + c * KC_CH;
                if (i == 0) { for (int j = 0; j < 5; ++j) pe[j] = __builtin_amdgcn_perm(kv[2 * j + 1], kv[2 * j], 0x05040100u); }
                if (i == 1) ldsw<u4v>(d + 3 * KC_CP, u4v{pe[0], pe[1], pe[2], pe[3]});
                if (i == 2) { for (int j = 0; j < 5; ++j) po[j] = __builtin_amdgcn_perm(kv[2 * j + 2], kv[2 * j + 1], 0x05040100u); }
                if (i == 3) ldsw<u4v>(d + 2 * KC_CP, u4v{po[0], po[1], po[2], po[3]});
                if (i == 5) ldsw<u4v>(d + KC_CP, u4v{pe[1], pe[2], pe[3], pe[4]});
                if (i == 7) ldsw<u4v>(d, u4v{po[1], po[2], po[3], po[4]});
                asm volatile("" ::: "memory");
            };
            u64 fpa, fpb;
            const u4v wsq = mkrsrc(ws, WS_BYTES);
            auto poll = [&](u64& f, unsigned o) { asm volatile("global_load_dwordx2 %0, %1, %2 sc1" : "=v"(f) : "v"(o), "s"(ws) : "memory"); };
            stage8(IC<2>{}, gap);
            if (w == 5) ldsw<u4v>(L_KC + min(lane >> 1, 11) * KC_CP + (lane & 1) * 16, u4v{0u, 0u, 0u, 0u});
            WAIT_V(8);
            arrive_publish(L_CNT + 8, ws, WS_FW + ((bid & 1) * 128 + (bid >> 1)) * FL, tag, 3u);
            STAMP(1);
            BAR_L();
            STAMP(15);
            stage8(IC<0>{}, [&](int i) { ex_read(0, i); });
            WAIT_BAR(8);
            stage8(IC<1>{}, [&](int i) { ex_write(0, i); });
            WAIT_BAR(8);
            stage8(IC<2>{}, [&](int i) { ex_read(1, i); });
            WAIT_BAR(8);
            STAMP(12);
            stage8(IC<0>{}, [&](int i) { ex_write(1, i); });
            WAIT_BAR(8);
            stage8(IC<1>{}, [&](int i) { ex_read(2, i); });
            poll(fpa, fa_off);
            WAIT_BAR(9);
            stage8(IC<2>{}, [&](int i) { ex_write(2, i); });
            poll(fpb, fb_off);
            asm volatile("s_waitcnt vmcnt(9)" : "+v"(fpa)::"memory");
            if (!__all(fpa == tag)) wait_flags(ws, fa_off, tag);
            BAR_L();
            stage8(IC<0>{}, gap);
            dma16_sc1(wsq, lo16, P(P_WA1), L_UA + UA_PAD + fw * 1024);
            dma16_sc1(wsq, lo16, P(P_WA1) + 4096, L_UA + UA_CH + UA_PAD + fw * 1024);
            dma16_sc1(wsq, lo16, P(P_WA1) + (nch - 1) * 4096, L_UA + 2 * UA_CH + UA_PAD + fw * 1024);
            if (w < 7) ldsw<u4v>(L_UA + fw * UA_CH + min(lane, 29) * 16, u4v{0u, 0u, 0u, 0u});
            asm volatile("s_waitcnt vmcnt(11)" : "+v"(fpb)::"memory");
            if (!__all(fpb == tag)) wait_flags(ws, fb_off, tag);
            BAR_L();
            if (w < 6) dma16_sc1(wsq, w == 4 ? (lane >> 5) * (WS_B1 - WS_CS) + (lane & 31) * 16 : lo16, P(P_WA2), w == 4 ? L_CB : L_ST);
            else dma16_lanes16(mkrsrc(b2, 1280), lo16, cb2 * 160, L_XS + 10240);
            dma16(mkrsrc(&seltab, 6144), lo16, fw * 1024, L_SEL + fw * 1024); dma16(mkrsrc(&seltab, 6144), lo16, 4096 + (w & 1) * 1024, L_SEL + 4096 + (w & 1) * 1024);
            dma16(rx, xs0, P(P_XS), L_XS + w * 1280); dma16_lanes16(rx, xs1, P(P_XS), L_XS + w * 1280 + 1024);
            dma16(rx, xs0, P(P_XS) - 32 * 1280, L_XS + fw * 1280); dma16_lanes16(rx, xs1, P(P_XS) - 32 * 1280, L_XS + fw * 1280 + 1024);
#pragma unroll
            for (int n = 0; n < 4; ++n) loadb(n);
            WAIT_BAR(14);
#pragma unroll
            for (int n = 4; n < HW; ++n) loadb(n);
            WAIT_BARN(HW);
            asm volatile("s_barrier" ::: "memory");
            conv(IC<HW>{}, IC<HQ0>{});
            __builtin_amdgcn_s_waitcnt(0x0F70);
        }
        STAMP(5);
    } else {
        const int a = (r - 28) * 8 + xcd;
        const u4v rx = mkrsrc(x, 2048 * 1280);
        {
            unsigned vo[5];
#pragma unroll
            for (int i = 0; i < 5; ++i) {
                const int n = i * 64 + lane, rl = n / 80, cp = n - rl * 80;
                vo[i] = (a * 64 + 8 * w + rl) * 1280 + ((cp ^ (2 * w) ^ ((rl >> 1) << 3)) << 4);
                dma16(rx, vo[i], 0, L_AX + (w * 10 + i) * 1024);
            }
#pragma unroll
            for (int i = 5; i < 10; ++i) dma16(rx, vo[i - 5], 4 * 1280, L_AX + (w * 10 + i) * 1024);
        }
        const u64 e0 = ld_flag(ws, WS_E);
        load_table();
        if (w == 0) ldsw<unsigned>(L_ACNT + (lane & 7) * 4, 0u);
        STAMP(10);
        WAIT_V(1);
        STAMP(11);
        {
            const int row = 8 * w + (lane >> 3), sw = (2 * w) ^ (((row >> 1) & 1) << 3);
            u4v v[10];
#pragma unroll
            for (int i = 0; i < 10; ++i) v[i] = ldsr<u4v>(L_AX + row * 1280 + (((lane & 7) + 8 * i) ^ sw) * 16);
            SB();
            float s1[2] = {0.f, 0.f}, s2[2] = {0.f, 0.f};
#pragma unroll
            for (int i = 0; i < 10; ++i)
#pragma unroll
                for (int j = 0; j < 4; ++j) { s1[j & 1] = dot2(v[i][j], 0x3f803f80u, s1[j & 1]); s2[j & 1] = dot2(v[i][j], v[i][j], s2[j & 1]); }
            const f2v sv = ln_stats(sum8(s1[0] + s1[1]), sum8(s2[0] + s2[1]));
            ldsw<f2v>(L_AST + row * 8, sv);
            __builtin_amdgcn_raw_buffer_store_b64(__builtin_bit_cast(u2v, sv), wsr, (WS_STATS + (a * 64 + row) * 8) | oob_if(lane & 7), 0, 16);
        }
        u4v cv[10];
        {
            const auto rw1 = __builtin_amdgcn_make_buffer_rsrc((void*)(u64)w1, 0, 640 * 3456, 0x00020000);
#pragma unroll
            for (int i = 0; i < 10; ++i) cv[i] = __builtin_amdgcn_raw_buffer_load_b128(rw1, ((lane >> 3) * 3456 + (lane & 7) * 16) | oob_if(a >= 27), (80 * w + 8 * i) * 3456 + a * 128, 0);
        }
        WAIT_BAR(11);
        STAMP(14);
        {
            const int o = lane & 7, cq = lane >> 3;
            f2v rs2[4], nm2[4];
#pragma unroll
            for (int i = 0; i < 4; ++i) { const f4v q = ldsr<f4v>(L_AST + o * 64 + i * 16); rs2[i] = f2v{q[0], q[2]}; nm2[i] = f2v{-q[1], -q[3]}; }
            u2v v[3][8];
#pragma unroll
            for (int k = 0; k < 3; ++k)
#pragma unroll
                for (int t = 0; t < 8; ++t) v[k][t] = ldsr<u2v>(L_AX + (8 * o + t) * 1280 + ((4 * min(w + 8 * k, 19) + (cq >> 1)) ^ ((2 * o) ^ (((t >> 1) & 1) << 3))) * 16 + (cq & 1) * 8);
            SB();
#pragma unroll
            for (int k = 0; k < 3; ++k) {
                if (k < 2 || w < 4) {
#pragma unroll
                    for (int e = 0; e < 4; ++e) {
                        u4v ov;
#pragma unroll
                        for (int p = 0; p < 4; ++p) {
                            const unsigned va = v[k][2 * p][e >> 1], vb = v[k][2 * p + 1][e >> 1];
                            const f2v n2 = __builtin_elementwise_fma((e & 1) ? f2v{bfhi(va), bfhi(vb)} : f2v{bflo(va), bflo(vb)}, rs2[p], nm2[p]);
                            ov[p] = pk2(n2[0], n2[1]);
                        }
                        __builtin_amdgcn_raw_buffer_store_b128(ov, wsr, WS_XT + (32 * (w + 8 * k) + 4 * cq + e) * 4096 + a * 128 + o * 16, 0, 16);
                        SB();
                    }
                }
                if (k == 0) {
                    WAIT_V(4);
                    tag = mk64(__builtin_amdgcn_readfirstlane((unsigned)e0), __builtin_amdgcn_readfirstlane((unsigned)(e0 >> 32))) + 1ull;
                    arrive_publish(L_ACNT + 8, ws, WS_FS + a * FL, tag);
                }
            }
        }
        STAMP(15);
        {
            unsigned selo = 0x00003f80u, sehi = 0x3f800000u;
            asm volatile("" : "+v"(selo), "+v"(sehi));
            float sm[8] = {0.f, 0.f, 0.f, 0.f, 0.f, 0.f, 0.f, 0.f};
#pragma unroll
            for (int i = 0; i < 10; ++i)
#pragma unroll
                for (int j = 0; j < 4; ++j) { sm[2 * j] = dot2(cv[i][j], selo, sm[2 * j]); sm[2 * j + 1] = dot2(cv[i][j], sehi, sm[2 * j + 1]); }
#pragma unroll
            for (int j = 0; j < 8; ++j) sm[j] = dpp_add<0x128>(sm[j]);
            ldsw<f4v>(L_ACS + (4 * w + g) * 256 + (lane & 7) * 32 + (lane & 8) * 2, (lane & 8) ? f4v{sm[4], sm[5], sm[6], sm[7]} : f4v{sm[0], sm[1], sm[2], sm[3]});
        }
        BAR_L();
        if (w != 7 || a >= 27) {
            WAIT_V(0);
            arrive_publish(L_ACNT + 12, ws, WS_FA + a * FL, tag);
        } else {
            const float bf = bf2f(b1[64 * a + lane]);
            float p[32], sum = 0.f;
#pragma unroll
            for (int i = 0; i < 32; ++i) p[i] = ldsr<float>(L_ACS + i * 256 + lane * 4);
#pragma unroll
            for (int i = 0; i < 32; i += 4) sum += (p[i] + p[i + 1]) + (p[i + 2] + p[i + 3]);
            __builtin_amdgcn_raw_buffer_store_b32(__builtin_bit_cast(unsigned, sum), wsr, WS_CS + (64 * a + lane) * 4, 0, 16);
            __builtin_amdgcn_raw_buffer_store_b32(__builtin_bit_cast(unsigned, bf), wsr, WS_B1 + (64 * a + lane) * 4, 0, 16);
            WAIT_V(0);
            raise_flag(ws, WS_FC + a * FL, tag);
            arrive_publish(L_ACNT + 12, ws, WS_FA + a * FL, tag);
        }
        STAMP(2);
        if (w < 2) wait_flags(ws, P(P_WA3) + min(lane, 53) * FL, tag);
        if (a == 31 && w == 2) {
#pragma unroll
            for (int i = 0; i < 4; ++i) wait_flags(ws, (i & 1) && lane >= 48 ? WS_FS + ((i >> 1) * 16 + lane - 48) * FL : WS_FW + (i * 64 + lane) * FL, tag);
            raise_flag(ws, WS_E, tag);
        }
        BAR_L();
        STAMP(3);
        stage_x_init();
        stage_x();
        if (w < 6) dma16(mkrsrc(&seltab, 6144), lo16, w * 1024, L_SEL + w * 1024);
#pragma unroll
        for (int n = 0; n < NB2; ++n) loadb(n);
        __builtin_amdgcn_s_waitcnt(0x0F70);
    }

    if (w == HPW) wait_flags(ws, WS_FH + ((rb2 >> 1) * 16 + min(lane, 13)) * FL, tag);
    WAIT_BAR(0);
    STAMP(6);
    u4v ha[7][4];
    auto loada = [&](int i, int t) { ha[i][t] = __builtin_amdgcn_raw_buffer_load_b128(wsr, lo16 + t * 1024, P(P_SA + i), 16); asm volatile("" ::: "memory"); };
#pragma unroll
    for (int i = 0; i < 8; ++i) loada(i >> 2, i & 3);
    if (is_gemm && w == 7) raise_flag(ws, WS_FY + bid * FL, tag);
    u64 ypoll = tag;
    u4v y0, y1;
    const int tq = w & 3, hi4 = w >> 2, cq = 3 * hi4;
    unsigned yf_off, yv0, yv1, da, ys0, ys1, ra, ya, xa, ba, oa, or0, or1, og0, og1;
    auto prep = [&](int k) {
        int lz = lane;
        asm volatile("" : "+v"(lz));
        const int n = lz & 15, gz = lz >> 4;
        if (k == 0) {
            yf_off = (P(P_FY) & 0xffffffu) + min((unsigned)lz, P(P_FY) >> 24) * FL;
            asm volatile("" : "+v"(yf_off));
        }
        if (k == 1) {
            yv0 = (lz >> 3) * 4096 + (lz & 7) * 16; yv1 = yv0 | oob_if(lz >= 16);
            asm volatile("" : "+v"(yv0), "+v"(yv1));
        }
        if (k == 2) {
            da = gz * 1024 + n * 16 + (L_RED + (w >> 1) * 256 + (w & 1) * 8);
            ys0 = (lz >> 3) * YS_ROW + (lz & 7) * 16 + (L_YS + 10 * w * YS_ROW); ys1 = lz < 16 ? ys0 + 8 * YS_ROW : L_YS + 80 * YS_ROW;
            ra = lz * 16 + (L_RED + (5 * tq + cq) * 4096);
            asm volatile("" : "+v"(da), "+v"(ys0), "+v"(ys1), "+v"(ra));
        }
        if (k == 3) {
            ya = n * YS_ROW + gz * 16 + (L_YS + 16 * cq * YS_ROW + (tq >> 1) * 64);
            xa = n * 160 + gz * 16 + (L_XS + 16 * tq * 160);
            ba = gz * 8 + (L_XS + 10240 + cq * 32); oa = n * OUT_ROW + gz * 8 + (L_OUT + 16 * tq * OUT_ROW + cq * 32);
            asm volatile("" : "+v"(ya), "+v"(xa), "+v"(ba), "+v"(oa));
        }
        if (k == 4) {
            const int q0 = (lz * 205) >> 11, q1 = ((64 + n) * 205) >> 11;
            or0 = lz * 16 + q0 * (OUT_ROW - 160) + (L_OUT + 8 * w * OUT_ROW); or1 = n * 16 + q1 * (OUT_ROW - 160) + (L_OUT + 8 * w * OUT_ROW + 1024);
            og0 = lz * 16 + q0 * 1120 + P(P_XS); og1 = (n * 16 + q1 * 1120 + (P(P_XS) + 1024)) | oob_if(lz >= 16);
            asm volatile("" : "+v"(or0), "+v"(or1), "+v"(og0), "+v"(og1));
        }
    };
    prep(0); prep(1); prep(2); prep(3); prep(4);
    f4v acc2[4][5];
#pragma unroll
    for (int i = 0; i < 20; ++i) acc2[i / 5][i % 5] = f4v{};
#pragma unroll
    for (int i = 0; i < 5; ++i) {
#pragma unroll
        for (int t = 0; t < 4; ++t) {
            SB();
#pragma unroll
            for (int c = 0; c < 5; ++c) acc2[t][c] = __builtin_amdgcn_mfma_f32_16x16x32_bf16(asbf(ha[i][t]), asbf(bw[5 * i + c]), acc2[t][c], 0, 0, 0);
            SB();
            if (i > 0) loada(i + 2, t);
            else if (t == 3) { loada(2, 0); loada(2, 1); loada(2, 2); loada(2, 3); }
            if (i >= 1 && NB2 + 4 * (i - 1) + t < 35) loadb(NB2 + 4 * (i - 1) + t);
            if (i == YPI && t == 2) ypoll = ld_flag(ws, yf_off);
            if (i == YLI && t == 3) {
                if (!__all(ypoll == tag)) wait_flags(ws, yf_off, tag);
                y0 = __builtin_amdgcn_raw_buffer_load_b128(wsr, yv0, P(P_YL), 16);
                y1 = __builtin_amdgcn_raw_buffer_load_b128(wsr, yv1, P(P_YL) + 8 * 4096, 16);
                asm volatile("" ::: "memory");
            }
        }
    }
    STAMP(7);
    auto dump = [&](int t) {
        const unsigned db = t < 3 ? da : da + 15 * 4096;
#pragma unroll
        for (int c = 0; c < 5; ++c) ldsw<u2v>(db + ((t < 3 ? 5 * t : 0) + c) * 4096, u2v{pk2(acc2[t][c][0], acc2[t][c][1]), pk2(acc2[t][c][2], acc2[t][c][3])});
    };
#pragma unroll
    for (int t = 0; t < 4; ++t) {
        SB();
#pragma unroll
        for (int c = 0; c < 5; ++c) acc2[t][c] = __builtin_amdgcn_mfma_f32_16x16x32_bf16(asbf(ha[5][t]), asbf(bw[25 + c]), acc2[t][c], 0, 0, 0);
        SB();
        if (t > 0) dump(t - 1);
        SB();
#pragma unroll
        for (int c = 0; c < 5; ++c) acc2[t][c] = __builtin_amdgcn_mfma_f32_16x16x32_bf16(asbf(ha[6][t]), asbf(bw[30 + c]), acc2[t][c], 0, 0, 0);
    }
    SB();
    ldsw<u4v>(ys0, y0);
    ldsw<u4v>(ys1, y1);
    const unsigned d3 = hi4 ? 0u : 2u;
    const u4v sel[4] = {ldsr<u4v>(L_SEL + lo16), ldsr<u4v>(L_SEL + 1024 + lo16), ldsr<u4v>(L_SEL + 2048 + lo16), ldsr<u4v>(L_SEL + 3072 + lo16)};
    const u4v oy = ldsr<u4v>(lo16 + (L_SEL + 4096 + (tq & 1) * 1024)), oxa = ldsr<u4v>(lo16 + (L_SEL + 4096 + hi4 * 1024)), oxb = ldsr<u4v>(lo16 + (L_SEL + 5120 - hi4 * 1024));
    f4v d[3];
    {
        const u4v xf0 = ldsr<u4v>(xa + hi4 * 64), xf1 = ldsr<u4v>(xa + hi4 * 128), xf2 = ldsr<u4v>(xa + 64);
        const u2v bv[3] = {ldsr<u2v>(ba), ldsr<u2v>(ba + 32), ldsr<u2v>(ba + d3 * 32)};
        dump(3);
#pragma unroll
        for (int k = 0; k < 3; ++k) d[k] = f4v{bflo(bv[k][0]), bfhi(bv[k][0]), bflo(bv[k][1]), bfhi(bv[k][1])};
        d[0] = __builtin_amdgcn_mfma_f32_16x16x32_bf16(asbf(oxa), asbf(xf0), d[0], 0, 0, 0);
        d[1] = __builtin_amdgcn_mfma_f32_16x16x32_bf16(asbf(oxb), asbf(xf1), d[1], 0, 0, 0);
        d[2] = __builtin_amdgcn_mfma_f32_16x16x32_bf16(asbf(oxa), asbf(xf2), d[2], 0, 0, 0);
    }
    unsigned ra3 = ra + d3 * 4096, ya3 = ya + d3 * (16 * YS_ROW), oa3 = oa + d3 * 32;
    asm volatile("" : "+v"(ra3), "+v"(ya3), "+v"(oa3));
    SB();
    BAR_L();
    STAMP(8);
    {
        u4v p[3][4];
#pragma unroll
        for (int q = 0; q < 4; ++q) { p[0][q] = ldsr<u4v>(ra + q * 1024); p[1][q] = ldsr<u4v>(ra + 4096 + q * 1024); p[2][q] = ldsr<u4v>(ra3 + q * 1024); }
        const u4v yf[3] = {ldsr<u4v>(ya), ldsr<u4v>(ya + 16 * YS_ROW), ldsr<u4v>(ya3)};
        SB();
#pragma unroll
        for (int q = 0; q < 4; ++q)
#pragma unroll
            for (int k = 0; k < 3; ++k) d[k] = __builtin_amdgcn_mfma_f32_16x16x32_bf16(asbf(p[k][q]), asbf(sel[q]), d[k], 0, 0, 0);
#pragma unroll
        for (int k = 0; k < 3; ++k) d[k] = __builtin_amdgcn_mfma_f32_16x16x32_bf16(asbf(yf[k]), asbf(oy), d[k], 0, 0, 0);
#pragma unroll
        for (int k = 0; k < 3; ++k) ldsw<u2v>(k == 0 ? oa : k == 1 ? oa + 32 : oa3, u2v{pk2(d[k][0], d[k][1]), pk2(d[k][2], d[k][3])});
        BAR_L();
        const auto ro = __builtin_amdgcn_make_buffer_rsrc((void*)(u64)out, 0, 2048 * 1280, 0x00020000);
        const u4v v0 = ldsr<u4v>(or0), v1 = ldsr<u4v>(or1);
        __builtin_amdgcn_raw_buffer_store_b128(v0, ro, og0, 0, 0);
        __builtin_amdgcn_raw_buffer_store_b128(v1, ro, og1, 0, 0);
    }
}

extern "C" void kernel_launch(void* const* d_in, const int* in_sizes, int n_in, void* d_out, int out_size, void* d_ws, size_t ws_size, hipStream_t stream) {
    (void)in_sizes; (void)n_in; (void)out_size; (void)ws_size;
    (void)hipFuncSetAttribute((const void*)s4ffn_fused, hipFuncAttributeMaxDynamicSharedMemorySize, LDS_BYTES);
    s4ffn_fused<<<256, 512, LDS_BYTES, stream>>>((const u16*)d_in[0], (const u16*)d_in[1], (const u16*)d_in[2], (const u16*)d_in[3], (const u16*)d_in[4],
                                                 (const u16*)d_in[5], (const u16*)d_in[6], (const u16*)d_in[7], (const u16*)d_in[8], (const u16*)d_in[10],
                                                 (const u16*)d_in[11], (const u16*)d_in[12], (const u16*)d_in[13], (u16*)d_out, (char*)d_ws);
}
```
